```python
import torch

def _softmax(x, axis=-1):
    return torch.softmax(x, dim=axis)
def _gelu(x):
    return torch.nn.functional.gelu(x, approximate="tanh")
def _silu(x):
    return torch.nn.functional.silu(x)
def _sigmoid(x):
    return torch.sigmoid(x)
def _layernorm(x, axis=-1, eps=1e-5):
    m = x.mean(dim=axis, keepdim=True)
    v = x.var(dim=axis, keepdim=True, unbiased=False)
    return (x - m) / torch.sqrt(v + eps)
def _scatter_add(tgt, idx, src, axis=0):
    return tgt.clone().index_add_(axis, idx, src)
def _segment_sum(src, seg, n):
    out = torch.zeros((n,) + src.shape[1:], dtype=src.dtype)
    return out.index_add_(0, seg, src)
def _pad(x, pw):
    flat = tuple(v for pair in reversed(pw) for v in pair)
    return torch.nn.functional.pad(x, flat)

kernel_name = 'gated_mix__linear_attn_block__ssm_conv_s4__prompt'

def setup_inputs():

    _w = (lambda a: (lambda t: t.to(torch.bfloat16) if t.is_floating_point() else t)(torch.as_tensor(a)))
    def _A_setup():
        B, S, D = 1, 2048, 576
        return {
            "x": _w((lambda *s: torch.randn(*s, dtype=torch.bfloat16) * 0.05)(B, S, D)),
            "wqkv": _w((lambda *s: torch.randn(*s, dtype=torch.bfloat16) * 0.8)(D, 3 * D)),
            "wo": _w((lambda *s: torch.randn(*s, dtype=torch.bfloat16) * 0.05)(D, D)),
        }
    _A_in = _A_setup()

    def _B_setup():
        B, L, D, N = 1, 2048, 576, 128
        A_re = -(lambda _x: torch.exp(torch.as_tensor(_x)))((lambda *s: torch.randn(*s, dtype=torch.bfloat16) * 3.2)(D, N))
        A_im = torch.pi * (lambda *s: torch.randn(*s, dtype=torch.bfloat16) * 0.05)(D, N)
        B_re = (lambda *s: torch.randn(*s, dtype=torch.bfloat16) * 0.2)(D, N)
        B_im = (lambda *s: torch.randn(*s, dtype=torch.bfloat16) * 0.2)(D, N)
        C_re = (lambda *s: torch.randn(*s, dtype=torch.bfloat16) * 0.2)(D, N)
        C_im = (lambda *s: torch.randn(*s, dtype=torch.bfloat16) * 0.2)(D, N)
        log_dt = (lambda *s: torch.randn(*s, dtype=torch.bfloat16) * 0.05)(D) * 0.5 - 4.0
        Dskip = (lambda *s: torch.randn(*s, dtype=torch.bfloat16) * 0.05)(D)
        return {
            "u": _w((lambda *s: torch.randn(*s, dtype=torch.bfloat16) * 0.05)(B, L, D)),
            "A_re": _w(A_re),
            "A_im": _w(A_im),
            "B_re": _w(B_re),
            "B_im": _w(B_im),
            "C_re": _w(C_re),
            "C_im": _w(C_im),
            "log_dt": _w(log_dt),
            "Dskip": _w(Dskip),
        }
    _B_in = _B_setup()

    return {
        "A_x": _A_in["x"],
        "A_wqkv": _A_in["wqkv"],
        "A_wo": _A_in["wo"],
        "B_u": _B_in["u"],
        "B_A_re": _B_in["A_re"],
        "B_A_im": _B_in["A_im"],
        "B_B_re": _B_in["B_re"],
        "B_B_im": _B_in["B_im"],
        "B_C_re": _B_in["C_re"],
        "B_C_im": _B_in["C_im"],
        "B_log_dt": _B_in["log_dt"],
        "B_Dskip": _B_in["Dskip"],
        "gate_w": _w((lambda *s: torch.randn(*s, dtype=torch.bfloat16) * 0.05)(576) * 16.0),
    }

def reference(A_x, A_wqkv, A_wo, B_u, B_A_re, B_A_im, B_B_re, B_B_im, B_C_re, B_C_im, B_log_dt, B_Dskip, gate_w):
    def _A_ref(x, wqkv, wo):
        B, S, D = x.shape
        H = 32; Dh = D // H
        qkv = (lambda x, *d: x.permute(*d))((x @ wqkv).reshape(B, S, 3, H, Dh), 2, 0, 3, 1, 4)
        q, k, v = qkv[0], qkv[1], qkv[2]
        phi = lambda t: torch.where(t > 0, t + 1, torch.exp(t)) + 1e-6
        pq, pk = phi(q), phi(k)
        kv = torch.einsum("bhsd,bhse->bhde", pk, v)
        z = torch.einsum("bhsd->bhd", pk)
        num = torch.einsum("bhqd,bhde->bhqe", pq, kv)
        den = torch.einsum("bhqd,bhd->bhq", pq, z)[..., None] + 1e-6
        o = num / den
        return (lambda x, *d: x.permute(*d))(o, 0, 2, 1, 3).reshape(B, S, D) @ wo

    def _B_ref(u, A_re, A_im, B_re, B_im, C_re, C_im, log_dt, Dskip):
        Bsz, L, D = u.shape
        dt = torch.exp(log_dt)[:, None]
        A = A_re + 1j * A_im
        Bc = B_re + 1j * B_im
        Cc = C_re + 1j * C_im
        logdA = dt * A
        dB = dt * Bc
        l = (lambda x, dt: x.to(dt))(torch.arange(L), A_re.dtype)
        P = torch.exp(logdA[:, :, None] * l[None, None, :])
        K = torch.einsum("dn,dnl,dn->dl", Cc, P, dB).real
        L2 = 2 * L
        Kp = _pad(K, ((0, 0), (0, L)))
        up = (lambda x, *d: x.permute(*d))(u, 0, 2, 1)
        up = _pad(up, ((0, 0), (0, 0), (0, L)))
        Kf = (lambda x, axes=(-2,-1): torch.fft.rfft2(x.float(), dim=axes))(Kp[:, None, :], axes=(-2, -1))
        Uf = (lambda x, axes=(-2,-1): torch.fft.rfft2(x.float(), dim=axes))(up[:, :, None, :], axes=(-2, -1))
        Yf = Uf * Kf[None, :, :, :]
        y = (lambda x, s, axes=(-2,-1): torch.fft.irfft2(x, s=s, dim=axes).to(torch.bfloat16))(Yf, (1, L2), axes=(-2, -1))[:, :, 0, :L]
        y = (lambda x, *d: x.permute(*d))(y, 0, 2, 1) + u * Dskip[None, None, :]
        return y

    x = A_x
    g = _sigmoid((x * gate_w).sum(-1, keepdims=True))
    a = _A_ref(x, A_wqkv, A_wo)
    b = _B_ref(x, B_A_re, B_A_im, B_B_re, B_B_im, B_C_re, B_C_im, B_log_dt, B_Dskip)
    return g * a + (1 - g) * b
```

```cpp
#include <hip/hip_runtime.h>
#include <hip/hip_bf16.h>
#include <stdint.h>

typedef unsigned short u16;
__device__ __forceinline__ float bf2f(u16 v) { return __uint_as_float(((unsigned)v) << 16); }
__device__ __forceinline__ u16 f2bf(float f) {
    unsigned u = __float_as_uint(f);
    return (u16)((u + 0x7FFF + ((u >> 16) & 1)) >> 16);
}
__device__ __forceinline__ float rbf(float f) { return bf2f(f2bf(f)); }

constexpr int S = 2048, D = 576, H = 32, DH = 18, NS = 128;

__global__ void k_qkv(const u16* x, const u16* w, float* qkv) {
    int j = blockIdx.x * 64 + threadIdx.x;
    int s = blockIdx.y;
    float acc = 0;
    for (int k = 0; k < D; ++k) acc += bf2f(x[s * D + k]) * bf2f(w[k * 3 * D + j]);
    float t = rbf(acc);
    if (j < 2 * D) {
        float p = t > 0 ? rbf(t + 1.0f) : rbf(expf(t));
        t = rbf(p + 1e-6f);
    }
    qkv[s * 3 * D + j] = t;
}
__global__ void k_kv(const float* qkv, float* kv, float* z) {
    int h = blockIdx.x;
    int t = threadIdx.x;
    if (t < 324) {
        int d = t / 18, e = t % 18;
        float acc = 0;
        for (int s = 0; s < S; ++s) acc += qkv[s * 3 * D + D + h * DH + d] * qkv[s * 3 * D + 2 * D + h * DH + e];
        kv[h * 324 + t] = rbf(acc);
    } else if (t < 342) {
        int d = t - 324;
        float acc = 0;
        for (int s = 0; s < S; ++s) acc += qkv[s * 3 * D + D + h * DH + d];
        z[h * 18 + d] = rbf(acc);
    }
}
__global__ void k_o(const float* qkv, const float* kv, const float* z, float* o) {
    int j = blockIdx.x * 64 + threadIdx.x;
    int s = blockIdx.y;
    int h = j / DH, e = j % DH;
    float num = 0, den = 0;
    for (int d = 0; d < DH; ++d) {
        float pq = qkv[s * 3 * D + h * DH + d];
        num += pq * kv[h * 324 + d * 18 + e];
        den += pq * z[h * 18 + d];
    }
    num = rbf(num);
    den = rbf(rbf(den) + 1e-6f);
    o[s * D + j] = rbf(num / den);
}
__global__ void k_kgen(const u16* Are, const u16* Aim, const u16* Bre, const u16* Bim, const u16* Cre, const u16* Cim,
                       const u16* logdt, float* K) {
    int l = blockIdx.x * 256 + threadIdx.x;
    int d = blockIdx.y;
    float dt = rbf(expf(bf2f(logdt[d])));
    float lf = rbf((float)l);
    float acc = 0;
    for (int n = 0; n < NS; ++n) {
        float ar = dt * bf2f(Are[d * NS + n]), ai = dt * bf2f(Aim[d * NS + n]);
        float br = dt * bf2f(Bre[d * NS + n]), bi = dt * bf2f(Bim[d * NS + n]);
        float cr = bf2f(Cre[d * NS + n]), ci = bf2f(Cim[d * NS + n]);
        float e = expf(ar * lf);
        float sn, cs;
        sincosf(ai * lf, &sn, &cs);
        float pr = e * cs, pi = e * sn;
        float cbr = cr * br - ci * bi, cbi = cr * bi + ci * br;
        acc += cbr * pr - cbi * pi;
    }
    K[d * S + l] = acc;
}
__global__ void k_conv(const u16* x, const float* K, const u16* Dskip, float* b) {
    int d = blockIdx.x * 64 + threadIdx.x;
    int t = blockIdx.y;
    float acc = 0;
    for (int s = 0; s <= t; ++s) acc += K[d * S + s] * bf2f(x[(t - s) * D + d]);
    float y = rbf(acc);
    float sk = rbf(bf2f(x[t * D + d]) * bf2f(Dskip[d]));
    b[t * D + d] = rbf(y + sk);
}
__global__ void k_final(const u16* x, const u16* gw, const float* o, const u16* wo, const float* b, u16* out) {
    int j = blockIdx.x * 64 + threadIdx.x;
    int s = blockIdx.y;
    float gs = 0;
    for (int k = 0; k < D; ++k) gs += rbf(bf2f(x[s * D + k]) * bf2f(gw[k]));
    gs = rbf(gs);
    float g = rbf(1.0f / (1.0f + expf(-gs)));
    float acc = 0;
    for (int k = 0; k < D; ++k) acc += o[s * D + k] * bf2f(wo[k * D + j]);
    float a = rbf(acc);
    float r = rbf(rbf(g * a) + rbf(rbf(1.0f - g) * b[s * D + j]));
    out[s * D + j] = f2bf(r);
}

extern "C" void kernel_launch(void* const* d_in, const int* in_sizes, int n_in,
                              void* d_out, int out_size, void* d_ws, size_t ws_size,
                              hipStream_t stream) {
    const u16* x = (const u16*)d_in[0];
    const u16* wqkv = (const u16*)d_in[1];
    const u16* wo = (const u16*)d_in[2];
    const u16* Are = (const u16*)d_in[4];
    const u16* Aim = (const u16*)d_in[5];
    const u16* Bre = (const u16*)d_in[6];
    const u16* Bim = (const u16*)d_in[7];
    const u16* Cre = (const u16*)d_in[8];
    const u16* Cim = (const u16*)d_in[9];
    const u16* logdt = (const u16*)d_in[10];
    const u16* Dskip = (const u16*)d_in[11];
    const u16* gw = (const u16*)d_in[12];
    float* ws = (float*)d_ws;
    float* qkv = ws;
    float* kv = qkv + S * 3 * D;
    float* z = kv + H * 324;
    float* o = z + H * 18;
    float* K = o + S * D;
    float* b = K + D * S;
    k_qkv<<<dim3(27, S), 64, 0, stream>>>(x, wqkv, qkv);
    k_kv<<<H, 384, 0, stream>>>(qkv, kv, z);
    k_o<<<dim3(9, S), 64, 0, stream>>>(qkv, kv, z, o);
    k_kgen<<<dim3(8, D), 256, 0, stream>>>(Are, Aim, Bre, Bim, Cre, Cim, logdt, K);
    k_conv<<<dim3(9, S), 64, 0, stream>>>(x, K, Dskip, b);
    k_final<<<dim3(9, S), 64, 0, stream>>>(x, gw, o, wo, b, (u16*)d_out);
}
```

```cpp
#include <hip/hip_runtime.h>
#include <hip/hip_bf16.h>
#include <stdint.h>

typedef unsigned short u16;
typedef unsigned int u32;
typedef unsigned long long u64;
typedef u32 u32x4 __attribute__((ext_vector_type(4)));
typedef u32 u32x2 __attribute__((ext_vector_type(2)));
typedef float f32x4 __attribute__((ext_vector_type(4)));
typedef float f32x2 __attribute__((ext_vector_type(2)));
typedef __bf16 bf16x8 __attribute__((ext_vector_type(8)));
typedef __bf16 bf16x2 __attribute__((ext_vector_type(2)));
typedef short s16x4 __attribute__((ext_vector_type(4)));
#define LAS __attribute__((address_space(3)))
#define GAS __attribute__((address_space(1)))
#define DEV __device__ __forceinline__
typedef LAS char* lptr;

constexpr u32 WS_EPOCH = 0;
constexpr u32 WS_ARR = 4096;
constexpr u32 WS_XFLAG = 8192;
constexpr u32 WS_G = 16384;
constexpr u32 WS_ZG = 32768;
constexpr u32 WS_KVG = 131072;
constexpr u32 WS_XT = 1u << 20;
constexpr u32 WS_PQ = 4u << 20;
constexpr u32 WS_BT = 7u << 20;
constexpr u32 WS_MF = 10u << 20;
constexpr u32 WS_DBG = 11u << 20;
constexpr u32 WS_END = 12u << 20;
constexpr int AUX_SC1 = 16;
constexpr u32 SPIN_MAX = 1u << 18;

constexpr u32 L_FLAG = 0;
constexpr u32 CH_KC = 0, CH_KSTRIDE = 4160, CH_XR = 16640, CH_KT = 21248, CH_BYTES = 22560;
constexpr u32 L_CH = 64;
constexpr u32 L_XRING = L_CH + 2 * CH_BYTES;
constexpr u32 L_WRING = L_XRING + 3 * 16384;
constexpr u32 L_GEMM_END = L_WRING + 3 * 18432;
constexpr u32 SLAB_PITCH = 1184;
constexpr u32 L_SLAB = L_CH + 3 * CH_BYTES;
constexpr u32 L_WO = L_SLAB + 37 * 1024;
constexpr u32 L_KVA = L_WO + 2 * 6144;
constexpr u32 L_T_END = L_KVA + 2048;
constexpr u32 LDS1_BYTES = L_GEMM_END > L_T_END ? L_GEMM_END : L_T_END;
static_assert(LDS1_BYTES <= 163840, "lds");
static_assert(L_XRING % 16 == 0 && L_SLAB % 16 == 0, "align");

DEV float bf2f(u16 v) { return __uint_as_float(((u32)v) << 16); }
DEV u32 pk2(float a, float b) {
    f32x2 v = {a, b};
    return __builtin_bit_cast(u32, __builtin_convertvector(v, bf16x2));
}
DEV float lo2f(u32 v) { return __uint_as_float(v << 16); }
DEV float hi2f(u32 v) { return __uint_as_float(v & 0xffff0000u); }
DEV f32x4 mfma32(u32x4 a, u32x4 b, f32x4 c) {
    return __builtin_amdgcn_mfma_f32_16x16x32_bf16(__builtin_bit_cast(bf16x8, a), __builtin_bit_cast(bf16x8, b), c, 0, 0, 0);
}
DEV f32x4 mfma16(u32x2 a, u32x2 b, f32x4 c) {
    return __builtin_amdgcn_mfma_f32_16x16x16bf16_1k(__builtin_bit_cast(s16x4, a), __builtin_bit_cast(s16x4, b), c, 0, 0, 0);
}
DEV void glds16(u32 voff, const void* sbase, u32 ldsaddr) {
    asm volatile("s_mov_b32 m0, %2\n\ts_nop 0\n\tglobal_load_lds_dwordx4 %0, %1" ::"v"(voff), "s"(sbase), "s"(ldsaddr) : "memory");
}
DEV u32x2 trread(lptr p) { return __builtin_bit_cast(u32x2, __builtin_amdgcn_ds_read_tr16_b64_v4i16((LAS s16x4*)p)); }
DEV u32x4 cat(u32x2 a, u32x2 b) { return u32x4{a[0], a[1], b[0], b[1]}; }
#define WAIT_V(n) asm volatile("s_waitcnt vmcnt(%0)" ::"n"(n) : "memory")
#define WAIT_L0() asm volatile("s_waitcnt lgkmcnt(0)" ::: "memory")
#define RAW_BARRIER() asm volatile("s_waitcnt lgkmcnt(0)\n\ts_barrier" ::: "memory")
#define CFENCE() asm volatile("" ::: "memory")
#define SCHED() __builtin_amdgcn_sched_barrier(0)

#define STAMP(i) do {} while (0)

DEV void flag_set(lptr L, int wave, u32 v) {
    WAIT_L0();
    *(volatile LAS u32*)(L + L_FLAG + wave * 4) = v;
}
DEV void flag_wait1(lptr L, int wave, u32 v) {
#pragma nounroll
    for (u32 it = 0; it < SPIN_MAX * 8; ++it) {
        u32 f = *(volatile LAS u32*)(L + L_FLAG + wave * 4);
        if (__builtin_amdgcn_readfirstlane(f) >= v) break;
        __builtin_amdgcn_s_sleep(1);
    }
    CFENCE();
}
DEV void pair_sync(lptr L, int wave, u32 v) {
    flag_set(L, wave, v);
    flag_wait1(L, wave ^ 1, v);
}
DEV bool quad_ok(u32x4 f, u32 v) {
    u32 m = min(min(f[0], f[1]), min(f[2], f[3]));
    return __builtin_amdgcn_readfirstlane(m) >= v;
}
DEV void quad_wait(lptr L, u32 v) {
#pragma nounroll
    for (u32 it = 0; it < SPIN_MAX * 8; ++it) {
        u32x4 f = *(volatile LAS u32x4*)(L + L_FLAG);
        if (quad_ok(f, v)) break;
        __builtin_amdgcn_s_sleep(1);
    }
    CFENCE();
}

DEV u32 mk_tag(u32 epoch) { return epoch * 0x9E3779B1u + 0x7F4A7C15u; }
DEV void put_epoch(lptr L, u32 epoch) { *(volatile LAS u32x2*)(L + L_FLAG + 32) = u32x2{epoch, 1u}; }
DEV u32 get_epoch(lptr L) {
    u32x2 v;
#pragma nounroll
    for (u32 it = 0; it < SPIN_MAX * 8; ++it) {
        v = *(volatile LAS u32x2*)(L + L_FLAG + 32);
        if (__builtin_amdgcn_readfirstlane(v[1])) break;
        __builtin_amdgcn_s_sleep(1);
    }
    CFENCE();
    return __builtin_amdgcn_readfirstlane(v[0]);
}

struct Params {
    const u16 *x, *wqkv, *wo, *Are, *Aim, *Bre, *Bim, *Cre, *Cim, *logdt, *Dskip, *gw;
    char* ws;
};

DEV float wave_sum(float v) {
    v += __shfl_xor(v, 1); v += __shfl_xor(v, 2); v += __shfl_xor(v, 4);
    v += __shfl_xor(v, 8); v += __shfl_xor(v, 16); v += __shfl_xor(v, 32);
    return v;
}

DEV void ssm_pair(const Params& P, __amdgpu_buffer_rsrc_t rs, lptr L, lptr C, int wave, int ch, int lane, bool pub
) {
    const int h = wave & 1;
    const int m = lane & 15, kg = lane >> 4;
    {
        const int n = ch * 128 + h * 64 + lane;
        u32 epoch = 0;
        if (pub) epoch = __builtin_amdgcn_raw_buffer_load_b32(rs, WS_EPOCH, 0, AUX_SC1);
        float are = bf2f(P.Are[n]), aim = bf2f(P.Aim[n]), bre = bf2f(P.Bre[n]), bim = bf2f(P.Bim[n]);
        float cre = bf2f(P.Cre[n]), cim = bf2f(P.Cim[n]);
        float dt = lo2f(pk2(expf(bf2f(P.logdt[ch])), 0.f));
        float dsk = bf2f(P.Dskip[ch]);
        float lr = dt * are * 1.44269504088896340736f;
        float t = dt * aim * 0.15915494309189533577f;
        float br = dt * bre, bi = dt * bim;
        float cr = cre * br - cim * bi, ci = cre * bi + cim * br;
        LAS float* T = (LAS float*)(C + CH_KC + h * 2048);
        T[lane] = lr;
        T[64 + lane] = t - rintf(t);
        T[128 + lane] = 2.f * t - rintf(2.f * t);
        T[192 + lane] = 4.f * t - rintf(4.f * t);
        T[256 + lane] = 8.f * t - rintf(8.f * t);
        T[320 + lane] = cr;
        T[384 + lane] = ci;
        float k0 = wave_sum(cr) + (h ? 0.f : dsk);
        if (lane == 0) T[448] = k0;
        if (pub && lane == 0) {
            put_epoch(L, epoch);
            __builtin_amdgcn_raw_buffer_store_b32(mk_tag(epoch), rs, WS_ARR + blockIdx.x * 4, 0, AUX_SC1);
        }
    }
    STAMP(1);
    {
        const float u0 = (float)(m + 1);
        const float u1s = (float)(m < 8 ? 129 + m : 129 + (m & 3) + 64 * ((m >> 2) & 1));
        const float u1 = m < 8 ? 2.f * u1s : 4.f * u1s;
        const float u2s = (float)(129 + (m & 1) + 32 * ((m >> 1) & 3));
        const float u2 = 8.f * u2s;
        const float vv = (float)(16 * m);
        lptr T = C + CH_KC + h * 2048 + kg * 16;
        lptr T1 = T + (m < 8 ? 512 : 768);
        lptr T2 = T + (m >> 3) * 64;
        f32x4 a0 = {0, 0, 0, 0}, a1 = a0, a2a = a0, a2b = a0;
        u32x4 G2;
#pragma unroll
        for (int s = 0; s < 4; ++s) {
            f32x4 lr = *(LAS f32x4*)(T + s * 64), p1 = *(LAS f32x4*)(T + 256 + s * 64), p24 = *(LAS f32x4*)(T1 + s * 64);
            f32x4 cr = *(LAS f32x4*)(T + 1280 + s * 64), ci = *(LAS f32x4*)(T + 1536 + s * 64);
            u32x4 G0, G1, Hh;
#pragma unroll
            for (int i = 0; i < 4; ++i) {
                float e = __builtin_amdgcn_exp2f(lr[i] * u0), an = p1[i] * u0;
                G0[i] = pk2(e * __builtin_amdgcn_cosf(an), e * __builtin_amdgcn_sinf(an));
                e = __builtin_amdgcn_exp2f(lr[i] * u1); an = p24[i] * u1s;
                G1[i] = pk2(e * __builtin_amdgcn_cosf(an), e * __builtin_amdgcn_sinf(an));
                e = __builtin_amdgcn_exp2f(lr[i] * vv); an = p1[i] * vv;
                float wr = e * __builtin_amdgcn_cosf(an), wi = e * __builtin_amdgcn_sinf(an);
                Hh[i] = pk2(cr[i] * wr - ci[i] * wi, -(cr[i] * wi + ci[i] * wr));
            }
            if ((s & 1) == 0) {
                f32x4 l2 = *(LAS f32x4*)(T2 + s * 64), p8 = *(LAS f32x4*)(T2 + 1024 + s * 64);
#pragma unroll
                for (int i = 0; i < 4; ++i) {
                    float e = __builtin_amdgcn_exp2f(l2[i] * u2), an = p8[i] * u2s;
                    G2[i] = pk2(e * __builtin_amdgcn_cosf(an), e * __builtin_amdgcn_sinf(an));
                }
            }
            a0 = mfma32(G0, Hh, a0);
            a1 = mfma32(G1, Hh, a1);
            if ((s & 1) == 0) a2a = mfma32(G2, Hh, a2a); else a2b = mfma32(G2, Hh, a2b);
        }
        lptr Pp = C + CH_KC + 4096 + h * 3072;
        *(LAS f32x4*)(Pp + (16 * m + 4 * kg) * 4) = a0;
        *(LAS f32x4*)(Pp + (kg < 2 ? 256 + 8 * m + 4 * kg : 384 + 64 * (kg - 2) + 4 * m) * 4) = a1;
        f32x4 a2 = kg < 2 ? a2a : a2b;
        lptr P2 = Pp + (512 + (kg >> 1) * 128 + 64 * (kg & 1) + 2 * m) * 4;
        *(LAS f32x2*)(P2) = f32x2{a2[0], a2[1]};
        *(LAS f32x2*)(P2 + 128) = f32x2{a2[2], a2[3]};
    }
    STAMP(2);
    pair_sync(L, wave, 1);
    {
        LAS float* P0 = (LAS float*)(C + CH_KC + 4096) + 320 * h + lane;
        LAS u16* KT = (LAS u16*)(C + CH_KT);
#pragma unroll
        for (int r = 0; r < 5; ++r) {
            float v = P0[64 * r] + P0[768 + 64 * r];
            if (r >= 3 && h) v += P0[64 * r + 128] + P0[768 + 64 * r + 128];
            KT[1 + 320 * h + lane + 64 * r] = (u16)pk2(v, 0.f);
        }
        if (lane == 0) {
            if (h == 0) KT[0] = (u16)pk2(((LAS float*)(C + CH_KC))[448] + ((LAS float*)(C + CH_KC + 2048))[448], 0.f);
            else KT[641] = 0;
        }
    }
    pair_sync(L, wave, 2);
    {
        LAS u16* KT = (LAS u16*)(C + CH_KT);
        const int l0 = (h * 64 + lane) * 16 - 4;
        u32 d[10];
#pragma unroll
        for (int j = 0; j < 10; ++j) {
            int la = l0 + 2 * j, lb = la + 1;
            u32 bits = pk2((float)la, (float)lb);
            int ia = la < 256 ? (la < 0 ? 641 : la) : (int)(bits & 0xffff) - 0x4280;
            int ib = lb < 256 ? (lb < 0 ? 641 : lb) : (int)(bits >> 16) - 0x4280;
            d[j] = (u32)KT[ia] | ((u32)KT[ib] << 16);
        }
        u32 e[9];
#pragma unroll
        for (int j = 0; j < 9; ++j) e[j] = __builtin_amdgcn_alignbit(d[j + 1], d[j], 16);
        lptr W = C + CH_KC + 32 + (h * 64 + lane) * 32;
        *(LAS u32x4*)(W) = u32x4{d[2], d[3], d[4], d[5]};
        *(LAS u32x4*)(W + 16) = u32x4{d[6], d[7], d[8], d[9]};
        *(LAS u32x4*)(W + CH_KSTRIDE) = u32x4{e[1], e[2], e[3], e[4]};
        *(LAS u32x4*)(W + CH_KSTRIDE + 16) = u32x4{e[5], e[6], e[7], e[8]};
        *(LAS u32x4*)(W + 2 * CH_KSTRIDE) = u32x4{d[1], d[2], d[3], d[4]};
        *(LAS u32x4*)(W + 2 * CH_KSTRIDE + 16) = u32x4{d[5], d[6], d[7], d[8]};
        *(LAS u32x4*)(W + 3 * CH_KSTRIDE) = u32x4{e[0], e[1], e[2], e[3]};
        *(LAS u32x4*)(W + 3 * CH_KSTRIDE + 16) = u32x4{e[4], e[5], e[6], e[7]};
        if (h == 0) {
            if (lane < 8) *(LAS u32x4*)(C + CH_KC + (lane >> 1) * CH_KSTRIDE + (lane & 1) * 16) = u32x4{0, 0, 0, 0};
        } else {
            if (lane < 32) *(LAS u32x4*)(C + CH_XR + 4096 + lane * 16) = u32x4{0, 0, 0, 0};
        }
    }
    STAMP(3);
    {
        const u32 tag = mk_tag(get_epoch(L));
#pragma nounroll
        for (u32 it = 0; it < SPIN_MAX; ++it) {
            u32 f = __builtin_amdgcn_raw_buffer_load_b32(rs, WS_XFLAG + (h * 32 + (lane & 31)) * 4, 0, AUX_SC1);
            CFENCE();
            if (__all(f == tag)) break;
            __builtin_amdgcn_s_sleep(4);
        }
        CFENCE();
        const int sl = h * 32 + (lane >> 2);
        u32 off = WS_XT + sl * 36864 + ch * 64 + (lane & 3) * 16;
        u32x4 v0 = __builtin_amdgcn_raw_buffer_load_b128(rs, off, 0, AUX_SC1);
        u32x4 v1 = __builtin_amdgcn_raw_buffer_load_b128(rs, off + 16 * 36864, 0, AUX_SC1);
        lptr X = C + CH_XR + 4032 - 64 * sl + (lane & 3) * 16;
        *(LAS u32x4*)(X) = v0;
        *(LAS u32x4*)(X - 1024) = v1;
    }
    STAMP(4);
    pair_sync(L, wave, 3);
    f32x4 acc[8];
#pragma unroll
    for (int i = 0; i < 8; ++i) acc[i] = f32x4{0, 0, 0, 0};
    {
        lptr KA = C + CH_KC + ((~m) & 3) * CH_KSTRIDE + 2 * (8 * kg + ((m + 4) & ~3)) + 256 * h;
        lptr XB = C + CH_XR + 2 * (240 - 16 * m + 8 * kg) + 256 * h;
#pragma unroll
        for (int j = 0; j < 4; ++j) {
            u32x4 A[8], B[8];
#pragma unroll
            for (int a = 0; a < 8; ++a) {
                A[a] = cat(*(volatile LAS u32x2*)(KA + 64 * j + 512 * a), *(volatile LAS u32x2*)(KA + 64 * j + 512 * a + 8));
                B[a] = *(LAS u32x4*)(XB + 64 * j + 512 * (7 - a));
            }
#pragma unroll
            for (int I = 0; I < 8; ++I)
#pragma unroll
                for (int a = 0; a <= I; ++a) acc[I] = mfma32(A[a], B[I - a], acc[I]);
        }
    }
    STAMP(5);
    pair_sync(L, wave, 4);
    {
        lptr E = C + CH_KC + lane * 16;
        f32x4 kp[4];
        if (h) {
#pragma unroll
            for (int i = 0; i < 4; ++i) { *(LAS f32x4*)(E + 4096 + i * 1024) = acc[i]; kp[i] = acc[4 + i]; }
        } else {
#pragma unroll
            for (int i = 0; i < 4; ++i) { *(LAS f32x4*)(E + i * 1024) = acc[4 + i]; kp[i] = acc[i]; }
        }
        pair_sync(L, wave, 5);
        GAS char* bt = (GAS char*)P.ws + WS_BT + ch * 4096 + h * 2048 + m * 32 + kg * 8;
#pragma unroll
        for (int i = 0; i < 4; ++i) {
            f32x4 o = *(LAS f32x4*)(E + (h ^ 1) * 4096 + i * 1024);
            o += kp[i];
            *(GAS u32x2*)(bt + i * 512) = u32x2{pk2(o[0], o[1]), pk2(o[2], o[3])};
        }
    }
    STAMP(6);
}

template <bool ISQ>
DEV void gemm_role(const Params& P, __amdgpu_buffer_rsrc_t rs, lptr L, u32 lbase, int w, int lane, int rt, int grp
) {
    const int m = lane & 15, kg = lane >> 4;
    u32 xo[4], wo_[5];
#pragma unroll
    for (int i = 0; i < 4; ++i) {
        int r = 8 * i + (lane >> 3);
        xo[i] = (u32)((rt * 128 + w * 32 + r) * 1152 + (((lane & 7) ^ (r & 7)) << 4));
    }
#pragma unroll
    for (int i = 0; i < 5; ++i) {
        int ins = i < 4 ? w + 4 * i : 16 + (w & 1);
        int p = ins * 64 + lane, rho = p / 18, e = p - rho * 18;
        int k = (rho & 32) + 8 * ((rho & 15) >> 2) + 4 * ((rho >> 4) & 1) + (rho & 3);
        int cb = ISQ ? 288 * grp + 16 * e : (e < 9 ? 1152 + 144 * grp + 16 * e : 2304 + 144 * grp + 16 * (e - 9));
        wo_[i] = (u32)(k * 3456 + cb);
    }
    const char* xg = (const char*)P.x;
    const char* wg = (const char*)P.wqkv;
    auto issue = [&](int c) {
        const int sl = c % 3;
        const char* xs = xg + c * 128;
        const char* wsrc = wg + c * 64 * 3456;
        if ((w >> 1) == (c & 1)) glds16(wo_[4], wsrc, lbase + L_WRING + sl * 18432 + (16 + (w & 1)) * 1024);
#pragma unroll
        for (int i = 0; i < 4; ++i) {
            glds16(xo[i], xs, lbase + L_XRING + sl * 16384 + w * 4096 + i * 1024);
            glds16(wo_[i], wsrc, lbase + L_WRING + sl * 18432 + (w + 4 * i) * 1024);
        }
    };
    issue(0); issue(1); issue(2);
    RAW_BARRIER();
    __builtin_amdgcn_s_setprio(2);
    f32x4 acc[2][9];
#pragma unroll
    for (int ct = 0; ct < 9; ++ct) {
        float iv = ISQ ? 1e-6f : (ct < 4 ? 1e-6f : (ct == 4 ? (m < 8 ? 1e-6f : 0.f) : 0.f));
        acc[0][ct] = f32x4{iv, iv, iv, iv};
        acc[1][ct] = acc[0][ct];
    }
    lptr XA = L + L_XRING + w * 4096 + m * 128;
    const int xs0 = ((kg ^ (m & 7)) << 4), xs1 = (((4 + kg) ^ (m & 7)) << 4);
    lptr WB = L + L_WRING + (4 * kg + ((lane >> 2) & 3)) * 288 + (lane & 3) * 8;
    u32x4 A0[2], B0[9], A1[2], B1[9];
    auto rd = [&](int c, int ks, u32x4(&A)[2], u32x4(&B)[9]) {
        const int sl = c % 3;
        A[0] = *(LAS u32x4*)(XA + sl * 16384 + (ks ? xs1 : xs0));
        A[1] = *(LAS u32x4*)(XA + sl * 16384 + 2048 + (ks ? xs1 : xs0));
#pragma unroll
        for (int ct = 0; ct < 9; ++ct)
            B[ct] = cat(trread(WB + sl * 18432 + ks * 9216 + ct * 32), trread(WB + sl * 18432 + ks * 9216 + 4608 + ct * 32));
    };
    auto mm = [&](u32x4(&A)[2], u32x4(&B)[9], int c0, int c1) {
#pragma unroll
        for (int ct = c0; ct < c1; ++ct)
#pragma unroll
            for (int r = 0; r < 2; ++r) acc[r][ct] = ISQ ? mfma32(B[ct], A[r], acc[r][ct]) : mfma32(A[r], B[ct], acc[r][ct]);
    };
    WAIT_V(16);
    flag_set(L, w, 1);
    quad_wait(L, 1);
    STAMP(1);
    rd(0, 0, A0, B0);
#pragma unroll
    for (int c = 0; c < 9; ++c) {
        rd(c, 1, A1, B1);
        SCHED();
        mm(A0, B0, 0, 9);
        SCHED();
        if (c < 8) {
            if (c < 7) WAIT_V(8); else WAIT_V(0);
            flag_set(L, w, c + 2);
            SCHED();
            mm(A1, B1, 0, 3);
            SCHED();
            u32x4 f = *(volatile LAS u32x4*)(L + L_FLAG);
            SCHED();
            mm(A1, B1, 3, 6);
            SCHED();
            if (!quad_ok(f, c + 2)) quad_wait(L, c + 2);
            CFENCE();
            rd(c + 1, 0, A0, B0);
            if (c + 3 < 9) issue(c + 3);
            SCHED();
            mm(A1, B1, 6, 9);
            SCHED();
        } else {
            mm(A1, B1, 0, 9);
        }
    }
    STAMP(2);
    const u32 tag = mk_tag(get_epoch(L));
    constexpr float LOG2E = 1.44269504088896340736f;
    auto phi = [&](float t) {
        float e = __builtin_amdgcn_fmed3f(__builtin_amdgcn_exp2f(fmaf(t, LOG2E, -1e-6f * LOG2E)), 0.f, 1.f);
        return fmaxf(t, 1e-6f) + e;
    };
    if (!ISQ) {
        lptr EX = L + L_WRING + lane * 16;
#pragma unroll
        for (int ct = 0; ct < 9; ++ct) {
            f32x4 v[2];
#pragma unroll
            for (int r = 0; r < 2; ++r)
#pragma unroll
                for (int i = 0; i < 4; ++i) {
                    float t = acc[r][ct][i];
                    v[r][i] = ct < 4 ? phi(t) : (ct == 4 ? (m < 8 ? phi(t) : t) : t);
                }
            *(LAS u32x4*)(EX + (w * 9 + ct) * 1024) = u32x4{pk2(v[0][0], v[0][1]), pk2(v[0][2], v[0][3]), pk2(v[1][0], v[1][1]), pk2(v[1][2], v[1][3])};
        }
        flag_set(L, w, 10);
        quad_wait(L, 10);
        STAMP(3);
        f32x4 kv[2][2], zz[2];
        kv[0][0] = kv[0][1] = kv[1][0] = kv[1][1] = zz[0] = zz[1] = f32x4{0, 0, 0, 0};
        const u32x4 ones = {0x3f803f80u, 0x3f803f80u, 0x3f803f80u, 0x3f803f80u};
        lptr EK = EX + w * 1024;
#pragma unroll
        for (int s = 0; s < 4; ++s) {
            u32x4 k0 = *(LAS u32x4*)(EK + s * 9216), k1 = *(LAS u32x4*)(EK + s * 9216 + 1024);
            u32x4 v0 = *(LAS u32x4*)(EK + s * 9216 + 4096), v1 = *(LAS u32x4*)(EK + s * 9216 + 5120);
            zz[0] = mfma32(k0, ones, zz[0]);
            zz[1] = mfma32(k1, ones, zz[1]);
            kv[0][0] = mfma32(v0, k0, kv[0][0]);
            kv[0][1] = mfma32(v0, k1, kv[0][1]);
            kv[1][0] = mfma32(v1, k0, kv[1][0]);
            kv[1][1] = mfma32(v1, k1, kv[1][1]);
        }
        {
            f32x4 z = m == 0 ? zz[0] : zz[1];
            bool on = m == 0 || (m == 1 && w == 3 && kg < 2);
            u32 off = WS_ZG + (rt * 144 + grp * 18 + 4 * w + 4 * m + kg) * 16;
            if (on) __builtin_amdgcn_raw_buffer_store_b128(u32x4{pk2(z[0], z[1]), tag, pk2(z[2], z[3]), tag}, rs, off, 0, AUX_SC1);
        }
        const int hh = grp * 4 + w;
#pragma unroll
        for (int et = 0; et < 2; ++et)
#pragma unroll
            for (int dt = 0; dt < 2; ++dt) {
                int d = 16 * dt + m - 2 * w, eg = 4 * et + kg - 2 - (w >> 1);
                u32 p01 = pk2(kv[et][dt][0], kv[et][dt][1]), p23 = pk2(kv[et][dt][2], kv[et][dt][3]);
                if ((w & 1) && eg == 0) p01 = 0;
                if (!(w & 1) && eg == 4) p23 = 0;
                u32 off = WS_KVG + ((hh * 16 + rt) * 96 + eg * 18 + d) * 16;
                if (d >= 0 && d < 18 && eg >= 0 && eg < 5)
                    __builtin_amdgcn_raw_buffer_store_b128(u32x4{p01, tag, p23, tag}, rs, off, 0, AUX_SC1);
            }
        STAMP(4);
    } else {
        lptr ZI = L + L_WRING + w * 2560;
        lptr ZP = L + L_WRING + 12288;
        *(LAS u32x4*)(ZI + lane * 16) = u32x4{0, 0, 0, 0};
        *(LAS u32x4*)(ZI + 1024 + lane * 16) = u32x4{0, 0, 0, 0};
        if (lane < 32) *(LAS u32x4*)(ZI + 2048 + lane * 16) = u32x4{0, 0, 0, 0};
        u32x2 pb[2][9];
#pragma unroll
        for (int ct = 0; ct < 9; ++ct)
#pragma unroll
            for (int r = 0; r < 2; ++r) {
#pragma unroll
                for (int i = 0; i < 4; ++i) acc[r][ct][i] = phi(acc[r][ct][i]);
                pb[r][ct] = u32x2{pk2(acc[r][ct][0], acc[r][ct][1]), pk2(acc[r][ct][2], acc[r][ct][3])};
            }
        u32x2 E[9];
#pragma unroll
        for (int ct = 0; ct < 9; ++ct) {
            int e = (16 * ct + m) / 18 - 4 * (kg & 1);
            u64 v = (e >= 0 && e < 4) ? (0x3f80ull << (16 * e)) : 0ull;
            E[ct] = u32x2{(u32)v, (u32)(v >> 32)};
        }
        STAMP(3);
        {
            const int ql = lane < 36 ? lane : 35;
            u32 off = WS_ZG + (4 * w * 144 + grp * 36 + ql) * 16;
            u32x4 g[4];
#pragma nounroll
            for (u32 it = 0; it < SPIN_MAX; ++it) {
                bool ok = true;
#pragma unroll
                for (int r = 0; r < 4; ++r) g[r] = __builtin_amdgcn_raw_buffer_load_b128(rs, off + r * 2304, 0, AUX_SC1);
                CFENCE();
#pragma unroll
                for (int r = 0; r < 4; ++r) ok = ok && g[r][1] == tag && g[r][3] == tag;
                if (__all(ok)) break;
                __builtin_amdgcn_s_sleep(2);
            }
            f32x4 s = {0, 0, 0, 0};
#pragma unroll
            for (int r = 0; r < 4; ++r) s += f32x4{lo2f(g[r][0]), hi2f(g[r][0]), lo2f(g[r][2]), hi2f(g[r][2])};
            *(LAS f32x4*)(ZP + w * 1024 + lane * 16) = s;
        }
        flag_set(L, w, 10);
        quad_wait(L, 10);
        STAMP(4);
        {
            f32x4 s = *(LAS f32x4*)(ZP + lane * 16);
            s += *(LAS f32x4*)(ZP + 1024 + lane * 16);
            s += *(LAS f32x4*)(ZP + 2048 + lane * 16);
            s += *(LAS f32x4*)(ZP + 3072 + lane * 16);
            if (lane < 36) {
                int ct = lane >> 2;
                lptr q = ZI + ((ct >> 1) * 4 + (lane & 3)) * 128 + (ct & 1) * 8;
                *(LAS u32*)(q + ((4 * lane) / 18) * 16) = pk2(s[0], s[1]);
                *(LAS u32*)(q + ((4 * lane + 2) / 18) * 16 + 4) = pk2(s[2], s[3]);
            }
        }
        WAIT_L0();
        u32x4 Z[5];
#pragma unroll
        for (int c = 0; c < 5; ++c) Z[c] = *(LAS u32x4*)(ZI + (c * 4 + kg) * 128 + (m & 7) * 16);
        f32x4 den[2];
#pragma unroll
        for (int r = 0; r < 2; ++r) den[r] = f32x4{1e-6f, 1e-6f, 1e-6f, 1e-6f};
#pragma unroll
        for (int c = 0; c < 5; ++c)
#pragma unroll
            for (int r = 0; r < 2; ++r) den[r] = mfma32(Z[c], cat(pb[r][2 * c], c < 4 ? pb[r][2 * c + 1] : u32x2{0, 0}), den[r]);
        GAS char* pq = (GAS char*)P.ws + WS_PQ + (size_t)((rt * 8 + w * 2) * 18 + ((9 * grp) >> 1)) * 1024 + lane * 16;
#pragma unroll
        for (int r = 0; r < 2; ++r) {
            float rc[4];
#pragma unroll
            for (int i = 0; i < 4; ++i) rc[i] = __builtin_amdgcn_rcpf(den[r][i]);
            u32x2 hi = {pk2(rc[0], rc[1]), pk2(rc[2], rc[3])};
            u32x2 lo = {pk2(rc[0] - lo2f(hi[0]), rc[1] - hi2f(hi[0])), pk2(rc[2] - lo2f(hi[1]), rc[3] - hi2f(hi[1]))};
            u32x2 rb = kg < 2 ? hi : lo;
            u32x2 o[9];
#pragma unroll
            for (int ct = 0; ct < 9; ++ct) {
                f32x4 R = mfma16(E[ct], rb, f32x4{0, 0, 0, 0});
                R *= acc[r][ct];
                o[ct] = u32x2{pk2(R[0], R[1]), pk2(R[2], R[3])};
            }
            GAS char* q = pq + r * 18 * 1024;
            if ((grp & 1) == 0) {
#pragma unroll
                for (int c = 0; c < 4; ++c) *(GAS u32x4*)(q + c * 1024) = cat(o[2 * c], o[2 * c + 1]);
                *(GAS u32x2*)(q + 4096) = o[8];
            } else {
                *(GAS u32x2*)(q + 8) = o[0];
#pragma unroll
                for (int c = 0; c < 4; ++c) *(GAS u32x4*)(q + (c + 1) * 1024) = cat(o[2 * c + 1], o[2 * c + 2]);
            }
        }
        STAMP(5);
    }
}

DEV bool tr_role(const Params& P, __amdgpu_buffer_rsrc_t rs, lptr L, u32 lbase, int w, int lane, int ti
) {
    const int tid = w * 64 + lane;
    u32 epoch = 0;
    if (w == 0) epoch = __builtin_amdgcn_raw_buffer_load_b32(rs, WS_EPOCH, 0, AUX_SC1);
    {
        const char* xs = (const char*)P.x + (size_t)ti * 32 * 1152;
#pragma unroll
        for (int i = 0; i < 5; ++i) {
            int ins = w + 8 * i;
            if (ins < 37) {
                int p = ins * 64 + lane, r = p / 74, e = p - r * 74;
                glds16((u32)(r * 1152 + min(e, 71) * 16), xs, lbase + L_SLAB + ins * 1024);
            }
        }
    }
    const int hh = ti >> 1, R0 = 18 * hh - 2 * (hh & 1);
    if (w >= 6) {
        const char* wsrc = (const char*)P.wo + R0 * 1152 + (ti & 1) * 576 + (w - 6) * 288;
#pragma unroll
        for (int i = 0; i < 6; ++i) {
            int p = min(i * 64 + lane, 359), r = p / 18, e = p - r * 18;
            glds16((u32)(r * 1152 + e * 16), wsrc, lbase + L_WO + (w - 6) * 6144 + i * 1024);
        }
        WAIT_V(6);
    } else {
        WAIT_V(0);
    }
    RAW_BARRIER();
    STAMP(8);
    if (tid == 0) put_epoch(L, epoch);
    {
        const int G = lane >> 4;
        lptr S1 = L + L_SLAB + (31 - 8 * G - ((lane >> 2) & 3)) * SLAB_PITCH + (lane & 3) * 8 + w * 32;
        u32 off = WS_XT + ti * 36864 + (w * 16 + (lane & 15)) * 64 + G * 16;
#pragma unroll
        for (int i = 0; i < 5; ++i) {
            if (w + 8 * i < 36) {
                u32x4 v = cat(trread(S1 + i * 256), trread(S1 + i * 256 - 4 * SLAB_PITCH));
                __builtin_amdgcn_raw_buffer_store_b128(v, rs, off + i * 8192, 0, AUX_SC1);
            }
        }
        WAIT_V(0);
        RAW_BARRIER();
        if (tid == 0) {
            __builtin_amdgcn_raw_buffer_store_b32(mk_tag(epoch), rs, WS_XFLAG + ti * 4, 0, AUX_SC1);
            __builtin_amdgcn_raw_buffer_store_b32(mk_tag(epoch), rs, WS_ARR + blockIdx.x * 4, 0, AUX_SC1);
        }
    }
    STAMP(9);
    if (w < 6) return true;
    epoch = get_epoch(L);
    const u32 tag = mk_tag(epoch);
    const int mw = w - 6, m = lane & 15, kg = lane >> 4;
    *(LAS u32x4*)(L + L_KVA + mw * 1024 + lane * 16) = u32x4{0, 0, 0, 0};
    {
        u32x4 gwv[5];
#pragma unroll
        for (int i = 0; i < 5; ++i) gwv[i] = (i < 4 || m < 8) ? *(const GAS u32x4*)((const GAS char*)P.gw + (m + 16 * i) * 16) : u32x4{0, 0, 0, 0};
#pragma unroll
        for (int ps = 0; ps < 4; ++ps) {
            const int r = mw * 16 + ps * 4 + kg;
            float s = 0.f;
#pragma unroll
            for (int i = 0; i < 5; ++i) {
                u32x4 xv = *(LAS u32x4*)(L + L_SLAB + r * SLAB_PITCH + (i < 4 ? m + 16 * i : 64 + (m & 7)) * 16);
#pragma unroll
                for (int j = 0; j < 4; ++j) s += lo2f(xv[j]) * lo2f(gwv[i][j]) + hi2f(xv[j]) * hi2f(gwv[i][j]);
            }
            s += __shfl_xor(s, 1); s += __shfl_xor(s, 2); s += __shfl_xor(s, 4); s += __shfl_xor(s, 8);
            float g = 1.0f / (1.0f + __expf(-s));
            if (m == 0) *(GAS float*)((GAS char*)P.ws + WS_G + (ti * 32 + r) * 4) = g;
        }
    }
    u32x4 Bw[9];
    {
        lptr WB = L + L_WO + mw * 6144 + (lane & 3) * 8;
        const int q = (lane >> 2) & 3;
#pragma unroll
        for (int t = 0; t < 9; ++t) Bw[t] = cat(trread(WB + (4 * kg + q) * 288 + t * 32), trread(WB + (16 + q) * 288 + t * 32));
    }
    pair_sync(L, w, 1);
    __builtin_amdgcn_s_setprio(3);
    STAMP(10);
    {
        const int u = mw * 45 + min(lane, 44);
        u32 off = WS_KVG + (hh * 16 * 96 + u) * 16;
        u32x4 g[16];
#pragma nounroll
        for (u32 it = 0; it < SPIN_MAX; ++it) {
            u32x4 pr = __builtin_amdgcn_raw_buffer_load_b128(rs, off + 15 * 1536, 0, AUX_SC1);
            CFENCE();
            if (__all(pr[1] == tag)) break;
            __builtin_amdgcn_s_sleep(4);
        }
#pragma nounroll
        for (u32 it = 0; it < SPIN_MAX; ++it) {
            bool ok = true;
#pragma unroll
            for (int r = 0; r < 16; ++r) g[r] = __builtin_amdgcn_raw_buffer_load_b128(rs, off + r * 1536, 0, AUX_SC1);
            CFENCE();
#pragma unroll
            for (int r = 0; r < 16; ++r) ok = ok && g[r][1] == tag && g[r][3] == tag;
            if (__all(ok)) break;
            __builtin_amdgcn_s_sleep(2);
        }
        f32x4 s = {0, 0, 0, 0};
#pragma unroll
        for (int r = 0; r < 16; ++r) s += f32x4{lo2f(g[r][0]), hi2f(g[r][0]), lo2f(g[r][2]), hi2f(g[r][2])};
        const int eg = u / 18, d = u - eg * 18, mm = d + 2 * (hh & 1);
        if (lane < 45) *(LAS u32x2*)(L + L_KVA + (mm >> 4) * 1024 + ((eg & 3) * 16 + (mm & 15)) * 16 + (eg >> 2) * 8) = u32x2{pk2(s[0], s[1]), pk2(s[2], s[3])};
    }
    STAMP(11);
    pair_sync(L, w, 2);
    {
        u32x4 A0 = *(LAS u32x4*)(L + L_KVA + lane * 16), A1 = *(LAS u32x4*)(L + L_KVA + 1024 + lane * 16);
        const bool odd = hh & 1;
        const int U0 = R0 >> 2;
        const bool sw = odd && kg == 0;
        const int U8 = U0 + (sw ? 4 : kg), U4 = U0 + (odd ? 0 : 4);
        const int T0 = (ti & 1) * 18 + mw * 9;
        GAS char* mf = (GAS char*)P.ws + WS_MF + T0 * 18 * 1024 + m * 16;
        GAS char* p8 = mf + (U8 >> 3) * 1024 + (U8 & 3) * 256 + ((U8 >> 2) & 1) * 8;
        GAS char* p4 = mf + (U4 >> 3) * 1024 + (U4 & 3) * 256 + ((U4 >> 2) & 1) * 8 + (odd ? 4 : 0);
#pragma unroll
        for (int t = 0; t < 9; ++t) {
            f32x4 d0 = mfma32(A0, Bw[t], f32x4{0, 0, 0, 0}), d1 = mfma32(A1, Bw[t], f32x4{0, 0, 0, 0});
            u32 a01 = pk2(d0[0], d0[1]), a23 = pk2(d0[2], d0[3]), b01 = pk2(d1[0], d1[1]), b23 = pk2(d1[2], d1[3]);
            *(GAS u32x2*)(p8 + t * 18432) = sw ? u32x2{b01, b23} : u32x2{a01, a23};
            if (kg == 0) *(GAS u32*)(p4 + t * 18432) = odd ? a23 : b01;
        }
    }
    STAMP(12);
    if (blockIdx.x == 0 && w == 7) {
#pragma nounroll
        for (u32 it = 0; it < SPIN_MAX; ++it) {
            u32x4 a = __builtin_amdgcn_raw_buffer_load_b128(rs, WS_ARR + lane * 16, 0, AUX_SC1);
            CFENCE();
            if (__all(a[0] == tag && a[1] == tag && a[2] == tag && a[3] == tag)) break;
            __builtin_amdgcn_s_sleep(4);
        }
        if (lane == 0) __builtin_amdgcn_raw_buffer_store_b32(epoch + 1, rs, WS_EPOCH, 0, AUX_SC1);
    }
    return false;
}

__global__ __launch_bounds__(512, 2) void k1(Params P) {
    extern __shared__ __attribute__((aligned(16))) char smem[];
    lptr L = (lptr)smem;
    const u32 lbase = (u32)(uintptr_t)L;
    const int tid = threadIdx.x, lane = tid & 63, w = __builtin_amdgcn_readfirstlane(tid >> 6);
    const int bid = blockIdx.x, xcd = bid & 7, slot = bid >> 3;
    const __amdgpu_buffer_rsrc_t rs = __builtin_amdgcn_make_buffer_rsrc((void*)P.ws, 0, (int)WS_END, 0x00020000);
#define SARG
    STAMP(0);
    if (tid < 16) *(LAS u32*)(L + L_FLAG + tid * 4) = 0;
    int cl, ch;
    if (slot >= 8) {
        const int s2 = slot - 8;
        const int rt = (xcd >> 1) * 4 + (s2 & 3);
        if (w < 4) {
            if (s2 < 16) gemm_role<false>(P, rs, L, lbase, w, lane, rt, (xcd & 1) * 4 + (s2 >> 2) SARG);
            else gemm_role<true>(P, rs, L, lbase, w, lane, rt, (xcd & 1) * 2 + ((s2 - 16) >> 2) SARG);
            return;
        }
        RAW_BARRIER();
        cl = (w - 4) >> 1;
        ch = 192 + (s2 * 8 + xcd) * 2 + cl;
    } else {
        if (!tr_role(P, rs, L, lbase, w, lane, xcd * 8 + slot SARG)) return;
        cl = w >> 1;
        ch = (xcd * 8 + slot) * 3 + cl;
    }
    ssm_pair(P, rs, L, L + L_CH + cl * CH_BYTES, w, ch, lane, slot >= 8 && w == 4 SARG);
}

constexpr u32 L2_A = 0;
constexpr u32 L2_B = 92160;
constexpr u32 L2_O = L2_B + 10240;
constexpr u32 LDS2_BYTES = L2_O + 80 * 144;

__global__ __launch_bounds__(256, 1) void k2(char* ws, u16* out) {
    extern __shared__ __attribute__((aligned(16))) char smem[];
    lptr L = (lptr)smem;
    const u32 lbase = (u32)(uintptr_t)L;
    const int tid = threadIdx.x, lane = tid & 63, w = __builtin_amdgcn_readfirstlane(tid >> 6);
    const int idx = (blockIdx.x & 7) * 30 + (blockIdx.x >> 3);
    if (idx >= 234) return;
    const int rtile = idx / 9, cb = idx - rtile * 9;
    const int m = lane & 15, kg = lane >> 4;
    const u32 lo = lane * 16;
    const char* mf = ws + WS_MF + (cb * 4 + w) * 18432;
    u32x4 B[18];
    auto ldB = [&](int ks) { asm volatile("global_load_dwordx4 %0, %1, %2" : "=v"(B[ks]) : "v"(lo), "s"(mf + ks * 1024) : "memory"); };
    auto ldA = [&](int j) {
        const int i = w + 4 * j, ks = i / 5, rb = min(rtile * 5 + (i - ks * 5), 127);
        glds16(lo, ws + WS_PQ + (rb * 18 + ks) * 1024, lbase + L2_A + i * 1024);
    };
#pragma unroll
    for (int gq = 0; gq < 3; ++gq) {
#pragma unroll
        for (int j = 0; j < 8; ++j) {
            if (gq < 2 || j < 6) ldA(gq * 8 + j);
            else if (j == 6 && w < 2) ldA(22);
            if (j < 6) ldB(gq * 6 + j);
        }
    }
    {
        const char* bt = ws + WS_BT + cb * 64 * 4096 + rtile * 160;
#pragma unroll
        for (int i = 0; i < 3; ++i) {
            int ins = w + 4 * i;
            if (ins < 10) {
                int p = ins * 64 + lane, c = p / 10, e = p - c * 10;
                glds16((u32)(c * 4096 + e * 16), bt, lbase + L2_B + ins * 1024);
            }
        }
    }
    float gv[5];
#pragma unroll
    for (int r = 0; r < 5; ++r) {
        u32 o = (u32)min(rtile * 80 + r * 16 + m, 2047) * 4;
        asm volatile("global_load_dword %0, %1, %2" : "=v"(gv[r]) : "v"(o), "s"(ws + WS_G) : "memory");
    }
    f32x4 acc[5];
#pragma unroll
    for (int r = 0; r < 5; ++r) acc[r] = f32x4{0, 0, 0, 0};
    lptr A = L + L2_A + lo;
#pragma unroll
    for (int gq = 0; gq < 3; ++gq) {
        if (gq == 0) WAIT_V(33); else if (gq == 1) WAIT_V(19); else WAIT_V(7);
        asm volatile("" : "+v"(B[gq * 6]), "+v"(B[gq * 6 + 1]), "+v"(B[gq * 6 + 2]), "+v"(B[gq * 6 + 3]), "+v"(B[gq * 6 + 4]), "+v"(B[gq * 6 + 5])::"memory");
        RAW_BARRIER();
#pragma unroll
        for (int k = 0; k < 6; ++k) {
            const int ks = gq * 6 + k;
#pragma unroll
            for (int r = 0; r < 5; ++r) acc[r] = mfma32(B[ks], *(LAS u32x4*)(A + (ks * 5 + r) * 1024), acc[r]);
        }
    }
    WAIT_V(0);
    asm volatile("" : "+v"(gv[0]), "+v"(gv[1]), "+v"(gv[2]), "+v"(gv[3]), "+v"(gv[4])::"memory");
    RAW_BARRIER();
    {
        lptr BT = L + L2_B + (w * 16 + 4 * kg + ((lane >> 2) & 3)) * 160 + (lane & 3) * 8;
        lptr O = L + L2_O + m * 144 + w * 32 + kg * 8;
#pragma unroll
        for (int r = 0; r < 5; ++r) {
            u32x2 b = trread(BT + r * 32);
            float g = gv[r], g1 = 1.0f - g;
            *(LAS u32x2*)(O + r * 16 * 144) = u32x2{pk2(g * acc[r][0] + g1 * lo2f(b[0]), g * acc[r][1] + g1 * hi2f(b[0])),
                                                    pk2(g * acc[r][2] + g1 * lo2f(b[1]), g * acc[r][3] + g1 * hi2f(b[1]))};
        }
    }
    RAW_BARRIER();
#pragma unroll
    for (int i = 0; i < 3; ++i) {
        const int p = i * 256 + tid, r = p >> 3;
        if (p < 640 && rtile * 80 + r < 2048)
            *(GAS u32x4*)((GAS char*)out + (size_t)(rtile * 80 + r) * 1152 + cb * 128 + (p & 7) * 16) = *(LAS u32x4*)(L + L2_O + r * 144 + (p & 7) * 16);
    }
}

extern "C" void kernel_launch(void* const* d_in, const int* in_sizes, int n_in, void* d_out, int out_size, void* d_ws,
                              size_t ws_size, hipStream_t stream) {
    Params P;
    P.x = (const u16*)d_in[0];
    P.wqkv = (const u16*)d_in[1];
    P.wo = (const u16*)d_in[2];
    P.Are = (const u16*)d_in[4];
    P.Aim = (const u16*)d_in[5];
    P.Bre = (const u16*)d_in[6];
    P.Bim = (const u16*)d_in[7];
    P.Cre = (const u16*)d_in[8];
    P.Cim = (const u16*)d_in[9];
    P.logdt = (const u16*)d_in[10];
    P.Dskip = (const u16*)d_in[11];
    P.gw = (const u16*)d_in[12];
    P.ws = (char*)d_ws;
    (void)hipFuncSetAttribute((const void*)k1, hipFuncAttributeMaxDynamicSharedMemorySize, LDS1_BYTES);
    (void)hipFuncSetAttribute((const void*)k2, hipFuncAttributeMaxDynamicSharedMemorySize, LDS2_BYTES);
    k1<<<256, 512, LDS1_BYTES, stream>>>(P);
    k2<<<240, 256, LDS2_BYTES, stream>>>((char*)d_ws, (u16*)d_out);
}
```

```cpp
#include <hip/hip_runtime.h>
#include <hip/hip_bf16.h>
#include <stdint.h>
#define STAMPS 1
#define DIAG 0

typedef unsigned short u16;
typedef unsigned int u32;
typedef unsigned long long u64;
typedef u32 u32x4 __attribute__((ext_vector_type(4)));
typedef u32 u32x2 __attribute__((ext_vector_type(2)));
typedef float f32x4 __attribute__((ext_vector_type(4)));
typedef float f32x2 __attribute__((ext_vector_type(2)));
typedef __bf16 bf16x8 __attribute__((ext_vector_type(8)));
typedef __bf16 bf16x2 __attribute__((ext_vector_type(2)));
typedef short s16x4 __attribute__((ext_vector_type(4)));
#define LAS __attribute__((address_space(3)))
#define GAS __attribute__((address_space(1)))
#define DEV __device__ __forceinline__
typedef LAS char* lptr;

constexpr u32 WS_EPOCH = 0;
constexpr u32 WS_ARR = 4096;
constexpr u32 WS_XFLAG = 8192;
constexpr u32 WS_G = 16384;
constexpr u32 WS_ZG = 32768;
constexpr u32 WS_KVG = 131072;
constexpr u32 WS_XT = 1u << 20;
constexpr u32 WS_PQ = 4u << 20;
constexpr u32 WS_BT = 7u << 20;
constexpr u32 WS_MF = 10u << 20;
constexpr u32 WS_DBG = 11u << 20;
constexpr u32 WS_END = 12u << 20;
constexpr int AUX_SC1 = 16;
constexpr u32 SPIN_MAX = 1u << 18;

constexpr u32 L_FLAG = 0;
constexpr u32 CH_KC = 0, CH_KSTRIDE = 4160, CH_XR = 16640, CH_KT = 21248, CH_BYTES = 22560;
constexpr u32 L_CH = 64;
constexpr u32 L_XRING = L_CH + 2 * CH_BYTES;
constexpr u32 L_WRING = L_XRING + 3 * 16384;
constexpr u32 L_GEMM_END = L_WRING + 3 * 18432;
constexpr u32 SLAB_PITCH = 1184;
constexpr u32 L_SLAB = L_CH + 3 * CH_BYTES;
constexpr u32 L_WO = L_SLAB + 37 * 1024;
constexpr u32 L_KVA = L_WO + 2 * 6144;
constexpr u32 L_T_END = L_KVA + 2048;
constexpr u32 LDS1_BYTES = L_GEMM_END > L_T_END ? L_GEMM_END : L_T_END;
static_assert(LDS1_BYTES <= 163840, "lds");
static_assert(L_XRING % 16 == 0 && L_SLAB % 16 == 0, "align");

DEV float bf2f(u16 v) { return __uint_as_float(((u32)v) << 16); }
DEV u32 pk2(float a, float b) {
    f32x2 v = {a, b};
    return __builtin_bit_cast(u32, __builtin_convertvector(v, bf16x2));
}
DEV float lo2f(u32 v) { return __uint_as_float(v << 16); }
DEV float hi2f(u32 v) { return __uint_as_float(v & 0xffff0000u); }
DEV f32x4 mfma32(u32x4 a, u32x4 b, f32x4 c) {
    return __builtin_amdgcn_mfma_f32_16x16x32_bf16(__builtin_bit_cast(bf16x8, a), __builtin_bit_cast(bf16x8, b), c, 0, 0, 0);
}
DEV f32x4 mfma16(u32x2 a, u32x2 b, f32x4 c) {
    return __builtin_amdgcn_mfma_f32_16x16x16bf16_1k(__builtin_bit_cast(s16x4, a), __builtin_bit_cast(s16x4, b), c, 0, 0, 0);
}
DEV void glds16(u32 voff, const void* sbase, u32 ldsaddr) {
    asm volatile("s_mov_b32 m0, %2\n\ts_nop 0\n\tglobal_load_lds_dwordx4 %0, %1" ::"v"(voff), "s"(sbase), "s"(ldsaddr) : "memory");
}
DEV u32x2 trread(lptr p) { return __builtin_bit_cast(u32x2, __builtin_amdgcn_ds_read_tr16_b64_v4i16((LAS s16x4*)p)); }
DEV u32x4 cat(u32x2 a, u32x2 b) { return u32x4{a[0], a[1], b[0], b[1]}; }
#define WAIT_V(n) asm volatile("s_waitcnt vmcnt(%0)" ::"n"(n) : "memory")
#define WAIT_L0() asm volatile("s_waitcnt lgkmcnt(0)" ::: "memory")
#define RAW_BARRIER() asm volatile("s_waitcnt lgkmcnt(0)\n\ts_barrier" ::: "memory")
#define CFENCE() asm volatile("" ::: "memory")
#define SCHED() __builtin_amdgcn_sched_barrier(0)

#define STAMP(i) do { if ((threadIdx.x & 63) == 0) { stamps[(blockIdx.x * 8 + (threadIdx.x >> 6)) * 16 + (i)] = __builtin_amdgcn_s_memrealtime(); stamps[32768 + (blockIdx.x * 8 + (threadIdx.x >> 6)) * 16 + (i)] = __builtin_amdgcn_s_memtime(); } } while (0)
#define STAMP2(i) do { if ((threadIdx.x & 63) == 0) ((u64*)(ws + WS_DBG))[65536 + (blockIdx.x * 4 + (threadIdx.x >> 6)) * 16 + (i)] = __builtin_amdgcn_s_memrealtime(); } while (0)

DEV void flag_set(lptr L, int wave, u32 v) {
    WAIT_L0();
    *(volatile LAS u32*)(L + L_FLAG + wave * 4) = v;
}
DEV void flag_wait1(lptr L, int wave, u32 v) {
#pragma nounroll
    for (u32 it = 0; it < SPIN_MAX * 8; ++it) {
        u32 f = *(volatile LAS u32*)(L + L_FLAG + wave * 4);
        if (__builtin_amdgcn_readfirstlane(f) >= v) break;
        __builtin_amdgcn_s_sleep(1);
    }
    CFENCE();
}
DEV void pair_sync(lptr L, int wave, u32 v) {
    flag_set(L, wave, v);
    flag_wait1(L, wave ^ 1, v);
}
DEV bool quad_ok(u32x4 f, u32 v) {
    u32 m = min(min(f[0], f[1]), min(f[2], f[3]));
    return __builtin_amdgcn_readfirstlane(m) >= v;
}
DEV void quad_wait(lptr L, u32 v) {
#pragma nounroll
    for (u32 it = 0; it < SPIN_MAX * 8; ++it) {
        u32x4 f = *(volatile LAS u32x4*)(L + L_FLAG);
        if (quad_ok(f, v)) break;
        __builtin_amdgcn_s_sleep(1);
    }
    CFENCE();
}

DEV u32 mk_tag(u32 epoch) { return epoch * 0x9E3779B1u + 0x7F4A7C15u; }
DEV void put_epoch(lptr L, u32 epoch) { *(volatile LAS u32x2*)(L + L_FLAG + 32) = u32x2{epoch, 1u}; }
DEV u32 get_epoch(lptr L) {
    u32x2 v;
#pragma nounroll
    for (u32 it = 0; it < SPIN_MAX * 8; ++it) {
        v = *(volatile LAS u32x2*)(L + L_FLAG + 32);
        if (__builtin_amdgcn_readfirstlane(v[1])) break;
        __builtin_amdgcn_s_sleep(1);
    }
    CFENCE();
    return __builtin_amdgcn_readfirstlane(v[0]);
}

struct Params {
    const u16 *x, *wqkv, *wo, *Are, *Aim, *Bre, *Bim, *Cre, *Cim, *logdt, *Dskip, *gw;
    char* ws;
};

DEV float wave_sum(float v) {
    v += __shfl_xor(v, 1); v += __shfl_xor(v, 2); v += __shfl_xor(v, 4);
    v += __shfl_xor(v, 8); v += __shfl_xor(v, 16); v += __shfl_xor(v, 32);
    return v;
}

DEV void ssm_pair(const Params& P, __amdgpu_buffer_rsrc_t rs, lptr L, lptr C, int wave, int ch, int lane, bool pub
                  , u64* stamps
) {
    const int h = wave & 1;
    const int m = lane & 15, kg = lane >> 4;
    {
        const int n = ch * 128 + h * 64 + lane;
        u32 epoch = 0;
        if (pub) epoch = __builtin_amdgcn_raw_buffer_load_b32(rs, WS_EPOCH, 0, AUX_SC1);
        float are = bf2f(P.Are[n]), aim = bf2f(P.Aim[n]), bre = bf2f(P.Bre[n]), bim = bf2f(P.Bim[n]);
        float cre = bf2f(P.Cre[n]), cim = bf2f(P.Cim[n]);
        float dt = lo2f(pk2(expf(bf2f(P.logdt[ch])), 0.f));
        float dsk = bf2f(P.Dskip[ch]);
        float lr = dt * are * 1.44269504088896340736f;
        float t = dt * aim * 0.15915494309189533577f;
        float br = dt * bre, bi = dt * bim;
        float cr = cre * br - cim * bi, ci = cre * bi + cim * br;
        LAS float* T = (LAS float*)(C + CH_KC + h * 2048);
        T[lane] = lr;
        T[64 + lane] = t - rintf(t);
        T[128 + lane] = 2.f * t - rintf(2.f * t);
        T[192 + lane] = 4.f * t - rintf(4.f * t);
        T[256 + lane] = 8.f * t - rintf(8.f * t);
        T[320 + lane] = cr;
        T[384 + lane] = ci;
        float k0 = wave_sum(cr) + (h ? 0.f : dsk);
        if (lane == 0) T[448] = k0;
        if (pub && lane == 0) {
            put_epoch(L, epoch);
            __builtin_amdgcn_raw_buffer_store_b32(mk_tag(epoch), rs, WS_ARR + blockIdx.x * 4, 0, AUX_SC1);
        }
    }
    STAMP(1);
    {
        const float u0 = (float)(m + 1);
        const float u1s = (float)(m < 8 ? 129 + m : 129 + (m & 3) + 64 * ((m >> 2) & 1));
        const float u1 = m < 8 ? 2.f * u1s : 4.f * u1s;
        const float u2s = (float)(129 + (m & 1) + 32 * ((m >> 1) & 3));
        const float u2 = 8.f * u2s;
        const float vv = (float)(16 * m);
        lptr T = C + CH_KC + h * 2048 + kg * 16;
        lptr T1 = T + (m < 8 ? 512 : 768);
        lptr T2 = T + (m >> 3) * 64;
        f32x4 a0 = {0, 0, 0, 0}, a1 = a0, a2a = a0, a2b = a0;
        u32x4 G2;
#pragma unroll
        for (int s = 0; s < 4; ++s) {
            f32x4 lr = *(LAS f32x4*)(T + s * 64), p1 = *(LAS f32x4*)(T + 256 + s * 64), p24 = *(LAS f32x4*)(T1 + s * 64);
            f32x4 cr = *(LAS f32x4*)(T + 1280 + s * 64), ci = *(LAS f32x4*)(T + 1536 + s * 64);
            u32x4 G0, G1, Hh;
#pragma unroll
            for (int i = 0; i < 4; ++i) {
                float e = __builtin_amdgcn_exp2f(lr[i] * u0), an = p1[i] * u0;
                G0[i] = pk2(e * __builtin_amdgcn_cosf(an), e * __builtin_amdgcn_sinf(an));
                e = __builtin_amdgcn_exp2f(lr[i] * u1); an = p24[i] * u1s;
                G1[i] = pk2(e * __builtin_amdgcn_cosf(an), e * __builtin_amdgcn_sinf(an));
                e = __builtin_amdgcn_exp2f(lr[i] * vv); an = p1[i] * vv;
                float wr = e * __builtin_amdgcn_cosf(an), wi = e * __builtin_amdgcn_sinf(an);
                Hh[i] = pk2(cr[i] * wr - ci[i] * wi, -(cr[i] * wi + ci[i] * wr));
            }
            if ((s & 1) == 0) {
                f32x4 l2 = *(LAS f32x4*)(T2 + s * 64), p8 = *(LAS f32x4*)(T2 + 1024 + s * 64);
#pragma unroll
                for (int i = 0; i < 4; ++i) {
                    float e = __builtin_amdgcn_exp2f(l2[i] * u2), an = p8[i] * u2s;
                    G2[i] = pk2(e * __builtin_amdgcn_cosf(an), e * __builtin_amdgcn_sinf(an));
                }
            }
            a0 = mfma32(G0, Hh, a0);
            a1 = mfma32(G1, Hh, a1);
            if ((s & 1) == 0) a2a = mfma32(G2, Hh, a2a); else a2b = mfma32(G2, Hh, a2b);
        }
        lptr Pp = C + CH_KC + 4096 + h * 3072;
        *(LAS f32x4*)(Pp + (16 * m + 4 * kg) * 4) = a0;
        *(LAS f32x4*)(Pp + (kg < 2 ? 256 + 8 * m + 4 * kg : 384 + 64 * (kg - 2) + 4 * m) * 4) = a1;
        f32x4 a2 = kg < 2 ? a2a : a2b;
        lptr P2 = Pp + (512 + (kg >> 1) * 128 + 64 * (kg & 1) + 2 * m) * 4;
        *(LAS f32x2*)(P2) = f32x2{a2[0], a2[1]};
        *(LAS f32x2*)(P2 + 128) = f32x2{a2[2], a2[3]};
    }
    STAMP(2);
    pair_sync(L, wave, 1);
    {
        LAS float* P0 = (LAS float*)(C + CH_KC + 4096) + 320 * h + lane;
        LAS u16* KT = (LAS u16*)(C + CH_KT);
#pragma unroll
        for (int r = 0; r < 5; ++r) {
            float v = P0[64 * r] + P0[768 + 64 * r];
            if (r >= 3 && h) v += P0[64 * r + 128] + P0[768 + 64 * r + 128];
            KT[1 + 320 * h + lane + 64 * r] = (u16)pk2(v, 0.f);
        }
        if (lane == 0) {
            if (h == 0) KT[0] = (u16)pk2(((LAS float*)(C + CH_KC))[448] + ((LAS float*)(C + CH_KC + 2048))[448], 0.f);
            else KT[641] = 0;
        }
    }
    pair_sync(L, wave, 2);
    {
        LAS u16* KT = (LAS u16*)(C + CH_KT);
        const int l0 = (h * 64 + lane) * 16 - 4;
        u32 d[10];
#pragma unroll
        for (int j = 0; j < 10; ++j) {
            int la = l0 + 2 * j, lb = la + 1;
            u32 bits = pk2((float)la, (float)lb);
            int ia = la < 256 ? (la < 0 ? 641 : la) : (int)(bits & 0xffff) - 0x4280;
            int ib = lb < 256 ? (lb < 0 ? 641 : lb) : (int)(bits >> 16) - 0x4280;
            d[j] = (u32)KT[ia] | ((u32)KT[ib] << 16);
        }
        u32 e[9];
#pragma unroll
        for (int j = 0; j < 9; ++j) e[j] = __builtin_amdgcn_alignbit(d[j + 1], d[j], 16);
        lptr W = C + CH_KC + 32 + (h * 64 + lane) * 32;
        *(LAS u32x4*)(W) = u32x4{d[2], d[3], d[4], d[5]};
        *(LAS u32x4*)(W + 16) = u32x4{d[6], d[7], d[8], d[9]};
        *(LAS u32x4*)(W + CH_KSTRIDE) = u32x4{e[1], e[2], e[3], e[4]};
        *(LAS u32x4*)(W + CH_KSTRIDE + 16) = u32x4{e[5], e[6], e[7], e[8]};
        *(LAS u32x4*)(W + 2 * CH_KSTRIDE) = u32x4{d[1], d[2], d[3], d[4]};
        *(LAS u32x4*)(W + 2 * CH_KSTRIDE + 16) = u32x4{d[5], d[6], d[7], d[8]};
        *(LAS u32x4*)(W + 3 * CH_KSTRIDE) = u32x4{e[0], e[1], e[2], e[3]};
        *(LAS u32x4*)(W + 3 * CH_KSTRIDE + 16) = u32x4{e[4], e[5], e[6], e[7]};
        if (h == 0) {
            if (lane < 8) *(LAS u32x4*)(C + CH_KC + (lane >> 1) * CH_KSTRIDE + (lane & 1) * 16) = u32x4{0, 0, 0, 0};
        } else {
            if (lane < 32) *(LAS u32x4*)(C + CH_XR + 4096 + lane * 16) = u32x4{0, 0, 0, 0};
        }
    }
    STAMP(3);
    {
        const u32 tag = mk_tag(get_epoch(L));
#pragma nounroll
        for (u32 it = 0; it < SPIN_MAX; ++it) {
            u32 f = __builtin_amdgcn_raw_buffer_load_b32(rs, WS_XFLAG + (h * 32 + (lane & 31)) * 4, 0, AUX_SC1);
            CFENCE();
            if (__all(f == tag)) break;
            __builtin_amdgcn_s_sleep(4);
        }
        CFENCE();
        const int sl = h * 32 + (lane >> 2);
        u32 off = WS_XT + sl * 36864 + ch * 64 + (lane & 3) * 16;
        u32x4 v0 = __builtin_amdgcn_raw_buffer_load_b128(rs, off, 0, AUX_SC1);
        u32x4 v1 = __builtin_amdgcn_raw_buffer_load_b128(rs, off + 16 * 36864, 0, AUX_SC1);
        lptr X = C + CH_XR + 4032 - 64 * sl + (lane & 3) * 16;
        *(LAS u32x4*)(X) = v0;
        *(LAS u32x4*)(X - 1024) = v1;
    }
    STAMP(4);
    pair_sync(L, wave, 3);
    f32x4 acc[8];
#pragma unroll
    for (int i = 0; i < 8; ++i) acc[i] = f32x4{0, 0, 0, 0};
    {
        lptr KA = C + CH_KC + ((~m) & 3) * CH_KSTRIDE + 2 * (8 * kg + ((m + 4) & ~3)) + 256 * h;
        lptr XB = C + CH_XR + 2 * (240 - 16 * m + 8 * kg) + 256 * h;
#pragma unroll
        for (int j = 0; j < 4; ++j) {
            u32x4 A[8], B[8];
#pragma unroll
            for (int a = 0; a < 8; ++a) {
                A[a] = cat(*(volatile LAS u32x2*)(KA + 64 * j + 512 * a), *(volatile LAS u32x2*)(KA + 64 * j + 512 * a + 8));
                B[a] = *(LAS u32x4*)(XB + 64 * j + 512 * (7 - a));
            }
#pragma unroll
            for (int I = 0; I < 8; ++I)
#pragma unroll
                for (int a = 0; a <= I; ++a) acc[I] = mfma32(A[a], B[I - a], acc[I]);
        }
    }
    STAMP(5);
    pair_sync(L, wave, 4);
    {
        lptr E = C + CH_KC + lane * 16;
        f32x4 kp[4];
        if (h) {
#pragma unroll
            for (int i = 0; i < 4; ++i) { *(LAS f32x4*)(E + 4096 + i * 1024) = acc[i]; kp[i] = acc[4 + i]; }
        } else {
#pragma unroll
            for (int i = 0; i < 4; ++i) { *(LAS f32x4*)(E + i * 1024) = acc[4 + i]; kp[i] = acc[i]; }
        }
        pair_sync(L, wave, 5);
        GAS char* bt = (GAS char*)P.ws + WS_BT + ch * 4096 + h * 2048 + m * 32 + kg * 8;
#pragma unroll
        for (int i = 0; i < 4; ++i) {
            f32x4 o = *(LAS f32x4*)(E + (h ^ 1) * 4096 + i * 1024);
            o += kp[i];
            *(GAS u32x2*)(bt + i * 512) = u32x2{pk2(o[0], o[1]), pk2(o[2], o[3])};
        }
    }
    STAMP(6);
}

template <bool ISQ>
DEV void gemm_role(const Params& P, __amdgpu_buffer_rsrc_t rs, lptr L, u32 lbase, int w, int lane, int rt, int grp
                   , u64* stamps
) {
    const int m = lane & 15, kg = lane >> 4;
    u32 xo[4], wo_[5];
#pragma unroll
    for (int i = 0; i < 4; ++i) {
        int r = 8 * i + (lane >> 3);
        xo[i] = (u32)((rt * 128 + w * 32 + r) * 1152 + (((lane & 7) ^ (r & 7)) << 4));
    }
#pragma unroll
    for (int i = 0; i < 5; ++i) {
        int ins = i < 4 ? w + 4 * i : 16 + (w & 1);
        int p = ins * 64 + lane, rho = p / 18, e = p - rho * 18;
        int k = (rho & 32) + 8 * ((rho & 15) >> 2) + 4 * ((rho >> 4) & 1) + (rho & 3);
        int cb = ISQ ? 288 * grp + 16 * e : (e < 9 ? 1152 + 144 * grp + 16 * e : 2304 + 144 * grp + 16 * (e - 9));
        wo_[i] = (u32)(k * 3456 + cb);
    }
    const char* xg = (const char*)P.x;
    const char* wg = (const char*)P.wqkv;
    auto issue = [&](int c) {
        const int sl = c % 3;
        const char* xs = xg + c * 128;
        const char* wsrc = wg + c * 64 * 3456;
        if ((w >> 1) == (c & 1)) glds16(wo_[4], wsrc, lbase + L_WRING + sl * 18432 + (16 + (w & 1)) * 1024);
#pragma unroll
        for (int i = 0; i < 4; ++i) {
            glds16(xo[i], xs, lbase + L_XRING + sl * 16384 + w * 4096 + i * 1024);
            glds16(wo_[i], wsrc, lbase + L_WRING + sl * 18432 + (w + 4 * i) * 1024);
        }
    };
    issue(0); issue(1); issue(2);
    RAW_BARRIER();
    __builtin_amdgcn_s_setprio(2);
    f32x4 acc[2][9];
#pragma unroll
    for (int ct = 0; ct < 9; ++ct) {
        float iv = ISQ ? 1e-6f : (ct < 4 ? 1e-6f : (ct == 4 ? (m < 8 ? 1e-6f : 0.f) : 0.f));
        acc[0][ct] = f32x4{iv, iv, iv, iv};
        acc[1][ct] = acc[0][ct];
    }
    lptr XA = L + L_XRING + w * 4096 + m * 128;
    const int xs0 = ((kg ^ (m & 7)) << 4), xs1 = (((4 + kg) ^ (m & 7)) << 4);
    lptr WB = L + L_WRING + (4 * kg + ((lane >> 2) & 3)) * 288 + (lane & 3) * 8;
    u32x4 A0[2], B0[9], A1[2], B1[9];
    auto rd = [&](int c, int ks, u32x4(&A)[2], u32x4(&B)[9]) {
        const int sl = c % 3;
        A[0] = *(LAS u32x4*)(XA + sl * 16384 + (ks ? xs1 : xs0));
        A[1] = *(LAS u32x4*)(XA + sl * 16384 + 2048 + (ks ? xs1 : xs0));
#pragma unroll
        for (int ct = 0; ct < 9; ++ct)
            B[ct] = cat(trread(WB + sl * 18432 + ks * 9216 + ct * 32), trread(WB + sl * 18432 + ks * 9216 + 4608 + ct * 32));
    };
    auto mm = [&](u32x4(&A)[2], u32x4(&B)[9], int c0, int c1) {
#pragma unroll
        for (int ct = c0; ct < c1; ++ct)
#pragma unroll
            for (int r = 0; r < 2; ++r) acc[r][ct] = ISQ ? mfma32(B[ct], A[r], acc[r][ct]) : mfma32(A[r], B[ct], acc[r][ct]);
    };
    WAIT_V(16);
    flag_set(L, w, 1);
    quad_wait(L, 1);
    STAMP(1);
    rd(0, 0, A0, B0);
#pragma unroll
    for (int c = 0; c < 9; ++c) {
        rd(c, 1, A1, B1);
        SCHED();
        mm(A0, B0, 0, 9);
        SCHED();
        if (c < 8) {
            if (c < 7) WAIT_V(8); else WAIT_V(0);
            flag_set(L, w, c + 2);
            SCHED();
            mm(A1, B1, 0, 3);
            SCHED();
            u32x4 f = *(volatile LAS u32x4*)(L + L_FLAG);
            SCHED();
            mm(A1, B1, 3, 6);
            SCHED();
            if (!quad_ok(f, c + 2)) quad_wait(L, c + 2);
            CFENCE();
            rd(c + 1, 0, A0, B0);
            if (c + 3 < 9) issue(c + 3);
            SCHED();
            mm(A1, B1, 6, 9);
            SCHED();
        } else {
            mm(A1, B1, 0, 9);
        }
    }
    STAMP(2);
    const u32 tag = mk_tag(get_epoch(L));
    constexpr float LOG2E = 1.44269504088896340736f;
    auto phi = [&](float t) {
        float e = __builtin_amdgcn_fmed3f(__builtin_amdgcn_exp2f(fmaf(t, LOG2E, -1e-6f * LOG2E)), 0.f, 1.f);
        return fmaxf(t, 1e-6f) + e;
    };
    if (!ISQ) {
        lptr EX = L + L_WRING + lane * 16;
#pragma unroll
        for (int ct = 0; ct < 9; ++ct) {
            f32x4 v[2];
#pragma unroll
            for (int r = 0; r < 2; ++r)
#pragma unroll
                for (int i = 0; i < 4; ++i) {
                    float t = acc[r][ct][i];
                    v[r][i] = ct < 4 ? phi(t) : (ct == 4 ? (m < 8 ? phi(t) : t) : t);
                }
            *(LAS u32x4*)(EX + (w * 9 + ct) * 1024) = u32x4{pk2(v[0][0], v[0][1]), pk2(v[0][2], v[0][3]), pk2(v[1][0], v[1][1]), pk2(v[1][2], v[1][3])};
        }
        flag_set(L, w, 10);
        quad_wait(L, 10);
        STAMP(3);
        f32x4 kv[2][2], zz[2];
        kv[0][0] = kv[0][1] = kv[1][0] = kv[1][1] = zz[0] = zz[1] = f32x4{0, 0, 0, 0};
        const u32x4 ones = {0x3f803f80u, 0x3f803f80u, 0x3f803f80u, 0x3f803f80u};
        lptr EK = EX + w * 1024;
#pragma unroll
        for (int s = 0; s < 4; ++s) {
            u32x4 k0 = *(LAS u32x4*)(EK + s * 9216), k1 = *(LAS u32x4*)(EK + s * 9216 + 1024);
            u32x4 v0 = *(LAS u32x4*)(EK + s * 9216 + 4096), v1 = *(LAS u32x4*)(EK + s * 9216 + 5120);
            zz[0] = mfma32(k0, ones, zz[0]);
            zz[1] = mfma32(k1, ones, zz[1]);
            kv[0][0] = mfma32(v0, k0, kv[0][0]);
            kv[0][1] = mfma32(v0, k1, kv[0][1]);
            kv[1][0] = mfma32(v1, k0, kv[1][0]);
            kv[1][1] = mfma32(v1, k1, kv[1][1]);
        }
        {
            f32x4 z = m == 0 ? zz[0] : zz[1];
            bool on = m == 0 || (m == 1 && w == 3 && kg < 2);
            u32 off = WS_ZG + (rt * 144 + grp * 18 + 4 * w + 4 * m + kg) * 16;
            if (on) __builtin_amdgcn_raw_buffer_store_b128(u32x4{pk2(z[0], z[1]), tag, pk2(z[2], z[3]), tag}, rs, off, 0, AUX_SC1);
        }
        const int hh = grp * 4 + w;
#pragma unroll
        for (int et = 0; et < 2; ++et)
#pragma unroll
            for (int dt = 0; dt < 2; ++dt) {
                int d = 16 * dt + m - 2 * w, eg = 4 * et + kg - 2 - (w >> 1);
                u32 p01 = pk2(kv[et][dt][0], kv[et][dt][1]), p23 = pk2(kv[et][dt][2], kv[et][dt][3]);
                if ((w & 1) && eg == 0) p01 = 0;
                if (!(w & 1) && eg == 4) p23 = 0;
                u32 off = WS_KVG + ((hh * 16 + rt) * 96 + eg * 18 + d) * 16;
                if (d >= 0 && d < 18 && eg >= 0 && eg < 5)
                    __builtin_amdgcn_raw_buffer_store_b128(u32x4{p01, tag, p23, tag}, rs, off, 0, AUX_SC1);
            }
        STAMP(4);
    } else {
        lptr ZI = L + L_WRING + w * 2560;
        lptr ZP = L + L_WRING + 12288;
        *(LAS u32x4*)(ZI + lane * 16) = u32x4{0, 0, 0, 0};
        *(LAS u32x4*)(ZI + 1024 + lane * 16) = u32x4{0, 0, 0, 0};
        if (lane < 32) *(LAS u32x4*)(ZI + 2048 + lane * 16) = u32x4{0, 0, 0, 0};
        u32x2 pb[2][9];
#pragma unroll
        for (int ct = 0; ct < 9; ++ct)
#pragma unroll
            for (int r = 0; r < 2; ++r) {
#pragma unroll
                for (int i = 0; i < 4; ++i) acc[r][ct][i] = phi(acc[r][ct][i]);
                pb[r][ct] = u32x2{pk2(acc[r][ct][0], acc[r][ct][1]), pk2(acc[r][ct][2], acc[r][ct][3])};
            }
        u32x2 E[9];
#pragma unroll
        for (int ct = 0; ct < 9; ++ct) {
            int e = (16 * ct + m) / 18 - 4 * (kg & 1);
            u64 v = (e >= 0 && e < 4) ? (0x3f80ull << (16 * e)) : 0ull;
            E[ct] = u32x2{(u32)v, (u32)(v >> 32)};
        }
        STAMP(3);
        {
            const int ql = lane < 36 ? lane : 35;
            u32 off = WS_ZG + (4 * w * 144 + grp * 36 + ql) * 16;
            u32x4 g[4];
#pragma nounroll
            for (u32 it = 0; it < SPIN_MAX; ++it) {
                bool ok = true;
#pragma unroll
                for (int r = 0; r < 4; ++r) g[r] = __builtin_amdgcn_raw_buffer_load_b128(rs, off + r * 2304, 0, AUX_SC1);
                CFENCE();
#pragma unroll
                for (int r = 0; r < 4; ++r) ok = ok && g[r][1] == tag && g[r][3] == tag;
                if (__all(ok)) break;
                __builtin_amdgcn_s_sleep(2);
            }
            f32x4 s = {0, 0, 0, 0};
#pragma unroll
            for (int r = 0; r < 4; ++r) s += f32x4{lo2f(g[r][0]), hi2f(g[r][0]), lo2f(g[r][2]), hi2f(g[r][2])};
            *(LAS f32x4*)(ZP + w * 1024 + lane * 16) = s;
        }
        flag_set(L, w, 10);
        quad_wait(L, 10);
        STAMP(4);
        {
            f32x4 s = *(LAS f32x4*)(ZP + lane * 16);
            s += *(LAS f32x4*)(ZP + 1024 + lane * 16);
            s += *(LAS f32x4*)(ZP + 2048 + lane * 16);
            s += *(LAS f32x4*)(ZP + 3072 + lane * 16);
            if (lane < 36) {
                int ct = lane >> 2;
                lptr q = ZI + ((ct >> 1) * 4 + (lane & 3)) * 128 + (ct & 1) * 8;
                *(LAS u32*)(q + ((4 * lane) / 18) * 16) = pk2(s[0], s[1]);
                *(LAS u32*)(q + ((4 * lane + 2) / 18) * 16 + 4) = pk2(s[2], s[3]);
            }
        }
        WAIT_L0();
        u32x4 Z[5];
#pragma unroll
        for (int c = 0; c < 5; ++c) Z[c] = *(LAS u32x4*)(ZI + (c * 4 + kg) * 128 + (m & 7) * 16);
        f32x4 den[2];
#pragma unroll
        for (int r = 0; r < 2; ++r) den[r] = f32x4{1e-6f, 1e-6f, 1e-6f, 1e-6f};
#pragma unroll
        for (int c = 0; c < 5; ++c)
#pragma unroll
            for (int r = 0; r < 2; ++r) den[r] = mfma32(Z[c], cat(pb[r][2 * c], c < 4 ? pb[r][2 * c + 1] : u32x2{0, 0}), den[r]);
        GAS char* pq = (GAS char*)P.ws + WS_PQ + (size_t)((rt * 8 + w * 2) * 18 + ((9 * grp) >> 1)) * 1024 + lane * 16;
#pragma unroll
        for (int r = 0; r < 2; ++r) {
            float rc[4];
#pragma unroll
            for (int i = 0; i < 4; ++i) rc[i] = __builtin_amdgcn_rcpf(den[r][i]);
            u32x2 hi = {pk2(rc[0], rc[1]), pk2(rc[2], rc[3])};
            u32x2 lo = {pk2(rc[0] - lo2f(hi[0]), rc[1] - hi2f(hi[0])), pk2(rc[2] - lo2f(hi[1]), rc[3] - hi2f(hi[1]))};
            u32x2 rb = kg < 2 ? hi : lo;
            u32x2 o[9];
#pragma unroll
            for (int ct = 0; ct < 9; ++ct) {
                f32x4 R = mfma16(E[ct], rb, f32x4{0, 0, 0, 0});
                R *= acc[r][ct];
                o[ct] = u32x2{pk2(R[0], R[1]), pk2(R[2], R[3])};
            }
            GAS char* q = pq + r * 18 * 1024;
            if ((grp & 1) == 0) {
#pragma unroll
                for (int c = 0; c < 4; ++c) *(GAS u32x4*)(q + c * 1024) = cat(o[2 * c], o[2 * c + 1]);
                *(GAS u32x2*)(q + 4096) = o[8];
            } else {
                *(GAS u32x2*)(q + 8) = o[0];
#pragma unroll
                for (int c = 0; c < 4; ++c) *(GAS u32x4*)(q + (c + 1) * 1024) = cat(o[2 * c + 1], o[2 * c + 2]);
            }
        }
        STAMP(5);
    }
}

DEV bool tr_role(const Params& P, __amdgpu_buffer_rsrc_t rs, lptr L, u32 lbase, int w, int lane, int ti
                 , u64* stamps
) {
    const int tid = w * 64 + lane;
    u32 epoch = 0;
    if (w == 0) epoch = __builtin_amdgcn_raw_buffer_load_b32(rs, WS_EPOCH, 0, AUX_SC1);
    {
        const char* xs = (const char*)P.x + (size_t)ti * 32 * 1152;
#pragma unroll
        for (int i = 0; i < 5; ++i) {
            int ins = w + 8 * i;
            if (ins < 37) {
                int p = ins * 64 + lane, r = p / 74, e = p - r * 74;
                glds16((u32)(r * 1152 + min(e, 71) * 16), xs, lbase + L_SLAB + ins * 1024);
            }
        }
    }
    const int hh = ti >> 1, R0 = 18 * hh - 2 * (hh & 1);
    if (w >= 6) {
        const char* wsrc = (const char*)P.wo + R0 * 1152 + (ti & 1) * 576 + (w - 6) * 288;
#pragma unroll
        for (int i = 0; i < 6; ++i) {
            int p = min(i * 64 + lane, 359), r = p / 18, e = p - r * 18;
            glds16((u32)(r * 1152 + e * 16), wsrc, lbase + L_WO + (w - 6) * 6144 + i * 1024);
        }
        WAIT_V(6);
    } else {
        WAIT_V(0);
    }
    RAW_BARRIER();
    STAMP(8);
    if (tid == 0) put_epoch(L, epoch);
    {
        const int G = lane >> 4;
        lptr S1 = L + L_SLAB + (31 - 8 * G - ((lane >> 2) & 3)) * SLAB_PITCH + (lane & 3) * 8 + w * 32;
        u32 off = WS_XT + ti * 36864 + (w * 16 + (lane & 15)) * 64 + G * 16;
#pragma unroll
        for (int i = 0; i < 5; ++i) {
            if (w + 8 * i < 36) {
                u32x4 v = cat(trread(S1 + i * 256), trread(S1 + i * 256 - 4 * SLAB_PITCH));
                __builtin_amdgcn_raw_buffer_store_b128(v, rs, off + i * 8192, 0, AUX_SC1);
            }
        }
        WAIT_V(0);
        RAW_BARRIER();
        if (tid == 0) {
            __builtin_amdgcn_raw_buffer_store_b32(mk_tag(epoch), rs, WS_XFLAG + ti * 4, 0, AUX_SC1);
            __builtin_amdgcn_raw_buffer_store_b32(mk_tag(epoch), rs, WS_ARR + blockIdx.x * 4, 0, AUX_SC1);
        }
    }
    STAMP(9);
    if (w < 6) return true;
    epoch = get_epoch(L);
    const u32 tag = mk_tag(epoch);
    const int mw = w - 6, m = lane & 15, kg = lane >> 4;
    *(LAS u32x4*)(L + L_KVA + mw * 1024 + lane * 16) = u32x4{0, 0, 0, 0};
    {
        u32x4 gwv[5];
#pragma unroll
        for (int i = 0; i < 5; ++i) gwv[i] = (i < 4 || m < 8) ? *(const GAS u32x4*)((const GAS char*)P.gw + (m + 16 * i) * 16) : u32x4{0, 0, 0, 0};
#pragma unroll
        for (int ps = 0; ps < 4; ++ps) {
            const int r = mw * 16 + ps * 4 + kg;
            float s = 0.f;
#pragma unroll
            for (int i = 0; i < 5; ++i) {
                u32x4 xv = *(LAS u32x4*)(L + L_SLAB + r * SLAB_PITCH + (i < 4 ? m + 16 * i : 64 + (m & 7)) * 16);
#pragma unroll
                for (int j = 0; j < 4; ++j) s += lo2f(xv[j]) * lo2f(gwv[i][j]) + hi2f(xv[j]) * hi2f(gwv[i][j]);
            }
            s += __shfl_xor(s, 1); s += __shfl_xor(s, 2); s += __shfl_xor(s, 4); s += __shfl_xor(s, 8);
            float g = 1.0f / (1.0f + __expf(-s));
            if (m == 0) *(GAS float*)((GAS char*)P.ws + WS_G + (ti * 32 + r) * 4) = g;
        }
    }
    u32x4 Bw[9];
    {
        lptr WB = L + L_WO + mw * 6144 + (lane & 3) * 8;
        const int q = (lane >> 2) & 3;
#pragma unroll
        for (int t = 0; t < 9; ++t) Bw[t] = cat(trread(WB + (4 * kg + q) * 288 + t * 32), trread(WB + (16 + q) * 288 + t * 32));
    }
    pair_sync(L, w, 1);
    __builtin_amdgcn_s_setprio(3);
    STAMP(10);
    {
        const int u = mw * 45 + min(lane, 44);
        u32 off = WS_KVG + (hh * 16 * 96 + u) * 16;
        u32x4 g[16];
#pragma nounroll
        for (u32 it = 0; it < SPIN_MAX; ++it) {
            u32x4 pr = __builtin_amdgcn_raw_buffer_load_b128(rs, off + 15 * 1536, 0, AUX_SC1);
            CFENCE();
            if (__all(pr[1] == tag)) break;
            __builtin_amdgcn_s_sleep(4);
        }
#pragma nounroll
        for (u32 it = 0; it < SPIN_MAX; ++it) {
            bool ok = true;
#pragma unroll
            for (int r = 0; r < 16; ++r) g[r] = __builtin_amdgcn_raw_buffer_load_b128(rs, off + r * 1536, 0, AUX_SC1);
            CFENCE();
#pragma unroll
            for (int r = 0; r < 16; ++r) ok = ok && g[r][1] == tag && g[r][3] == tag;
            if (__all(ok)) break;
            __builtin_amdgcn_s_sleep(2);
        }
        f32x4 s = {0, 0, 0, 0};
#pragma unroll
        for (int r = 0; r < 16; ++r) s += f32x4{lo2f(g[r][0]), hi2f(g[r][0]), lo2f(g[r][2]), hi2f(g[r][2])};
        const int eg = u / 18, d = u - eg * 18, mm = d + 2 * (hh & 1);
        if (lane < 45) *(LAS u32x2*)(L + L_KVA + (mm >> 4) * 1024 + ((eg & 3) * 16 + (mm & 15)) * 16 + (eg >> 2) * 8) = u32x2{pk2(s[0], s[1]), pk2(s[2], s[3])};
    }
    STAMP(11);
    pair_sync(L, w, 2);
    {
        u32x4 A0 = *(LAS u32x4*)(L + L_KVA + lane * 16), A1 = *(LAS u32x4*)(L + L_KVA + 1024 + lane * 16);
        const bool odd = hh & 1;
        const int U0 = R0 >> 2;
        const bool sw = odd && kg == 0;
        const int U8 = U0 + (sw ? 4 : kg), U4 = U0 + (odd ? 0 : 4);
        const int T0 = (ti & 1) * 18 + mw * 9;
        GAS char* mf = (GAS char*)P.ws + WS_MF + T0 * 18 * 1024 + m * 16;
        GAS char* p8 = mf + (U8 >> 3) * 1024 + (U8 & 3) * 256 + ((U8 >> 2) & 1) * 8;
        GAS char* p4 = mf + (U4 >> 3) * 1024 + (U4 & 3) * 256 + ((U4 >> 2) & 1) * 8 + (odd ? 4 : 0);
#pragma unroll
        for (int t = 0; t < 9; ++t) {
            f32x4 d0 = mfma32(A0, Bw[t], f32x4{0, 0, 0, 0}), d1 = mfma32(A1, Bw[t], f32x4{0, 0, 0, 0});
            u32 a01 = pk2(d0[0], d0[1]), a23 = pk2(d0[2], d0[3]), b01 = pk2(d1[0], d1[1]), b23 = pk2(d1[2], d1[3]);
            *(GAS u32x2*)(p8 + t * 18432) = sw ? u32x2{b01, b23} : u32x2{a01, a23};
            if (kg == 0) *(GAS u32*)(p4 + t * 18432) = odd ? a23 : b01;
        }
    }
    STAMP(12);
    if (blockIdx.x == 0 && w == 7) {
#pragma nounroll
        for (u32 it = 0; it < SPIN_MAX; ++it) {
            u32x4 a = __builtin_amdgcn_raw_buffer_load_b128(rs, WS_ARR + lane * 16, 0, AUX_SC1);
            CFENCE();
            if (__all(a[0] == tag && a[1] == tag && a[2] == tag && a[3] == tag)) break;
            __builtin_amdgcn_s_sleep(4);
        }
        if (lane == 0) __builtin_amdgcn_raw_buffer_store_b32(epoch + 1, rs, WS_EPOCH, 0, AUX_SC1);
    }
    return false;
}

__global__ __launch_bounds__(512, 2) void k1(Params P) {
    extern __shared__ __attribute__((aligned(16))) char smem[];
    lptr L = (lptr)smem;
    const u32 lbase = (u32)(uintptr_t)L;
    const int tid = threadIdx.x, lane = tid & 63, w = __builtin_amdgcn_readfirstlane(tid >> 6);
    const int bid = blockIdx.x, xcd = bid & 7, slot = bid >> 3;
    const __amdgpu_buffer_rsrc_t rs = __builtin_amdgcn_make_buffer_rsrc((void*)P.ws, 0, (int)WS_END, 0x00020000);
    u64* stamps = (u64*)(P.ws + WS_DBG);
#define SARG , stamps
    STAMP(0);
    if (tid < 16) *(LAS u32*)(L + L_FLAG + tid * 4) = 0;
    int cl, ch;
    if (slot >= 8) {
        const int s2 = slot - 8;
        const int rt = (xcd >> 1) * 4 + (s2 & 3);
        if (w < 4) {
            if (s2 < 16) gemm_role<false>(P, rs, L, lbase, w, lane, rt, (xcd & 1) * 4 + (s2 >> 2) SARG);
            else gemm_role<true>(P, rs, L, lbase, w, lane, rt, (xcd & 1) * 2 + ((s2 - 16) >> 2) SARG);
            return;
        }
        RAW_BARRIER();
        cl = (w - 4) >> 1;
        ch = 192 + (s2 * 8 + xcd) * 2 + cl;
    } else {
        if (!tr_role(P, rs, L, lbase, w, lane, xcd * 8 + slot SARG)) return;
        cl = w >> 1;
        ch = (xcd * 8 + slot) * 3 + cl;
    }
    ssm_pair(P, rs, L, L + L_CH + cl * CH_BYTES, w, ch, lane, slot >= 8 && w == 4 SARG);
}

constexpr u32 L2_A = 0;
constexpr u32 L2_B = 92160;
constexpr u32 L2_O = L2_B + 10240;
constexpr u32 LDS2_BYTES = L2_O + 80 * 144;

__global__ __launch_bounds__(256, 1) void k2(char* ws, u16* out) {
    extern __shared__ __attribute__((aligned(16))) char smem[];
    lptr L = (lptr)smem;
    const u32 lbase = (u32)(uintptr_t)L;
    const int tid = threadIdx.x, lane = tid & 63, w = __builtin_amdgcn_readfirstlane(tid >> 6);
    const int idx = (blockIdx.x & 7) * 30 + (blockIdx.x >> 3);
    if (idx >= 234) return;
    STAMP2(0);
    WAIT_V(0);
    const int rtile = idx / 9, cb = idx - rtile * 9;
    const int m = lane & 15, kg = lane >> 4;
    const u32 lo = lane * 16;
    const char* mf = ws + WS_MF + (cb * 4 + w) * 18432;
    u32x4 B[18];
    auto ldB = [&](int ks) { asm volatile("global_load_dwordx4 %0, %1, %2" : "=v"(B[ks]) : "v"(lo), "s"(mf + ks * 1024) : "memory"); };
    auto ldA = [&](int j) {
        const int i = w + 4 * j, ks = i / 5, rb = min(rtile * 5 + (i - ks * 5), 127);
        glds16(lo, ws + WS_PQ + (rb * 18 + ks) * 1024, lbase + L2_A + i * 1024);
    };
#pragma unroll
    for (int gq = 0; gq < 3; ++gq) {
#pragma unroll
        for (int j = 0; j < 8; ++j) {
            if (gq < 2 || j < 6) ldA(gq * 8 + j);
            else if (j == 6 && w < 2) ldA(22);
            if (j < 6) ldB(gq * 6 + j);
        }
    }
    {
        const char* bt = ws + WS_BT + cb * 64 * 4096 + rtile * 160;
#pragma unroll
        for (int i = 0; i < 3; ++i) {
            int ins = w + 4 * i;
            if (ins < 10) {
                int p = ins * 64 + lane, c = p / 10, e = p - c * 10;
                glds16((u32)(c * 4096 + e * 16), bt, lbase + L2_B + ins * 1024);
            }
        }
    }
    float gv[5];
#pragma unroll
    for (int r = 0; r < 5; ++r) {
        u32 o = (u32)min(rtile * 80 + r * 16 + m, 2047) * 4;
        asm volatile("global_load_dword %0, %1, %2" : "=v"(gv[r]) : "v"(o), "s"(ws + WS_G) : "memory");
    }
    f32x4 acc[5];
#pragma unroll
    for (int r = 0; r < 5; ++r) acc[r] = f32x4{0, 0, 0, 0};
    lptr A = L + L2_A + lo;
    u64 t1 = 0;
#pragma unroll
    for (int gq = 0; gq < 3; ++gq) {
        if (gq == 0) WAIT_V(33); else if (gq == 1) WAIT_V(19); else WAIT_V(7);
        asm volatile("" : "+v"(B[gq * 6]), "+v"(B[gq * 6 + 1]), "+v"(B[gq * 6 + 2]), "+v"(B[gq * 6 + 3]), "+v"(B[gq * 6 + 4]), "+v"(B[gq * 6 + 5])::"memory");
        RAW_BARRIER();
        if (gq == 0) t1 = __builtin_amdgcn_s_memrealtime();
#pragma unroll
        for (int k = 0; k < 6; ++k) {
            const int ks = gq * 6 + k;
#pragma unroll
            for (int r = 0; r < 5; ++r) acc[r] = mfma32(B[ks], *(LAS u32x4*)(A + (ks * 5 + r) * 1024), acc[r]);
        }
    }
    WAIT_V(0);
    asm volatile("" : "+v"(gv[0]), "+v"(gv[1]), "+v"(gv[2]), "+v"(gv[3]), "+v"(gv[4])::"memory");
    RAW_BARRIER();
    if (lane == 0) ((u64*)(ws + WS_DBG))[65536 + (blockIdx.x * 4 + w) * 16 + 1] = t1;
    STAMP2(2);
    {
        lptr BT = L + L2_B + (w * 16 + 4 * kg + ((lane >> 2) & 3)) * 160 + (lane & 3) * 8;
        lptr O = L + L2_O + m * 144 + w * 32 + kg * 8;
#pragma unroll
        for (int r = 0; r < 5; ++r) {
            u32x2 b = trread(BT + r * 32);
            float g = gv[r], g1 = 1.0f - g;
            *(LAS u32x2*)(O + r * 16 * 144) = u32x2{pk2(g * acc[r][0] + g1 * lo2f(b[0]), g * acc[r][1] + g1 * hi2f(b[0])),
                                                    pk2(g * acc[r][2] + g1 * lo2f(b[1]), g * acc[r][3] + g1 * hi2f(b[1]))};
        }
    }
    RAW_BARRIER();
#pragma unroll
    for (int i = 0; i < 3; ++i) {
        const int p = i * 256 + tid, r = p >> 3;
        if (p < 640 && rtile * 80 + r < 2048)
            *(GAS u32x4*)((GAS char*)out + (size_t)(rtile * 80 + r) * 1152 + cb * 128 + (p & 7) * 16) = *(LAS u32x4*)(L + L2_O + r * 144 + (p & 7) * 16);
    }
    STAMP2(3);
    WAIT_V(0);
    STAMP2(4);
}

extern "C" void kernel_launch(void* const* d_in, const int* in_sizes, int n_in, void* d_out, int out_size, void* d_ws,
                              size_t ws_size, hipStream_t stream) {
    Params P;
    P.x = (const u16*)d_in[0];
    P.wqkv = (const u16*)d_in[1];
    P.wo = (const u16*)d_in[2];
    P.Are = (const u16*)d_in[4];
    P.Aim = (const u16*)d_in[5];
    P.Bre = (const u16*)d_in[6];
    P.Bim = (const u16*)d_in[7];
    P.Cre = (const u16*)d_in[8];
    P.Cim = (const u16*)d_in[9];
    P.logdt = (const u16*)d_in[10];
    P.Dskip = (const u16*)d_in[11];
    P.gw = (const u16*)d_in[12];
    P.ws = (char*)d_ws;
    (void)hipFuncSetAttribute((const void*)k1, hipFuncAttributeMaxDynamicSharedMemorySize, LDS1_BYTES);
    (void)hipFuncSetAttribute((const void*)k2, hipFuncAttributeMaxDynamicSharedMemorySize, LDS2_BYTES);
    k1<<<256, 512, LDS1_BYTES, stream>>>(P);
    k2<<<240, 256, LDS2_BYTES, stream>>>((char*)d_ws, (u16*)d_out);
}
```

```cpp
#include <hip/hip_runtime.h>
#include <hip/hip_bf16.h>
#include <stdint.h>

typedef unsigned short u16;
typedef unsigned int u32;
typedef unsigned long long u64;
typedef u32 u32x4 __attribute__((ext_vector_type(4)));
typedef u32 u32x2 __attribute__((ext_vector_type(2)));
typedef float f32x4 __attribute__((ext_vector_type(4)));
typedef float f32x2 __attribute__((ext_vector_type(2)));
typedef __bf16 bf16x8 __attribute__((ext_vector_type(8)));
typedef __bf16 bf16x2 __attribute__((ext_vector_type(2)));
typedef short s16x4 __attribute__((ext_vector_type(4)));
#define LAS __attribute__((address_space(3)))
#define GAS __attribute__((address_space(1)))
#define DEV __device__ __forceinline__
typedef LAS char* lptr;
typedef __amdgpu_buffer_rsrc_t rsrc_t;

constexpr u32 WS_EPOCH = 0;
constexpr u32 WS_WFF = 256;
constexpr u32 WS_G = 4096;
constexpr u32 WS_KVG = 65536;
constexpr u32 WS_WF = WS_KVG + 524288;
constexpr u32 WS_XT = WS_WF + 663552;
constexpr u32 WS_OG = WS_XT + 3145728;
constexpr u32 WS_BT = WS_OG + 3145728;
constexpr u32 WS_DBG = WS_BT + 4718592;
constexpr u32 WS_END = WS_DBG + 1048576;
constexpr int AUX_SC1 = 16;
#define EXP 0
#define SPIN_LOG 17
constexpr u32 SPIN_MAX = 1u << SPIN_LOG;

constexpr u32 L_FLAG = 0;
constexpr u32 CH_KC = 0, CH_KSTRIDE = 4160, CH_XR = 16640, CH_BYTES = 21248;
constexpr u32 L_CH = 64;
constexpr u32 L_KT = L_CH + 3 * CH_BYTES;
constexpr u32 L_XS = L_KT + 3 * 1312;
constexpr u32 L_XRING = L_XS + 10240;
constexpr u32 L_WRING = L_XRING + 49152;
constexpr u32 L_AIMG = L_WRING + 30720;
constexpr u32 LDS_BYTES = L_AIMG + 5632;
constexpr u32 L_KVX = L_WRING;
constexpr u32 TQ = 944;
static_assert(LDS_BYTES <= 163840, "lds");
static_assert(L_XRING % 32 == 0 && L_XS % 16 == 0, "align");

DEV float bf2f(u16 v) { return __uint_as_float(((u32)v) << 16); }
DEV u32 pk2(float a, float b) {
    f32x2 v = {a, b};
    return __builtin_bit_cast(u32, __builtin_convertvector(v, bf16x2));
}
DEV u32 pk2s(float a, float b) {
    u32 r;
    asm("v_cvt_pk_bf16_f32 %0, %1, %2" : "=v"(r) : "v"(a), "v"(b));
    return r;
}
DEV float lo2f(u32 v) { return __uint_as_float(v << 16); }
DEV float hi2f(u32 v) { return __uint_as_float(v & 0xffff0000u); }
DEV f32x4 mfma32(u32x4 a, u32x4 b, f32x4 c) {
    return __builtin_amdgcn_mfma_f32_16x16x32_bf16(__builtin_bit_cast(bf16x8, a), __builtin_bit_cast(bf16x8, b), c, 0, 0, 0);
}
DEV void glds16(u32 voff, const void* sbase, u32 ldsaddr) {
    asm volatile("s_mov_b32 m0, %2\n\ts_nop 0\n\tglobal_load_lds_dwordx4 %0, %1" ::"v"(voff), "s"(sbase), "s"(ldsaddr) : "memory");
}
DEV void glds16x4(u32 v0, u32 v1, u32 v2, u32 v3, const void* sbase, u32 ldsaddr) {
    asm volatile("s_mov_b32 m0, %5\n\ts_nop 0\n\tglobal_load_lds_dwordx4 %0, %4\n\tglobal_load_lds_dwordx4 %1, %4 offset:1024\n\t"
                 "global_load_lds_dwordx4 %2, %4 offset:2048\n\tglobal_load_lds_dwordx4 %3, %4 offset:3072" ::"v"(v0), "v"(v1), "v"(v2), "v"(v3),
                 "s"(sbase), "s"(ldsaddr) : "memory");
}
DEV u32x2 trread(lptr p) { return __builtin_bit_cast(u32x2, __builtin_amdgcn_ds_read_tr16_b64_v4i16((LAS s16x4*)p)); }
DEV u32x4 cat(u32x2 a, u32x2 b) { return u32x4{a[0], a[1], b[0], b[1]}; }
DEV u32x4 ld16(rsrc_t rs, u32 voff, u32 soff) { return __builtin_amdgcn_raw_buffer_load_b128(rs, voff, soff, AUX_SC1); }
DEV void st16(rsrc_t rs, u32x4 v, u32 voff, u32 soff) { __builtin_amdgcn_raw_buffer_store_b128(v, rs, voff + soff, 0, AUX_SC1); }
DEV u32x4 seal(u32 a, u32 b, u32 c, u32 tag) { return u32x4{a, b, c, a ^ b ^ c ^ tag}; }
DEV bool sealed(u32x4 g, u32 tag) { return (g[0] ^ g[1] ^ g[2] ^ g[3]) == tag; }
#define WAIT_V(n) asm volatile("s_waitcnt vmcnt(%0)" ::"n"(n) : "memory")
#define WAIT_L(n) asm volatile("s_waitcnt lgkmcnt(%0)" ::"n"(n) : "memory")
#define WAIT_L0() asm volatile("s_waitcnt lgkmcnt(0)" ::: "memory")
#define RAW_BARRIER() asm volatile("s_waitcnt lgkmcnt(0)\n\ts_barrier" ::: "memory")
#define CFENCE() asm volatile("" ::: "memory")
#define SCHED() __builtin_amdgcn_sched_barrier(0)

#define STAMP(i) do {} while (0)

DEV void flag_set(lptr L, int wave, u32 v) {
    WAIT_L0();
    *(volatile LAS u32*)(L + L_FLAG + wave * 4) = v;
}
DEV void flag_wait1(lptr L, int wave, u32 v) {
#pragma nounroll
    for (u32 it = 0; it < SPIN_MAX * 8; ++it) {
        u32 f = *(volatile LAS u32*)(L + L_FLAG + wave * 4);
        if (__builtin_amdgcn_readfirstlane(f) >= v) break;
        __builtin_amdgcn_s_sleep(1);
    }
    CFENCE();
}
DEV bool quad_ok(u32x4 f, u32 v) {
    u32 m = min(min(f[0], f[1]), min(f[2], f[3]));
    return __builtin_amdgcn_readfirstlane(m) >= v;
}
DEV void quad_wait(lptr Q, u32 v) {
#pragma nounroll
    for (u32 it = 0; it < SPIN_MAX * 8; ++it) {
        u32x4 f = *(volatile LAS u32x4*)(Q);
        if (quad_ok(f, v)) break;
        __builtin_amdgcn_s_sleep(1);
    }
    CFENCE();
}
DEV void quad_spin(lptr Q, u32 v) {
#pragma nounroll
    for (u32 it = 0; it < SPIN_MAX * 16; ++it) {
        u32x4 f = *(volatile LAS u32x4*)(Q);
        if (quad_ok(f, v)) break;
    }
    CFENCE();
}
DEV u32 mk_tag(u32 epoch) { return epoch * 0x9E3779B1u + 0x7F4A7C15u; }
DEV void put_epoch(lptr L, u32 epoch) { *(volatile LAS u32x2*)(L + L_FLAG + 32) = u32x2{epoch, 1u}; }
DEV u32 get_epoch(lptr L) {
    u32x2 v;
#pragma nounroll
    for (u32 it = 0; it < SPIN_MAX * 8; ++it) {
        v = *(volatile LAS u32x2*)(L + L_FLAG + 32);
        if (__builtin_amdgcn_readfirstlane(v[1])) break;
        __builtin_amdgcn_s_sleep(1);
    }
    CFENCE();
    return __builtin_amdgcn_readfirstlane(v[0]);
}
template <int N>
DEV void poll_rows(u32x4 (&g)[N], rsrc_t rs, u32 voff, u32 soff, u32 stride, u32 tag) {
    u32 pend = 0;
#pragma unroll
    for (int i = 0; i < N; ++i) pend |= __all(sealed(g[i], tag)) ? 0u : 1u << i;
#pragma nounroll
    for (u32 it = 0; pend && it < SPIN_MAX; ++it) {
        __builtin_amdgcn_s_sleep(2);
#pragma unroll
        for (int i = 0; i < N; ++i)
            if (pend >> i & 1) g[i] = ld16(rs, voff, soff + i * stride);
        CFENCE();
        u32 p2 = 0;
#pragma unroll
        for (int i = 0; i < N; ++i) p2 |= __all(sealed(g[i], tag)) ? 0u : 1u << i;
        pend = p2;
    }
    CFENCE();
}

struct Args {
    const u16 *x, *wqkv, *wo, *Are, *Aim, *Bre, *Bim, *Cre, *Cim, *logdt, *Dskip, *gw;
    char* ws;
    u16* out;
};
typedef u64 u64x8 __attribute__((ext_vector_type(8)));
typedef u64 u64x4 __attribute__((ext_vector_type(4)));
typedef u64 u64x2 __attribute__((ext_vector_type(2)));
struct Params : Args {
    u64x8 k0; u64x4 k1; u64x2 k2;
    rsrc_t rs;
    DEV void fetch() {
        asm volatile("s_load_dwordx16 %0, %3, 0x0\n\ts_load_dwordx8 %1, %3, 0x40\n\ts_load_dwordx4 %2, %3, 0x60"
                     : "=&s"(k0), "=&s"(k1), "=&s"(k2) : "s"(__builtin_amdgcn_kernarg_segment_ptr()));
    }
    DEV static u64 take(u64 v) {
        u64 r;
        asm volatile("s_mov_b64 %0, %1" : "=s"(r) : "s"(v));
        return r;
    }
    template <int ROLE>
    DEV void arrive() {
        asm volatile("s_waitcnt lgkmcnt(0)" ::"s"(k0), "s"(k1), "s"(k2));
        if (ROLE != 2) x = (const u16*)take(k0[0]);
        if (ROLE < 2) wqkv = (const u16*)take(k0[1]);
        if (ROLE == 3) { wo = (const u16*)take(k0[2]); gw = (const u16*)take(k1[3]); }
        if (ROLE == 2) {
            Are = (const u16*)take(k0[3]); Aim = (const u16*)take(k0[4]); Bre = (const u16*)take(k0[5]); Bim = (const u16*)take(k0[6]);
            Cre = (const u16*)take(k0[7]); Cim = (const u16*)take(k1[0]); logdt = (const u16*)take(k1[1]); Dskip = (const u16*)take(k1[2]);
        }
        ws = (char*)take(k2[0]);
        if (ROLE == 0) out = (u16*)take(k2[1]);
        rs = __builtin_amdgcn_make_buffer_rsrc((void*)ws, 0, (int)WS_END, 0x00020000);
    }
};
DEV void pos5(u32 idx, u32& pos, u32& piece) {
    pos = (idx * 52429u) >> 18;
    piece = idx - 5 * pos;
}
DEV u32 pos2row(u32 pos) { return (pos & 56) | ((pos >> 1) & 3) | ((pos & 1) << 2); }

DEV int first_chunk(int rt, int hp) {
    const int c0 = (hp & 7) + 2 * (rt & 3);
    return c0 >= 9 ? c0 - 9 : c0;
}
DEV u32 loader_role(Params& P, lptr L, u32 lbase, int lane, int rt, int hp) {
    u32 wv[5], sv[5], xv[4];
#pragma unroll
    for (int p = 0; p < 5; ++p) {
        u32 pos, piece;
        pos5(p * 64 + lane, pos, piece);
        wv[p] = pos2row(pos) * 3456 + piece * 16 - (p & 3) * 1024;
        sv[p] = (126 - 2 * pos) * 1152 + piece * 16 - (p & 3) * 1024;
    }
#pragma unroll
    for (int j = 0; j < 4; ++j) xv[j] = (u32)((lane >> 3) * 1152 + (((lane & 7) ^ (lane >> 3)) << 4)) + j * 8192;
    int c = first_chunk(rt, hp);
    P.arrive<1>();
    u32 epoch;
    asm volatile("global_load_dword %0, %1, %2 sc1" : "=v"(epoch) : "v"(WS_EPOCH), "s"(P.ws) : "memory");
    const char* xg = (const char*)P.x + rt * 128 * 1152;
    const char* wg = (const char*)P.wqkv + 72 * hp;
    auto five = [&](u32(&v)[5], const char* src, u32 dst) {
        glds16x4(v[0], v[1], v[2], v[3], src, dst);
        glds16(v[4], src, dst + 4096);
    };
    {
        const char* xs = xg + 72 * hp - (hp ? 8 : 0);
        five(sv, xs, lbase + L_XS);
        five(sv, xs + 1152, lbase + L_XS + 5120);
    }
    RAW_BARRIER();
    __builtin_amdgcn_s_setprio(3);
    auto next = [&]() { c = c == 8 ? 0 : c + 1; };
    auto ldx = [&](u32 sl) {
#pragma unroll
        for (int w = 0; w < 4; ++w) glds16x4(xv[0], xv[1], xv[2], xv[3], xg + c * 128 + w * 36864, lbase + L_XRING + sl * 16384 + w * 4096);
    };
    auto ldw = [&](u32 sl) {
        five(wv, wg + c * 221184 + 1144, lbase + L_WRING + sl * 10240);
        five(wv, wg + c * 221184 + 2296, lbase + L_WRING + sl * 10240 + 5120);
    };
    volatile LAS u32* FULL = (volatile LAS u32*)(L + L_FLAG + 40);
    next();
    ldx(1);
    ldw(1);
    asm volatile("s_waitcnt vmcnt(26)" : "+v"(epoch)::"memory");
    const u32 ep = __builtin_amdgcn_readfirstlane(epoch);
    put_epoch(L, ep);
    STAMP(1);
    next();
    ldx(2);
    WAIT_V(16);
    *FULL = 2;
    ldw(2);
    u32 sl = 0;
#pragma nounroll
    for (u32 j = 3; j < 9; ++j) {
        next();
        quad_spin(L + L_FLAG, 2 * j - 4);
        ldx(sl);
        if (j == 3) { WAIT_V(16); *FULL = 3; }
        quad_spin(L + L_FLAG, 2 * j - 3);
        ldw(sl);
        WAIT_V(26);
        *FULL = j;
        sl = sl == 2 ? 0 : sl + 1;
    }
#pragma nounroll
    for (u32 s = 0; s < 3; ++s) {
        quad_spin(L + L_FLAG, 14 + 2 * s);
#pragma unroll
        for (int i = 0; i < 3; ++i) {
            next();
            five(wv, wg + c * 221184, lbase + L_XRING + s * 16384 + i * 5120);
        }
        if (s == 0) {
            quad_spin(L + L_FLAG, 15);
            WAIT_V(15);
            *FULL = 9;
        }
    }
    WAIT_V(0);
    *FULL = 10;
    __builtin_amdgcn_s_setprio(0);
    STAMP(2);
    return mk_tag(ep);
}

constexpr float LOG2E = 1.44269504088896340736f;
constexpr float EPS = 1e-6f;
constexpr float ONE_EPS = 1.0f + 1e-6f;
DEV float phi(float a) {
    float e = __builtin_amdgcn_exp2f(fmaf(a, LOG2E, -ONE_EPS * LOG2E));
    return __builtin_amdgcn_fmed3f(e + EPS, a, ONE_EPS);
}

DEV bool wf_ok(u32x4 f, int lane, u32 tag) { return __all(f[0] == tag && f[1] == tag && (lane >= 40 || (f[2] == tag && f[3] == tag))); }

DEV void gemm_role(Params& P, lptr L, u32 lbase, int w, int lane, int rt, int hp, int rb, int cq) {
    const int m = lane & 15, kg = lane >> 4;
    {
        const u32 xo = (u32)((lane >> 3) * 1152 + (((lane & 7) ^ (lane >> 3)) << 4));
        u32 wv[3];
#pragma unroll
        for (int j = 0; j < 3; ++j) {
            u32 pos, piece;
            pos5((w + 4 * j) * 64 + lane, pos, piece);
            wv[j] = pos2row(pos) * 3456 + (pos >> 6) * 1152 + piece * 16;
        }
        const int c0 = first_chunk(rt, hp);
        P.arrive<0>();
        STAMP(40);
        glds16x4(xo, xo + 8192, xo + 16384, xo + 24576, (const char*)P.x + (rt * 128 + w * 32) * 1152 + c0 * 128, lbase + L_XRING + w * 4096);
        const char* wsrc = (const char*)P.wqkv + 72 * hp + c0 * 221184 + 1144;
        glds16(wv[0], wsrc, lbase + L_WRING + w * 1024);
        glds16(wv[1], wsrc, lbase + L_WRING + w * 1024 + 4096);
        if (w < 2) glds16(wv[2], wsrc, lbase + L_WRING + w * 1024 + 8192);
    }
    const rsrc_t rs = P.rs;
    STAMP(41);
    RAW_BARRIER();
    STAMP(42);
    __builtin_amdgcn_s_setprio(2);
    *(LAS u32x4*)(L + L_AIMG + w * 1024 + lane * 16) = u32x4{0, 0, 0, 0};
    if (lane < 48) *(LAS u32x2*)(L + L_AIMG + 4096 + w * 384 + lane * 8) = u32x2{0, 0};
    f32x4 acc[2][5];
    const bool kl = !(m & 4);
#pragma unroll
    for (int ct = 0; ct < 5; ++ct) {
        float iv = ct < 2 ? ONE_EPS : (ct < 4 ? 0.f : (kl ? ONE_EPS : 0.f));
        acc[0][ct] = f32x4{iv, iv, iv, iv};
        acc[1][ct] = acc[0][ct];
    }
    lptr XA0 = L + L_XRING + w * 4096 + m * 128 + ((kg ^ (m & 7)) << 4);
    lptr XA1 = L + L_XRING + w * 4096 + m * 128 + (((4 + kg) ^ (m & 7)) << 4);
    const int ra = (8 * kg + 2 * ((lane >> 2) & 3)) * 80;
    lptr WA = L + L_WRING + ra + (lane & 3) * 8 + 8;
    lptr WL = L + L_WRING + ra + (lane & 1) * 5120 + 40;
    volatile LAS u32* FULL = (volatile LAS u32*)(L + L_FLAG + 40);
    volatile LAS u32* FREE = (volatile LAS u32*)(L + L_FLAG + w * 4);
    u32x4 X[18][2];
    u32x2 B[2][5][2];
    auto rdx = [&](int i, int e) {
        X[2 * i + (e >> 1)][e & 1] = *(LAS u32x4*)(((e >> 1) ? XA1 : XA0) + (i % 3) * 16384 + (e & 1) * 2048);
    };
    auto rdb = [&](int i, int ks, int q) {
        const int o = (i % 3) * 10240 + ks * 2560;
        lptr p = q == 2 ? WL + o : WA + o + (q >= 3 ? 5120 : 0) + ((q == 1 || q == 4) ? 40 : 0);
        B[ks][q][0] = trread(p);
        B[ks][q][1] = trread(p + 80);
    };
    auto mm = [&](int k, int s) {
        constexpr int ord[5] = {0, 1, 4, 2, 3};
        const int q = s >> 1, r = s & 1;
        acc[r][ord[q]] = mfma32(X[k][r], cat(B[k & 1][q][0], B[k & 1][q][1]), acc[r][ord[q]]);
    };
    WAIT_V(0);
    flag_set(L, w, 1);
    quad_wait(L + L_FLAG, 1);
    STAMP(1);
#pragma unroll
    for (int e = 0; e < 4; ++e) rdx(0, e);
#pragma unroll
    for (int q = 0; q < 5; ++q) rdb(0, 0, q);
    SCHED();
#pragma unroll
    for (int i = 0; i < 9; ++i) {
        u32 f = 0;
#pragma unroll
        for (int s = 0; s < 10; ++s) {
            mm(2 * i, s);
            if (s == 1) { WAIT_L(12); *FREE = 2 * i + 2; }
            if (s < 5) rdb(i, 1, s);
            if (s == 5 && i < 8) f = *FULL;
            SCHED();
        }
#pragma unroll
        for (int s = 0; s < 10; ++s) {
            mm(2 * i + 1, s);
            if (s == 0 && i < 8) {
                WAIT_L0();
                *FREE = 2 * i + 3;
                if (__builtin_amdgcn_readfirstlane(f) < (u32)(i + 2)) flag_wait1(L, 10, i + 2);
                CFENCE();
            }
            if (i < 8) {
                if (s == 1 || s == 2) { rdx(i + 1, 2 * s - 2); rdx(i + 1, 2 * s - 1); }
                if (s >= 3 && s < 8) rdb(i + 1, 0, s - 3);
            }
            SCHED();
        }
        STAMP(16 + i);
    }
    STAMP(2);
    const u32 tag = mk_tag(get_epoch(L));
    {
        u32x4 op[5];
        const float ca = kl ? EPS : __builtin_inff(), cb = kl ? ONE_EPS : -__builtin_inff();
#pragma unroll
        for (int ct = 0; ct < 5; ++ct) {
            f32x4 v[2];
#pragma unroll
            for (int r = 0; r < 2; ++r)
#pragma unroll
                for (int i = 0; i < 4; ++i) {
                    float a = acc[r][ct][i];
                    if (ct < 2) a = phi(a);
                    if (ct == 4) a = __builtin_amdgcn_fmed3f(__builtin_amdgcn_exp2f(fmaf(a, LOG2E, -ONE_EPS * LOG2E)) + ca, a, cb);
                    v[r][i] = a;
                }
            op[ct] = u32x4{pk2(v[0][0], v[0][1]), pk2(v[0][2], v[0][3]), pk2(v[1][0], v[1][1]), pk2(v[1][2], v[1][3])};
        }
#pragma unroll
        for (int i = 0; i < 4; ++i) op[4][i] = m >= 12 ? 0x3f803f80u : op[4][i];
        const f32x4 Z = {0, 0, 0, 0};
        f32x4 j1[2], j3[2], j5[2], j7;
        j3[0] = mfma32(op[4], op[0], Z);
        j3[1] = mfma32(op[4], op[1], Z);
        j7 = mfma32(op[4], op[4], Z);
        j5[0] = mfma32(op[4], op[2], Z);
        j5[1] = mfma32(op[4], op[3], Z);
        j1[0] = mfma32(op[0], op[2], Z);
        j1[1] = mfma32(op[1], op[3], Z);
        f32x2 ex[2];
#pragma unroll
        for (int h = 0; h < 2; ++h)
#pragma unroll
            for (int i = 0; i < 2; ++i) ex[h][i] = (kg & 1) ? j3[h][2 * h + i] : (kg ? j7[2 * h + i] : j5[h][2 * h + i]);
        lptr K4 = L + L_KVX + lane * 16, K2 = L + L_KVX + 1024 + lane * 8;
        if (w >= 2) {
#pragma unroll
            for (int h = 0; h < 2; ++h) {
                *(LAS f32x4*)(K4 + (2 * w - 2 + h) * 1536) = j1[h];
                *(LAS f32x2*)(K2 + (2 * w - 2 + h) * 1536) = ex[h];
            }
            flag_set(L, w, 20);
        } else {
            *(LAS f32x4*)(K4 + w * 1536) = w ? j1[0] : j1[1];
            *(LAS f32x2*)(K2 + w * 1536) = w ? ex[0] : ex[1];
            flag_set(L, w, 20);
            f32x4 s4 = w ? j1[1] : j1[0];
            f32x2 s2 = w ? ex[1] : ex[0];
            quad_wait(L + L_FLAG, 20);
            s4 += *(LAS f32x4*)(K4 + (1 - w) * 1536);
            s2 += *(LAS f32x2*)(K2 + (1 - w) * 1536);
            f32x4 t4 = *(LAS f32x4*)(K4 + (2 + w) * 1536) + *(LAS f32x4*)(K4 + (4 + w) * 1536);
            f32x2 t2 = *(LAS f32x2*)(K2 + (2 + w) * 1536) + *(LAS f32x2*)(K2 + (4 + w) * 1536);
            s4 += t4;
            s2 += t2;
            st16(rs, seal(pk2(s4[0], s4[1]), pk2(s4[2], s4[3]), pk2(s2[0], s2[1]), tag), kg * 4096 + m * 16, WS_KVG + (hp * 8 + w * 4) * 4096 + rt * 256);
        }
    }
    STAMP(3);
    const u32 lo = lane * 16;
    u32x4 wff = ld16(rs, WS_WFF + min(lane, 40) * 16, 0);
    flag_wait1(L, 10, 10);
    STAMP(4);
    f32x4 q[2][3];
#pragma unroll
    for (int r = 0; r < 2; ++r)
#pragma unroll
        for (int t = 0; t < 3; ++t) q[r][t] = f32x4{ONE_EPS, ONE_EPS, ONE_EPS, ONE_EPS};
    u32x4 g[8], WF[45];
    const int grp = 9 * (cq >> 1) + 5 * (cq & 1) + w;
    const u32 wf_main = WS_WF + grp * 36864;
    const u32 wf_ext = WS_WF + (((9 * (cq >> 1) + 4) * 18 + 9 * (w >> 1)) * 2 + (cq & 1)) * 1024;
    auto ldwf = [&](int i) { WF[i] = i < 9 ? ld16(rs, lo, wf_ext + i * 2048) : ld16(rs, lo, wf_main + (i - 9) * 1024); };
    {
        lptr QA = L + L_XRING + ra + (lane & 3) * 8, QL = L + L_XRING + ra + 32;
        u32x2 a[2][3][2];
        auto rdq = [&](int k, int t) {
            const int i = k >> 1, o = (i / 3) * 16384 + (i % 3) * 5120 + (k & 1) * 2560;
            lptr p = t == 2 ? QL + o : QA + o + t * 40;
            a[k & 1][t][0] = trread(p);
            a[k & 1][t][1] = trread(p + 80);
        };
        rdq(0, 0); rdq(0, 1); rdq(0, 2);
        SCHED();
#pragma unroll
        for (int k = 0; k < 18; ++k) {
            if (k == 3) {
#pragma nounroll
                for (u32 it = 0; !wf_ok(wff, lane, tag) && it < SPIN_MAX; ++it) {
                    __builtin_amdgcn_s_sleep(8);
                    wff = ld16(rs, WS_WFF + min(lane, 40) * 16, 0);
                }
                CFENCE();
            }
#pragma unroll
            for (int s = 0; s < 6; ++s) {
                const int t = s >> 1, r = s & 1;
                q[r][t] = mfma32(cat(a[k & 1][t][0], a[k & 1][t][1]), X[k][r], q[r][t]);
                if (s < 3 && k < 17) rdq(k + 1, s);
                if ((s == 3 || s == 5) && k >= 4 && k < 17) ldwf(2 * (k - 4) + (s == 5));
                SCHED();
            }
        }
    }
#pragma unroll
    for (int i = 0; i < 8; ++i) g[i] = ld16(rs, lo, WS_KVG + (hp * 8 + 2 * w) * 4096 + i * 1024);
    SCHED();
    STAMP(5);
    u32x4 pq1[2], pq2[2];
#pragma unroll
    for (int r = 0; r < 2; ++r) {
        u32 p[6];
#pragma unroll
        for (int t = 0; t < 3; ++t) {
            p[2 * t] = pk2(phi(q[r][t][0]), phi(q[r][t][1]));
            p[2 * t + 1] = pk2(phi(q[r][t][2]), phi(q[r][t][3]));
        }
        pq1[r] = u32x4{p[0], p[1], p[2], p[3]};
        pq2[r] = u32x4{p[4], p[5], 0, 0};
    }
    u32x4 sel[2];
    {
        const u32 one = (m & 1) ? 0x3f800000u : 0x3f80u;
#pragma unroll
        for (int s = 0; s < 2; ++s)
#pragma unroll
            for (int i = 0; i < 4; ++i) sel[s][i] = (i < 3 && (m >> 1) == 4 * s + i) ? one : 0u;
    }
    const int hd = w >> 1, kgp = 2 * (w & 1) + (kg >> 1);
    int a0, a1;
    {
        const int DUMP = 38 * 144;
        const int rowE = (20 * hd + m) * 144, ex0 = (16 + 2 * hd) * 144, den = (36 + hd) * 144;
        const int cd = 16 * (m >> 2) + 8 * hd + 2 * (m & 3), cx = 64 + 4 * hd;
        if (!(kg & 1)) {
            a0 = rowE + 16 * kgp + 8 * hd; a1 = 0;
        } else if (kgp == 0) {
            a0 = rowE + cx; a1 = a0 + 2;
        } else if (kgp == 1) {
            a0 = ex0 + cd; a1 = a0 + 144;
        } else if (kgp == 2) {
            a0 = (m >> 1) == 2 + hd ? ex0 + (m & 1) * 144 + cx : (m == 12 ? den + cx : DUMP); a1 = a0 + 2;
        } else {
            a0 = den + cd; a1 = DUMP;
        }
    }
    poll_rows<8>(g, rs, lo, WS_KVG + (hp * 8 + 2 * w) * 4096, 1024, tag);
    STAMP(6);
    {
        f32x4 d0 = {0, 0, 0, 0}, d1 = d0;
#pragma unroll
        for (int i = 0; i < 4; ++i) {
            g[i][3] = 0; g[4 + i][3] = 0;
            d0 = mfma32(sel[0], g[i], d0);
            d1 = mfma32(sel[1], g[4 + i], d1);
        }
        d0 += d1;
        const u32 p01 = pk2(d0[0], d0[1]), p23 = pk2(d0[2], d0[3]);
        lptr AI = L + L_AIMG;
        if (!(kg & 1)) {
            *(LAS u32x2*)(AI + a0) = u32x2{p01, p23};
        } else {
            *(LAS u16*)(AI + a0) = (u16)p01;
            *(LAS u16*)(AI + a1) = (u16)(p01 >> 16);
        }
    }
    flag_set(L, w, 21);
    quad_wait(L + L_FLAG, 21);
    {
        const int kr = m >> 2, i = m & 3;
        lptr A0 = L + L_AIMG + (2 * kr + (i & 1) + 8 * (i >> 1)) * 144 + kg * 16;
        lptr A2 = L + L_AIMG + (i == 0 ? 32 + kr : 36 + ((i - 1) & 1)) * 144 + kg * 16;
        u32x4 A[3][2];
        A[2][0] = *(LAS u32x4*)(A2); A[2][1] = *(LAS u32x4*)(A2 + 64);
        A[0][0] = *(LAS u32x4*)(A0); A[0][1] = *(LAS u32x4*)(A0 + 64);
        A[1][0] = *(LAS u32x4*)(A0 + 16 * 144); A[1][1] = *(LAS u32x4*)(A0 + 16 * 144 + 64);
        f32x4 o[2][3];
#pragma unroll
        for (int tt = 0; tt < 3; ++tt)
#pragma unroll
            for (int r = 0; r < 2; ++r) {
                const int t = (tt + 2) % 3;
                o[r][t] = mfma32(A[t][0], pq1[r], t == 2 ? f32x4{0, EPS, EPS, 0} : f32x4{0, 0, 0, 0});
                o[r][t] = mfma32(A[t][1], pq2[r], o[r][t]);
            }
        u32 d[2][4];
        float e[2];
#pragma unroll
        for (int r = 0; r < 2; ++r) {
            const float ra = __builtin_amdgcn_rcpf(o[r][2][1]), rb = __builtin_amdgcn_rcpf(o[r][2][2]), rc = kg ? rb : ra;
            d[r][0] = pk2s(o[r][0][0] * ra, o[r][0][1] * ra);
            d[r][1] = pk2s(o[r][0][2] * ra, o[r][0][3] * ra);
            d[r][2] = pk2s(o[r][1][0] * rc, o[r][1][1] * rc);
            d[r][3] = pk2s(o[r][1][2] * rb, o[r][1][3] * rb);
            e[r] = o[r][2][0] * rb;
        }
        const u32 so = WS_OG + ((rt * 4 + w) * 16 + hp) * 3072;
        st16(rs, seal(d[0][0], d[0][1], d[0][2], tag), lo, so);
        st16(rs, seal(d[1][0], d[1][1], d[1][2], tag), lo, so + 1024);
        st16(rs, seal(d[0][3], pk2s(e[0], e[1]), d[1][3], tag), lo, so + 2048);
    }
    STAMP(7);
#pragma unroll
    for (int i = 26; i < 45; ++i) ldwf(i);
    const int c8 = 32 * (9 * (cq >> 1) + 4) + 16 * (cq & 1);
    lptr IM = L + L_XRING;
    {
        u32x4 og[12];
        const u32 so = WS_OG + (rb * 16 + 4 * w) * 3072;
#pragma unroll
        for (int i = 0; i < 12; ++i) og[i] = ld16(rs, lo, so + i * 1024);
        STAMP(8);
        poll_rows<12>(og, rs, lo, so, 1024, tag);
        STAMP(9);
        lptr B0 = IM + w * 4608 + m * 16;
#pragma unroll
        for (int h = 0; h < 4; ++h) {
            lptr E = B0 + (h >> 1) * 2304 + ((h & 1) ? 1024 + (kg >> 1) * 256 + ((8 + 4 * kg) & 15) : 4 * kg);
            lptr E2 = B0 + (h >> 1) * 2304 + ((h & 1) ? 2048 + 8 : 1024) + 2 * kg;
#pragma unroll
            for (int i = 0; i < 3; ++i) {
                *(LAS u32*)(E + i * 256) = og[3 * h][i];
                *(LAS u32*)(E + 18432 + i * 256) = og[3 * h + 1][i];
            }
            *(LAS u32*)(E + 768) = og[3 * h + 2][0];
            *(LAS u32*)(E + 18432 + 768) = og[3 * h + 2][2];
            *(LAS u16*)(E2) = (u16)og[3 * h + 2][1];
            *(LAS u16*)(E2 + 18432) = (u16)(og[3 * h + 2][1] >> 16);
        }
    }
    SCHED();
    u32x4 bt[4], b8[1];
    const u32 bs = WS_BT + (rb * 576 + grp * 32) * 128, bs8 = WS_BT + (rb * 576 + c8) * 128 + (w & 1) * 16;
    const u32 bv = (lane >> 2) * 128 + (lane & 3) * 32, gs = WS_G + rb * 256;
    u32x2 g0, g1;
    flag_set(L, w, 22);
    quad_wait(L + L_FLAG, 22);
    STAMP(10);
    const f32x4 Z = {0, 0, 0, 0};
    f32x4 o[2][2] = {{Z, Z}, {Z, Z}}, a8 = Z;
    lptr EX = L + L_WRING + (w & 1) * 1024 + lo;
    {
        lptr F = IM + lo, F8 = IM + (w & 1) * 18432 + (w >> 1) * 9216 + lo;
        u32x4 f8[9], f[3][2];
#pragma unroll
        for (int k = 0; k < 9; ++k) f8[k] = *(LAS u32x4*)(F8 + k * 1024);
        auto rdf = [&](int k) { f[k % 3][0] = *(LAS u32x4*)(F + k * 1024); f[k % 3][1] = *(LAS u32x4*)(F + 18432 + k * 1024); };
        SCHED();
#pragma unroll
        for (int k = 0; k < 9; ++k) {
            a8 = mfma32(WF[k], f8[k], a8);
            if (k == 2 || k == 4) rdf(k / 2 - 1);
            SCHED();
        }
#pragma unroll
        for (int k = 0; k < 18; ++k) {
            if (k + 2 < 18) rdf(k + 2);
            if (k >= 8 && k < 12) bt[k - 8] = ld16(rs, lo, bs + (k - 8) * 1024);
            if (k == 12) b8[0] = ld16(rs, bv, bs8);
            if (k == 6) g0 = __builtin_amdgcn_raw_buffer_load_b64(rs, m * 8, gs, AUX_SC1);
            if (k == 7) g1 = __builtin_amdgcn_raw_buffer_load_b64(rs, m * 8, gs + 128, AUX_SC1);
#pragma unroll
            for (int r = 0; r < 2; ++r)
#pragma unroll
                for (int t = 0; t < 2; ++t) o[r][t] = mfma32(WF[9 + 2 * k + t], f[k % 3][r], o[r][t]);
            if (k == 1 && w >= 2) { *(LAS f32x4*)(EX) = a8; }
            if (k == 3 && w >= 2) flag_set(L, w, 23);
            SCHED();
        }
    }
    STAMP(11);
    poll_rows<4>(bt, rs, lo, bs, 1024, tag);
    lptr BI = L + L_WRING + 2048 + w * 2560;
#pragma unroll
    for (int i = 0; i < 4; ++i) *(LAS u32x2*)(BI + (lane & 1) * 1024 + i * 256 + (lane >> 1) * 8) = u32x2{bt[i][0], bt[i][1]};
    if (w < 2) {
        poll_rows<1>(b8, rs, bv, bs8, 0, tag);
        *(LAS u32x2*)(BI + 2048 + lane * 8) = u32x2{b8[0][0], b8[0][1]};
    }
#pragma nounroll
    for (u32 it = 0; it < SPIN_MAX && !__all((g0[0] ^ g0[1]) == tag && (g1[0] ^ g1[1]) == tag); ++it) {
        __builtin_amdgcn_s_sleep(2);
        g0 = __builtin_amdgcn_raw_buffer_load_b64(rs, m * 8, gs, AUX_SC1);
        g1 = __builtin_amdgcn_raw_buffer_load_b64(rs, m * 8, gs + 128, AUX_SC1);
        CFENCE();
    }
    CFENCE();
    STAMP(12);
    auto mix = [&](float g, f32x4 a, u32x2 b) {
        const float b0 = lo2f(b[0]), b1 = hi2f(b[0]), b2 = lo2f(b[1]), b3 = hi2f(b[1]);
        return u32x2{pk2s(fmaf(g, a[0] - b0, b0), fmaf(g, a[1] - b1, b1)), pk2s(fmaf(g, a[2] - b2, b2), fmaf(g, a[3] - b3, b3))};
    };
    GAS char* orow = (GAS char*)P.out + (rb * 32 + m) * 1152;
#pragma unroll
    for (int r = 0; r < 2; ++r) {
        const float g = __uint_as_float(r ? g1[0] : g0[0]);
        const u32x2 t0 = trread(BI + r * 1024 + kg * 256 + m * 8), t1 = trread(BI + r * 1024 + kg * 256 + 128 + m * 8);
        *(GAS u32x4*)(orow + r * 18432 + grp * 64 + kg * 16) = cat(mix(g, o[r][0], t0), mix(g, o[r][1], t1));
    }
    if (w < 2) {
        flag_wait1(L, w + 2, 23);
        a8 += *(LAS f32x4*)(EX);
        *(GAS u32x2*)(orow + w * 18432 + c8 * 2 + kg * 8) = mix(__uint_as_float(w ? g1[0] : g0[0]), a8, trread(BI + 2048 + lane * 8));
    }
    STAMP(13);
}

struct cf { float r, i; };
DEV cf cmul(cf a, cf b) { return cf{fmaf(a.r, b.r, -(a.i * b.i)), fmaf(a.r, b.i, a.i * b.r)}; }
DEV cf csq(cf a) { return cf{fmaf(a.r, a.r, -(a.i * a.i)), (a.r + a.r) * a.i}; }
DEV cf cexp(float lr, float t) {
    const float e = __builtin_amdgcn_exp2f(lr), f = __builtin_amdgcn_fractf(t);
    return cf{e * __builtin_amdgcn_cosf(f), e * __builtin_amdgcn_sinf(f)};
}

DEV u32 xt_part(const Params& P, lptr L, int cl, int lane, int rt, int hp) {
    const u32 tag = mk_tag(get_epoch(L));
    lptr p = L + L_XS + lane * 80 + cl * 24 + (hp ? 8 : 0);
    u32x2 ev[3], od[3];
#pragma unroll
    for (int e = 0; e < 3; ++e) { ev[e] = *(LAS u32x2*)(p + e * 8); od[e] = *(LAS u32x2*)(p + 5120 + e * 8); }
#pragma unroll
    for (int j = 0; j < 4; ++j) {
        u32 d[3];
#pragma unroll
        for (int c = 0; c < 3; ++c) {
            const int q = 3 * j + c;
            d[c] = __builtin_amdgcn_perm(ev[q >> 2][(q >> 1) & 1], od[q >> 2][(q >> 1) & 1], (q & 1) ? 0x07060302u : 0x05040100u);
        }
        st16(P.rs, seal(d[0], d[1], d[2], tag), lane * 16, WS_XT + (hp * 12 + cl * 4 + j) * 16384 + (15 - rt) * 1024);
    }
    return tag;
}

DEV u32 kgen_role(Params& P, lptr L, int cl, int ch, int lane, int rt, int hp) {
    const int m = lane & 15, kg = lane >> 4;
    lptr C = L + L_CH + cl * CH_BYTES;
    const int n = ch * 64 + lane;
    P.arrive<2>();
    const u32 are = ((const GAS u32*)P.Are)[n], aim = ((const GAS u32*)P.Aim)[n], bre = ((const GAS u32*)P.Bre)[n];
    const u32 bim = ((const GAS u32*)P.Bim)[n], cre = ((const GAS u32*)P.Cre)[n], cim = ((const GAS u32*)P.Cim)[n];
    const u16 ldt = ((const GAS u16*)P.logdt)[ch], dsk = ((const GAS u16*)P.Dskip)[ch];
    SCHED();
    RAW_BARRIER();
    u32 ga[2][20];
#pragma unroll
    for (int it = 0; it < 2; ++it) {
        const int l0 = (it * 64 + lane) * 16 - 4;
#pragma unroll
        for (int j = 0; j < 10; ++j) {
            const int la = l0 + 2 * j, lb = la + 1;
            const u32 bits = pk2((float)la, (float)lb);
            int ia = (int)(bits & 0xffff) - 0x4280, ib = (int)(bits >> 16) - 0x4280;
            if (it == 0) {
                ia = la < 256 ? (la < 0 ? 641 : la) : ia;
                ib = lb < 256 ? (lb < 0 ? 641 : lb) : ib;
            }
            ga[it][2 * j] = L_KT + cl * 1312 + 2 * ia;
            ga[it][2 * j + 1] = L_KT + cl * 1312 + 2 * ib;
        }
    }
#pragma unroll
    for (int it = 0; it < 2; ++it)
#pragma unroll
        for (int j = 0; j < 20; ++j) asm volatile("" : "+v"(ga[it][j]));
    SCHED();
    const u32 tag = xt_part(P, L, cl, lane, rt, hp);
    SCHED();
    const float dt = lo2f(pk2(expf(bf2f(ldt)), 0.f));
    STAMP(1);
    f32x4 a0 = {lane == 0 ? bf2f(dsk) : 0.f, 0, 0, 0}, a1 = {0, 0, 0, 0}, a2 = a1;
    LAS u32* T = (LAS u32*)(C + CH_KC + (lane >> 2) * TQ + (lane & 3) * 4);
    lptr R = C + CH_KC + kg * TQ + m * 16;
#pragma unroll
    for (int u = 0; u < 2; ++u) {
        auto hf = [&](u32 v) { return u ? hi2f(v) : lo2f(v); };
        const float lr = dt * hf(are) * LOG2E, t = dt * hf(aim) * 0.15915494309189533577f;
        const cf c = cmul(cf{hf(cre), hf(cim)}, cf{dt * hf(bre), dt * hf(bim)});
        cf p[16], A[9];
        p[1] = cexp(lr, t);
        const cf w16 = cexp(16.f * lr, 16.f * t);
        A[1] = cexp(256.f * lr, 256.f * t);
        auto put = [&](int row, cf v) { T[row * 4] = pk2s(v.r, v.i); };
        T[0] = 0x3f80u;
        put(1, p[1]);
#pragma unroll
        for (int k = 2; k < 16; ++k) {
            p[k] = (k & 1) ? cmul(p[k - 1], p[1]) : csq(p[k / 2]);
            put(k, p[k]);
        }
        {
            const cf w32 = csq(w16);
            cf h0 = cf{c.r, -c.i}, h1 = cmul(c, w16);
            h1.i = -h1.i;
            const cf wc = cf{w32.r, -w32.i};
#pragma unroll
            for (int k = 0; k < 8; ++k) {
                put(41 + 2 * k, h0);
                put(42 + 2 * k, h1);
                if (k < 7) { h0 = cmul(h0, wc); h1 = cmul(h1, wc); }
            }
        }
        A[2] = csq(A[1]); A[3] = cmul(A[2], A[1]); A[4] = csq(A[2]);
        A[5] = cmul(A[4], A[1]); A[6] = csq(A[3]); A[7] = cmul(A[4], A[3]); A[8] = csq(A[4]);
#pragma unroll
        for (int k = 0; k < 8; ++k) put(16 + k, k ? cmul(A[1], p[2 * k]) : A[1]);
#pragma unroll
        for (int k = 0; k < 8; ++k) put(24 + k, (k & 3) ? cmul(A[2 + (k >> 2)], p[4 * (k & 3)]) : A[2 + (k >> 2)]);
#pragma unroll
        for (int k = 0; k < 8; ++k) put(32 + k, (k & 1) ? cmul(A[4 + (k >> 1)], p[8]) : A[4 + (k >> 1)]);
        put(40, A[8]);
        CFENCE();
#pragma unroll
        for (int s = 0; s < 4; ++s) {
            const u32x4 H = *(LAS u32x4*)(R + s * 4 * TQ + 41 * 16);
            a0 = mfma32(*(LAS u32x4*)(R + s * 4 * TQ), H, a0);
            a1 = mfma32(*(LAS u32x4*)(R + s * 4 * TQ + 256), H, a1);
            a2 = mfma32(*(LAS u32x4*)(R + s * 4 * TQ + 512), H, a2);
        }
        CFENCE();
    }
    STAMP(2);
    {
        lptr KT = L + L_KT + cl * 1312;
        *(LAS u32x2*)(KT + 2 * (16 * m + 4 * kg)) = u32x2{pk2(a0[0], a0[1]), pk2(a0[2], a0[3])};
        *(LAS u32x2*)(KT + 2 * (kg < 2 ? 256 + 8 * m + 4 * kg : 256 + 64 * kg + 4 * m)) = u32x2{pk2(a1[0], a1[1]), pk2(a1[2], a1[3])};
        if (kg < 2) {
            *(LAS u32*)(KT + 2 * (512 + 64 * kg + 2 * m)) = pk2(a2[0], a2[1]);
            *(LAS u32*)(KT + 2 * (544 + 64 * kg + 2 * m)) = pk2(a2[2], a2[3]);
        }
        if (lane == 32) *(LAS u32*)(KT + 2 * 640) = pk2(a2[0], 0.f);
        CFENCE();
    }
#pragma unroll
    for (int it = 0; it < 2; ++it) {
        u32 v[20], d[10], e[9];
#pragma unroll
        for (int j = 0; j < 20; ++j) v[j] = *(LAS u16*)(L + ga[it][j]);
#pragma unroll
        for (int j = 1; j < 10; ++j) d[j] = v[2 * j] | (v[2 * j + 1] << 16);
#pragma unroll
        for (int j = 0; j < 9; ++j) e[j] = v[2 * j + 1] | (v[2 * j + 2] << 16);
        lptr W = C + CH_KC + 32 + (it * 64 + lane) * 32;
        *(LAS u32x4*)(W) = u32x4{d[2], d[3], d[4], d[5]};
        *(LAS u32x4*)(W + 16) = u32x4{d[6], d[7], d[8], d[9]};
        *(LAS u32x4*)(W + CH_KSTRIDE) = u32x4{e[1], e[2], e[3], e[4]};
        *(LAS u32x4*)(W + CH_KSTRIDE + 16) = u32x4{e[5], e[6], e[7], e[8]};
        *(LAS u32x4*)(W + 2 * CH_KSTRIDE) = u32x4{d[1], d[2], d[3], d[4]};
        *(LAS u32x4*)(W + 2 * CH_KSTRIDE + 16) = u32x4{d[5], d[6], d[7], d[8]};
        *(LAS u32x4*)(W + 3 * CH_KSTRIDE) = u32x4{e[0], e[1], e[2], e[3]};
        *(LAS u32x4*)(W + 3 * CH_KSTRIDE + 16) = u32x4{e[4], e[5], e[6], e[7]};
    }
    if (lane < 8) *(LAS u32x4*)(C + CH_KC + (lane >> 1) * CH_KSTRIDE + (lane & 1) * 16) = u32x4{0, 0, 0, 0};
    return tag;
}

template <int MASK, int NA>
DEV void conv_blocks(rsrc_t rs, lptr C, int ch, int lane, u32 tag) {
    const int m = lane & 15, kg = lane >> 4;
    f32x4 acc[8];
#pragma unroll
    for (int i = 0; i < 8; ++i) acc[i] = f32x4{0, 0, 0, 0};
    lptr KA = C + CH_KC + ((~m) & 3) * CH_KSTRIDE + 2 * (8 * kg + ((m + 4) & ~3));
    lptr XB = C + CH_XR + 2 * (240 - 16 * m + 8 * kg);
    u32x4 A[2][NA], B[2][NA];
    auto rd = [&](int j, int a) {
        A[j & 1][a] = cat(*(volatile LAS u32x2*)(KA + 64 * j + 512 * a), *(volatile LAS u32x2*)(KA + 64 * j + 512 * a + 8));
        B[j & 1][a] = *(LAS u32x4*)(XB + 64 * j + 512 * (7 - a));
    };
#pragma unroll
    for (int a = 0; a < NA; ++a) rd(0, a);
    SCHED();
#pragma unroll
    for (int j = 0; j < 8; ++j) {
        int n = 0;
#pragma unroll
        for (int I = 0; I < 8; ++I)
            if (MASK >> I & 1)
#pragma unroll
                for (int a = 0; a <= I; ++a) {
                    acc[I] = mfma32(A[j & 1][a], B[j & 1][I - a], acc[I]);
                    if (j < 7 && !(n & 1) && (n >> 1) < NA) rd(j + 1, n >> 1);
                    ++n;
                    SCHED();
                }
    }
    const u32 vo = (m >> 1) * 73728 + kg * 32 + (m & 1) * 16;
#pragma unroll
    for (int I = 0; I < 8; ++I)
        if (MASK >> I & 1) st16(rs, seal(pk2(acc[I][0], acc[I][1]), pk2(acc[I][2], acc[I][3]), 0u, tag), vo, WS_BT + I * 8 * 73728 + ch * 128);
}

DEV void xstage_role(const Params& P, rsrc_t rs, lptr L, int lane, int sg, u32 tag) {
    lptr XR = L + L_CH + CH_XR + lane * 4;
    if (lane < 32) {
#pragma unroll
        for (int c = 0; c < 3; ++c) *(LAS u32x4*)(L + L_CH + c * CH_BYTES + CH_XR + 4096 + lane * 16) = u32x4{0, 0, 0, 0};
    }
    u32x4 g[16];
#pragma unroll
    for (int i = 0; i < 16; ++i) g[i] = ld16(rs, lane * 16, WS_XT + sg * 16384 + i * 1024);
    poll_rows<16>(g, rs, lane * 16, WS_XT + sg * 16384, 1024, tag);
#pragma unroll
    for (int i = 0; i < 16; ++i)
#pragma unroll
        for (int c = 0; c < 3; ++c) *(LAS u32*)(XR + c * CH_BYTES + i * 256) = g[i][c];
}

DEV void gate_role(const Params& P, rsrc_t rs, int lane, int rb, int ps, u32 tag) {
    const GAS char* gw = (const GAS char*)P.gw + (lane & 3) * 16;
    {
        const int row = rb * 32 + ps * 16 + (lane >> 2);
        const GAS char* xr = (const GAS char*)P.x + row * 1152 + (lane & 3) * 16;
        u32x4 xv[18], gv[18];
#pragma unroll
        for (int i = 0; i < 18; ++i) { xv[i] = *(const GAS u32x4*)(xr + i * 64); gv[i] = *(const GAS u32x4*)(gw + i * 64); }
        float s0 = 0.f, s1 = 0.f;
#pragma unroll
        for (int i = 0; i < 18; ++i)
#pragma unroll
            for (int j = 0; j < 4; ++j) {
                s0 = fmaf(lo2f(xv[i][j]), lo2f(gv[i][j]), s0);
                s1 = fmaf(hi2f(xv[i][j]), hi2f(gv[i][j]), s1);
            }
        float s = s0 + s1;
        s += __shfl_xor(s, 1);
        s += __shfl_xor(s, 2);
        const u32 g = __float_as_uint(1.0f / (1.0f + __expf(-s)));
        if ((lane & 3) == 0) __builtin_amdgcn_raw_buffer_store_b64(u32x2{g, g ^ tag}, rs, WS_G + row * 8, 0, AUX_SC1);
    }
}

DEV u32 wf_role(Params& P, lptr L, u32 lbase, int cl, int lane, int unit, int rt, int hp) {
    const int ks = unit / 9, gp = unit - ks * 9;
    P.arrive<3>();
    const rsrc_t rs = P.rs;
    if (unit < 162) {
        const char* src = (const char*)P.wo + ks * 32 * 1152 + gp * 128;
#pragma unroll
        for (int i = 0; i < 4; ++i) glds16((u32)((lane >> 3) * 1152 + (lane & 7) * 16), src + i * 8 * 1152, lbase + L_CH + cl * CH_BYTES + i * 1040);
    }
    RAW_BARRIER();
    if (unit >= 162) return xt_part(P, L, cl, lane, rt, hp);
    WAIT_V(0);
    lptr S = L + L_CH + cl * CH_BYTES + (lane >> 4) * 1040 + ((lane >> 2) & 3) * 128;
#pragma unroll
    for (int h = 0; h < 2; ++h) {
        const int grp = 2 * gp + h;
        const bool sp = grp == 4 || grp == 13;
        lptr p = S + h * 64 + (lane & 3) * (sp ? 8 : 16);
#pragma unroll
        for (int t = 0; t < 2; ++t) {
            lptr q = p + t * (sp ? 32 : 8);
            st16(rs, cat(trread(q), trread(q + 512)), lane * 16, WS_WF + ((grp * 18 + ks) * 2 + t) * 1024);
        }
    }
    SCHED();
    const u32 tag = xt_part(P, L, cl, lane, rt, hp);
    WAIT_V(4);
    if (lane == 0) __builtin_amdgcn_raw_buffer_store_b32(tag, rs, WS_WFF + unit * 4, 0, AUX_SC1);
    return tag;
}

__global__ __launch_bounds__(512, 2) void fused(Args A) {
    Params P;
    P.fetch();
    extern __shared__ __attribute__((aligned(64))) char smem[];
    lptr L = (lptr)smem;
    const u32 lbase = (u32)(uintptr_t)L;
    const int tid = threadIdx.x, lane = tid & 63, w = __builtin_amdgcn_readfirstlane(tid >> 6);
    const int bid = blockIdx.x, xcd = bid & 7, slot = bid >> 3;
    if (tid < 16) *(LAS u32*)(L + L_FLAG + tid * 4) = 0;
    const int rt = (xcd >> 1) * 4 + (slot & 3), hp = (xcd & 1) * 8 + (slot >> 2);
    const int rb = 8 * xcd + (slot & 7), cq = slot >> 3, cg = w & 3;
    if (w < 4) {
        gemm_role(P, L, lbase, w, lane, rt, hp, rb, cq);
        if (bid == 0 && tid == 0) {
            const u32 ep = *(volatile LAS u32*)(L + L_FLAG + 32);
            __builtin_amdgcn_raw_buffer_store_b32(ep + 1, P.rs, WS_EPOCH, 0, AUX_SC1);
        }
    } else if (w == 7) {
        const u32 tag = loader_role(P, L, lbase, lane, rt, hp);
        if (slot < 24) {
            const int sg = xcd * 24 + slot;
            xstage_role(P, P.rs, L, lane, sg, tag);
            flag_set(L, 7, 1);
            STAMP(3);
#pragma nounroll
            for (int c = 0; c < 3; ++c) {
                flag_wait1(L, 4 + c, 1);
                STAMP(4);
                conv_blocks<0x0E, 4>(P.rs, L + L_CH + c * CH_BYTES, 3 * sg + c, lane, tag);
            }
            STAMP(5);
        }
    } else if (slot < 24) {
        const int sg = xcd * 24 + slot;
        const u32 tag = kgen_role(P, L, cg, 3 * sg + cg, lane, rt, hp);
        flag_set(L, w, 1);
        STAMP(3);
        flag_wait1(L, 7, 1);
        STAMP(4);
        conv_blocks<0xF1, 8>(P.rs, L + L_CH + cg * CH_BYTES, 3 * sg + cg, lane, tag);
        STAMP(5);
    } else {
        const u32 tag = wf_role(P, L, lbase, cg, lane, ((slot - 24) * 8 + xcd) * 3 + cg, rt, hp);
        STAMP(3);
        if (cg < 2) gate_role(P, P.rs, lane, rb, cg, tag);
        STAMP(4);
    }
}

extern "C" void kernel_launch(void* const* d_in, const int* in_sizes, int n_in, void* d_out, int out_size, void* d_ws,
                              size_t ws_size, hipStream_t stream) {
    Args P;
    P.x = (const u16*)d_in[0];
    P.wqkv = (const u16*)d_in[1];
    P.wo = (const u16*)d_in[2];
    P.Are = (const u16*)d_in[4];
    P.Aim = (const u16*)d_in[5];
    P.Bre = (const u16*)d_in[6];
    P.Bim = (const u16*)d_in[7];
    P.Cre = (const u16*)d_in[8];
    P.Cim = (const u16*)d_in[9];
    P.logdt = (const u16*)d_in[10];
    P.Dskip = (const u16*)d_in[11];
    P.gw = (const u16*)d_in[12];
    P.ws = (char*)d_ws;
    P.out = (u16*)d_out;
    (void)hipFuncSetAttribute((const void*)fused, hipFuncAttributeMaxDynamicSharedMemorySize, LDS_BYTES);
    fused<<<256, 512, LDS_BYTES, stream>>>(P);
}
```

```cpp
#include <hip/hip_runtime.h>
#include <hip/hip_bf16.h>
#include <stdint.h>

typedef unsigned short u16;
typedef unsigned int u32;
typedef unsigned long long u64;
typedef u32 u32x4 __attribute__((ext_vector_type(4)));
typedef u32 u32x2 __attribute__((ext_vector_type(2)));
typedef float f32x4 __attribute__((ext_vector_type(4)));
typedef float f32x2 __attribute__((ext_vector_type(2)));
typedef __bf16 bf16x8 __attribute__((ext_vector_type(8)));
typedef __bf16 bf16x2 __attribute__((ext_vector_type(2)));
typedef short s16x4 __attribute__((ext_vector_type(4)));
#define LAS __attribute__((address_space(3)))
#define GAS __attribute__((address_space(1)))
#define DEV __device__ __forceinline__
typedef LAS char* lptr;
typedef __amdgpu_buffer_rsrc_t rsrc_t;

constexpr u32 WS_EPOCH = 0;
constexpr u32 WS_WFF = 256;
constexpr u32 WS_G = 4096;
constexpr u32 WS_KVG = 65536;
constexpr u32 WS_WF = WS_KVG + 524288;
constexpr u32 WS_XT = WS_WF + 663552;
constexpr u32 WS_OG = WS_XT + 3145728;
constexpr u32 WS_BT = WS_OG + 3145728;
constexpr u32 WS_DBG = WS_BT + 4718592;
constexpr u32 WS_END = WS_DBG + 1048576;
constexpr int AUX_SC1 = 16;
#define EXP 0
#define SPIN_LOG 17
constexpr u32 SPIN_MAX = 1u << SPIN_LOG;

constexpr u32 L_FLAG = 0;
constexpr u32 CH_KC = 0, CH_KSTRIDE = 4160, CH_XR = 16640, CH_BYTES = 21248;
constexpr u32 L_CH = 64;
constexpr u32 L_KT = L_CH + 3 * CH_BYTES;
constexpr u32 L_XRING = L_KT + 3 * 1312;
constexpr u32 L_WRING = L_XRING + 49152;
constexpr u32 L_AIMG = L_WRING + 30720;
constexpr u32 LDS_BYTES = L_AIMG + 5632;
constexpr u32 L_KVX = L_WRING;
constexpr u32 TQ = 944;
static_assert(LDS_BYTES <= 163840, "lds");
static_assert(L_XRING % 32 == 0, "align");

DEV float bf2f(u16 v) { return __uint_as_float(((u32)v) << 16); }
DEV u32 pk2(float a, float b) {
    f32x2 v = {a, b};
    return __builtin_bit_cast(u32, __builtin_convertvector(v, bf16x2));
}
DEV u32 pk2s(float a, float b) {
    u32 r;
    asm("v_cvt_pk_bf16_f32 %0, %1, %2" : "=v"(r) : "v"(a), "v"(b));
    return r;
}
DEV float lo2f(u32 v) { return __uint_as_float(v << 16); }
DEV float hi2f(u32 v) { return __uint_as_float(v & 0xffff0000u); }
DEV f32x4 mfma32(u32x4 a, u32x4 b, f32x4 c) {
    return __builtin_amdgcn_mfma_f32_16x16x32_bf16(__builtin_bit_cast(bf16x8, a), __builtin_bit_cast(bf16x8, b), c, 0, 0, 0);
}
DEV void glds16(u32 voff, const void* sbase, u32 ldsaddr) {
    asm volatile("s_mov_b32 m0, %2\n\ts_nop 0\n\tglobal_load_lds_dwordx4 %0, %1" ::"v"(voff), "s"(sbase), "s"(ldsaddr) : "memory");
}
DEV void glds16x4(u32 v0, u32 v1, u32 v2, u32 v3, const void* sbase, u32 ldsaddr) {
    asm volatile("s_mov_b32 m0, %5\n\ts_nop 0\n\tglobal_load_lds_dwordx4 %0, %4\n\tglobal_load_lds_dwordx4 %1, %4 offset:1024\n\t"
                 "global_load_lds_dwordx4 %2, %4 offset:2048\n\tglobal_load_lds_dwordx4 %3, %4 offset:3072" ::"v"(v0), "v"(v1), "v"(v2), "v"(v3),
                 "s"(sbase), "s"(ldsaddr) : "memory");
}
DEV u32x2 trread(lptr p) { return __builtin_bit_cast(u32x2, __builtin_amdgcn_ds_read_tr16_b64_v4i16((LAS s16x4*)p)); }
DEV u32x4 cat(u32x2 a, u32x2 b) { return u32x4{a[0], a[1], b[0], b[1]}; }
DEV u32x4 ld16(rsrc_t rs, u32 voff, u32 soff) { return __builtin_amdgcn_raw_buffer_load_b128(rs, voff, soff, AUX_SC1); }
DEV void st16(rsrc_t rs, u32x4 v, u32 voff, u32 soff) { __builtin_amdgcn_raw_buffer_store_b128(v, rs, voff + soff, 0, AUX_SC1); }
DEV u32 xor3(u32 a, u32 b, u32 c) { return __builtin_amdgcn_bitop3_b32(a, b, c, 0x96); }
DEV u32x4 seal(u32 a, u32 b, u32 c, u32 tag) { return u32x4{a, b, c, xor3(a, b, tag) ^ c}; }
DEV u32 unseal(u32x4 g, u32 tag) { return xor3(xor3(g[0], g[1], tag), g[2], g[3]); }
#define WAIT_V(n) asm volatile("s_waitcnt vmcnt(%0)" ::"n"(n) : "memory")
#define WAIT_L(n) asm volatile("s_waitcnt lgkmcnt(%0)" ::"n"(n) : "memory")
#define WAIT_L0() asm volatile("s_waitcnt lgkmcnt(0)" ::: "memory")
#define RAW_BARRIER() asm volatile("s_waitcnt lgkmcnt(0)\n\ts_barrier" ::: "memory")
#define CFENCE() asm volatile("" ::: "memory")
#define SCHED() __builtin_amdgcn_sched_barrier(0)

#define STAMP(i) do {} while (0)
#define STAMPD(i) do {} while (0)
#define STAMPC(i) do {} while (0)

DEV void flag_set(lptr L, int wave, u32 v) {
    WAIT_L0();
    *(volatile LAS u32*)(L + L_FLAG + wave * 4) = v;
}
DEV void flag_wait1(lptr L, int wave, u32 v) {
#pragma nounroll
    for (u32 it = 0; it < SPIN_MAX * 8; ++it) {
        u32 f = *(volatile LAS u32*)(L + L_FLAG + wave * 4);
        if (__builtin_amdgcn_readfirstlane(f) >= v) break;
        __builtin_amdgcn_s_sleep(1);
    }
    CFENCE();
}
DEV bool quad_ok(u32x4 f, u32 v) {
    u32 m = min(min(f[0], f[1]), min(f[2], f[3]));
    return __builtin_amdgcn_readfirstlane(m) >= v;
}
DEV void quad_wait(lptr Q, u32 v) {
#pragma nounroll
    for (u32 it = 0; it < SPIN_MAX * 8; ++it) {
        u32x4 f = *(volatile LAS u32x4*)(Q);
        if (quad_ok(f, v)) break;
        __builtin_amdgcn_s_sleep(1);
    }
    CFENCE();
}
DEV void quad_spin(lptr Q, u32 v) {
#pragma nounroll
    for (u32 it = 0; it < SPIN_MAX * 16; ++it) {
        u32x4 f = *(volatile LAS u32x4*)(Q);
        if (quad_ok(f, v)) break;
        if (EXP & 8) __builtin_amdgcn_s_sleep(1);
    }
    CFENCE();
}
DEV u32 mk_tag(u32 epoch) { return epoch * 0x9E3779B1u + 0x7F4A7C15u; }
DEV void put_epoch(lptr L, u32 epoch) { *(volatile LAS u32x2*)(L + L_FLAG + 32) = u32x2{epoch, 1u}; }
DEV u32 get_epoch(lptr L) {
    u32x2 v;
#pragma nounroll
    for (u32 it = 0; it < SPIN_MAX * 8; ++it) {
        v = *(volatile LAS u32x2*)(L + L_FLAG + 32);
        if (__builtin_amdgcn_readfirstlane(v[1])) break;
        __builtin_amdgcn_s_sleep(1);
    }
    CFENCE();
    return __builtin_amdgcn_readfirstlane(v[0]);
}
template <int N>
DEV bool rows_bad(u32x4 (&g)[N], u32 tag) {
    u32 bad = 0;
#pragma unroll
    for (int i = 0; i < N; ++i) bad |= unseal(g[i], tag);
    return __any(bad != 0);
}
template <int N>
DEV void poll_rows(u32x4 (&g)[N], rsrc_t rs, u32 voff, u32 soff, u32 stride, u32 tag) {
    bool bad = rows_bad(g, tag);
#pragma nounroll
    for (u32 it = 0; __builtin_expect(bad, 0) && it < SPIN_MAX; ++it) {
#pragma unroll
        for (int i = 0; i < N; ++i)
            if (__any(unseal(g[i], tag) != 0))
                asm volatile("buffer_load_dwordx4 %0, %1, %2, %3 offen sc1" : "+v"(g[i]) : "v"(voff), "s"(rs), "s"(soff + i * stride) : "memory");
        WAIT_V(0);
#pragma unroll
        for (int i = 0; i < N; ++i) asm volatile("" : "+v"(g[i]));
        bad = rows_bad(g, tag);
    }
    CFENCE();
}
#define PIN(x) asm volatile("" : "+v"(x))

struct Args {
    const u16 *x, *wqkv, *wo, *Are, *Aim, *Bre, *Bim, *Cre, *Cim, *logdt, *Dskip, *gw;
    char* ws;
    u16* out;
};
typedef u64 u64x8 __attribute__((ext_vector_type(8)));
typedef u64 u64x4 __attribute__((ext_vector_type(4)));
typedef u64 u64x2 __attribute__((ext_vector_type(2)));
struct Params : Args {
    u64x8 k0; u64x4 k1; u64x2 k2;
    rsrc_t rs;
    DEV void fetch() {
        asm volatile("s_load_dwordx16 %0, %3, 0x0\n\ts_load_dwordx8 %1, %3, 0x40\n\ts_load_dwordx4 %2, %3, 0x60"
                     : "=&s"(k0), "=&s"(k1), "=&s"(k2) : "s"(__builtin_amdgcn_kernarg_segment_ptr()));
    }
    DEV static u64 take(u64 v) {
        u64 r;
        asm volatile("s_mov_b64 %0, %1" : "=s"(r) : "s"(v));
        return r;
    }
    template <int ROLE>
    DEV void arrive() {
        asm volatile("s_waitcnt lgkmcnt(0)" ::"s"(k0), "s"(k1), "s"(k2));
        if (ROLE != 2) x = (const u16*)take(k0[0]);
        if (ROLE < 2) wqkv = (const u16*)take(k0[1]);
        if (ROLE == 3) { wo = (const u16*)take(k0[2]); gw = (const u16*)take(k1[3]); }
        if (ROLE == 2) {
            Are = (const u16*)take(k0[3]); Aim = (const u16*)take(k0[4]); Bre = (const u16*)take(k0[5]); Bim = (const u16*)take(k0[6]);
            Cre = (const u16*)take(k0[7]); Cim = (const u16*)take(k1[0]); logdt = (const u16*)take(k1[1]); Dskip = (const u16*)take(k1[2]);
        }
        ws = (char*)take(k2[0]);
        if (ROLE == 0) out = (u16*)take(k2[1]);
        rs = __builtin_amdgcn_make_buffer_rsrc((void*)ws, 0, (int)WS_END, 0x00020000);
    }
};
DEV void pos5(u32 idx, u32& pos, u32& piece) {
    pos = (idx * 52429u) >> 18;
    piece = idx - 5 * pos;
}
DEV u32 pos2row(u32 pos) { return (pos & 56) | ((pos >> 1) & 3) | ((pos & 1) << 2); }

DEV int first_chunk(int hp) { return (9 * hp) >> 4; }
DEV u32 loader_role(Params& P, lptr L, u32 lbase, int lane, int rt, int hp) {
    u32 wv[5], xv[4];
#pragma unroll
    for (int p = 0; p < 5; ++p) {
        u32 pos, piece;
        pos5(p * 64 + lane, pos, piece);
        wv[p] = pos2row(pos) * 3456 + piece * 16 - (p & 3) * 1024;
    }
#pragma unroll
    for (int j = 0; j < 4; ++j) xv[j] = (u32)((lane >> 3) * 1152 + (((lane & 7) ^ (lane >> 3)) << 4)) + j * 8192;
    int c = first_chunk(hp);
    P.arrive<1>();
    u32 epoch;
    asm volatile("global_load_dword %0, %1, %2 sc1" : "=v"(epoch) : "v"(WS_EPOCH), "s"(P.ws) : "memory");
    const char* xg = (const char*)P.x + rt * 128 * 1152;
    const char* wg = (const char*)P.wqkv + 72 * hp;
    auto five = [&](u32(&v)[5], const char* src, u32 dst) {
        glds16x4(v[0], v[1], v[2], v[3], src, dst);
        glds16(v[4], src, dst + 4096);
    };
    const int c0 = c;
    int k = 0;
    auto next = [&]() {
        k = k == 1 ? 2 + 2 * (rt & 3) : (k == 8 ? 2 : k + 1);
        c = c0 + k >= 9 ? c0 + k - 9 : c0 + k;
    };
    auto ldx = [&](u32 sl) {
#pragma unroll
        for (int w = 0; w < 4; ++w) glds16x4(xv[0], xv[1], xv[2], xv[3], xg + c * 128 + w * 36864, lbase + L_XRING + sl * 16384 + w * 4096);
    };
    auto ldw = [&](u32 sl) {
        five(wv, wg + c * 221184 + 1144, lbase + L_WRING + sl * 10240);
        five(wv, wg + c * 221184 + 2296, lbase + L_WRING + sl * 10240 + 5120);
    };
    volatile LAS u32* FULL = (volatile LAS u32*)(L + L_FLAG + 40);
    next();
    ldx(1);
    ldw(1);
    RAW_BARRIER();
    __builtin_amdgcn_s_setprio(3);
    asm volatile("s_waitcnt vmcnt(26)" : "+v"(epoch)::"memory");
    const u32 ep = __builtin_amdgcn_readfirstlane(epoch);
    put_epoch(L, ep);
    STAMP(1);
    if (EXP & 1) { WAIT_V(0); __builtin_amdgcn_s_setprio(0); STAMP(2); return mk_tag(ep); }
    next();
    ldx(2);
    WAIT_V(16);
    *FULL = 2;
    ldw(2);
    u32 sl = 0;
#pragma nounroll
    for (u32 j = 3; j < 9; ++j) {
        next();
        if (!(EXP & 2)) quad_spin(L + L_FLAG, 2 * j - 4);
        ldx(sl);
        if (j == 3) { WAIT_V(16); *FULL = 3; }
        if (!(EXP & 2)) quad_spin(L + L_FLAG, 2 * j - 3);
        ldw(sl);
        WAIT_V(26);
        *FULL = j;
        sl = sl == 2 ? 0 : sl + 1;
    }
    k = -1;
#pragma nounroll
    for (u32 s = 0; s < 3; ++s) {
        if (!(EXP & 2)) quad_spin(L + L_FLAG, 14 + 2 * s);
#pragma unroll
        for (int i = 0; i < 3; ++i) {
            next();
            five(wv, wg + c * 221184, lbase + L_XRING + s * 16384 + i * 5120);
        }
        if (s == 0) {
            if (!(EXP & 2)) quad_spin(L + L_FLAG, 15);
            WAIT_V(15);
            *FULL = 9;
        }
    }
    WAIT_V(0);
    *FULL = 10;
    __builtin_amdgcn_s_setprio(0);
    STAMP(2);
    return mk_tag(ep);
}

constexpr float LOG2E = 1.44269504088896340736f;
constexpr float EPS = 1e-6f;
constexpr float ONE_EPS = 1.0f + 1e-6f;
DEV float phi(float a) {
    float e = __builtin_amdgcn_exp2f(fmaf(a, LOG2E, -ONE_EPS * LOG2E));
    return __builtin_amdgcn_fmed3f(e + EPS, a, ONE_EPS);
}

DEV bool wf_ok(u32x4 f, int lane, u32 tag) { return __all(f[0] == tag && f[1] == tag && (lane >= 40 || (f[2] == tag && f[3] == tag))); }

DEV void gemm_role(Params& P, lptr L, u32 lbase, int w, int lane, int rt, int hp, int rb, int cq) {
    const int m = lane & 15, kg = lane >> 4;
    {
        const u32 xo = (u32)((lane >> 3) * 1152 + (((lane & 7) ^ (lane >> 3)) << 4));
        u32 wv[3];
#pragma unroll
        for (int j = 0; j < 3; ++j) {
            u32 pos, piece;
            pos5((w + 4 * j) * 64 + lane, pos, piece);
            wv[j] = pos2row(pos) * 3456 + (pos >> 6) * 1152 + piece * 16;
        }
        const int c0 = first_chunk(hp);
        P.arrive<0>();
        STAMP(40);
        glds16x4(xo, xo + 8192, xo + 16384, xo + 24576, (const char*)P.x + (rt * 128 + w * 32) * 1152 + c0 * 128, lbase + L_XRING + w * 4096);
        const char* wsrc = (const char*)P.wqkv + 72 * hp + c0 * 221184 + 1144;
        glds16(wv[0], wsrc, lbase + L_WRING + w * 1024);
        glds16(wv[1], wsrc, lbase + L_WRING + w * 1024 + 4096);
        if (w < 2) glds16(wv[2], wsrc, lbase + L_WRING + w * 1024 + 8192);
    }
    const rsrc_t rs = P.rs;
    STAMP(41);
    RAW_BARRIER();
    STAMP(42);
    if (EXP & 3) return;
    __builtin_amdgcn_s_setprio(2);
    *(LAS u32x4*)(L + L_AIMG + w * 1024 + lane * 16) = u32x4{0, 0, 0, 0};
    if (lane < 48) *(LAS u32x2*)(L + L_AIMG + 4096 + w * 384 + lane * 8) = u32x2{0, 0};
    f32x4 acc[2][5];
    const bool kl = !(m & 4);
#pragma unroll
    for (int ct = 0; ct < 5; ++ct) {
        float iv = ct < 2 ? ONE_EPS : (ct < 4 ? 0.f : (kl ? ONE_EPS : 0.f));
        acc[0][ct] = f32x4{iv, iv, iv, iv};
        acc[1][ct] = acc[0][ct];
    }
    lptr XA0 = L + L_XRING + w * 4096 + m * 128 + ((kg ^ (m & 7)) << 4);
    lptr XA1 = L + L_XRING + w * 4096 + m * 128 + (((4 + kg) ^ (m & 7)) << 4);
    const int ra = (8 * kg + 2 * ((lane >> 2) & 3)) * 80;
    lptr WA = L + L_WRING + ra + (lane & 3) * 8 + 8;
    lptr WL = L + L_WRING + ra + (lane & 1) * 5120 + 40;
    volatile LAS u32* FULL = (volatile LAS u32*)(L + L_FLAG + 40);
    volatile LAS u32* FREE = (volatile LAS u32*)(L + L_FLAG + w * 4);
    u32x4 X[18][2];
    u32x2 B[2][5][2];
    auto rdx = [&](int i, int e) {
        X[2 * i + (e >> 1)][e & 1] = *(LAS u32x4*)(((e >> 1) ? XA1 : XA0) + (i % 3) * 16384 + (e & 1) * 2048);
    };
    auto rdb = [&](int i, int ks, int q) {
        const int o = (i % 3) * 10240 + ks * 2560;
        lptr p = q == 2 ? WL + o : WA + o + (q >= 3 ? 5120 : 0) + ((q == 1 || q == 4) ? 40 : 0);
        B[ks][q][0] = trread(p);
        B[ks][q][1] = trread(p + 80);
    };
    auto mm = [&](int k, int s) {
        constexpr int ord[5] = {0, 1, 4, 2, 3};
        const int q = s >> 1, r = s & 1;
        acc[r][ord[q]] = mfma32(X[k][r], cat(B[k & 1][q][0], B[k & 1][q][1]), acc[r][ord[q]]);
    };
    lptr XTA[3];
    u32 xts = WS_XT + (hp * 12 + min(kg, 2) * 4) * 16384 + (1008 - 64 * rt - 16 * w + m) * 16;
#pragma unroll
    for (int e = 0; e < 3; ++e) {
        const int B = ((72 * hp) & 127) + 24 * min(kg, 2) + 8 * e, r = 30 - 2 * m;
        XTA[e] = L + L_XRING + (B >> 7) * 16384 + w * 4096 + r * 128 + ((((B >> 4) ^ r) & 7) << 4) + (B & 8);
        PIN(XTA[e]);
    }
    PIN(xts);
    u32x2 ev[3], od[3];
    u32 tag = 0;
    WAIT_V(0);
    flag_set(L, w, 1);
    quad_wait(L + L_FLAG, 1);
    STAMP(1);
#pragma unroll
    for (int e = 0; e < 4; ++e) rdx(0, e);
#pragma unroll
    for (int q = 0; q < 5; ++q) rdb(0, 0, q);
    SCHED();
#pragma unroll
    for (int i = 0; i < 9; ++i) {
        u32 f = 0;
#pragma unroll
        for (int s = 0; s < 10; ++s) {
            mm(2 * i, s);
            if (s == 1 && i) { WAIT_L(12); *FREE = 2 * i + 2; }
            if (i == 1 && s >= 5 && s < 9) {
                u32 d[3];
#pragma unroll
                for (int c = 0; c < 3; ++c) {
                    const int q = 3 * (s - 5) + c;
                    d[c] = __builtin_amdgcn_perm(ev[q >> 2][(q >> 1) & 1], od[q >> 2][(q >> 1) & 1], (q & 1) ? 0x07060302u : 0x05040100u);
                }
                st16(rs, seal(d[0], d[1], d[2], tag), xts, (s - 5) * 16384);
            }
            if (s < 5) rdb(i, 1, s);
            if (s == 5 && i < 8) f = *FULL;
            SCHED();
        }
#pragma unroll
        for (int s = 0; s < 10; ++s) {
            mm(2 * i + 1, s);
            if (s == 0 && i < 8) {
                if (i) { WAIT_L0(); *FREE = 2 * i + 3; }
                if (__builtin_amdgcn_readfirstlane(f) < (u32)(i + 2)) flag_wait1(L, 10, i + 2);
                CFENCE();
                if (i == 0) {
#pragma unroll
                    for (int e = 0; e < 3; ++e) { ev[e] = *(LAS u32x2*)(XTA[e]); od[e] = *(LAS u32x2*)((lptr)((u32)(uintptr_t)XTA[e] ^ 16) + 128); }
                    tag = mk_tag(__builtin_amdgcn_readfirstlane(*(volatile LAS u32*)(L + L_FLAG + 32)));
                    WAIT_L0();
                    *FREE = 3;
                }
            }
            if (i < 8) {
                if (s == 1 || s == 2) { rdx(i + 1, 2 * s - 2); rdx(i + 1, 2 * s - 1); }
                if (s >= 3 && s < 8) rdb(i + 1, 0, s - 3);
            }
            SCHED();
        }
        STAMP(16 + i);
    }
    STAMP(2);
    {
        u32x4 op[5];
        const float ca = kl ? EPS : __builtin_inff(), cb = kl ? ONE_EPS : -__builtin_inff();
#pragma unroll
        for (int ct = 0; ct < 5; ++ct) {
            f32x4 v[2];
#pragma unroll
            for (int r = 0; r < 2; ++r)
#pragma unroll
                for (int i = 0; i < 4; ++i) {
                    float a = acc[r][ct][i];
                    if (ct < 2) a = phi(a);
                    if (ct == 4) a = __builtin_amdgcn_fmed3f(__builtin_amdgcn_exp2f(fmaf(a, LOG2E, -ONE_EPS * LOG2E)) + ca, a, cb);
                    v[r][i] = a;
                }
            op[ct] = u32x4{pk2(v[0][0], v[0][1]), pk2(v[0][2], v[0][3]), pk2(v[1][0], v[1][1]), pk2(v[1][2], v[1][3])};
        }
#pragma unroll
        for (int i = 0; i < 4; ++i) op[4][i] = m >= 12 ? 0x3f803f80u : op[4][i];
        const f32x4 Z = {0, 0, 0, 0};
        f32x4 j1[2], j3[2], j5[2], j7;
        j3[0] = mfma32(op[4], op[0], Z);
        j3[1] = mfma32(op[4], op[1], Z);
        j7 = mfma32(op[4], op[4], Z);
        j5[0] = mfma32(op[4], op[2], Z);
        j5[1] = mfma32(op[4], op[3], Z);
        j1[0] = mfma32(op[0], op[2], Z);
        j1[1] = mfma32(op[1], op[3], Z);
        f32x2 ex[2];
#pragma unroll
        for (int h = 0; h < 2; ++h)
#pragma unroll
            for (int i = 0; i < 2; ++i) ex[h][i] = (kg & 1) ? j3[h][2 * h + i] : (kg ? j7[2 * h + i] : j5[h][2 * h + i]);
        lptr K4 = L + L_KVX + lane * 16, K2 = L + L_KVX + 1024 + lane * 8;
        if (w >= 2) {
#pragma unroll
            for (int h = 0; h < 2; ++h) {
                *(LAS f32x4*)(K4 + (2 * w - 2 + h) * 1536) = j1[h];
                *(LAS f32x2*)(K2 + (2 * w - 2 + h) * 1536) = ex[h];
            }
            flag_set(L, w, 20);
        } else {
            *(LAS f32x4*)(K4 + w * 1536) = w ? j1[0] : j1[1];
            *(LAS f32x2*)(K2 + w * 1536) = w ? ex[0] : ex[1];
            flag_set(L, w, 20);
            f32x4 s4 = w ? j1[1] : j1[0];
            f32x2 s2 = w ? ex[1] : ex[0];
            quad_wait(L + L_FLAG, 20);
            s4 += *(LAS f32x4*)(K4 + (1 - w) * 1536);
            s2 += *(LAS f32x2*)(K2 + (1 - w) * 1536);
            f32x4 t4 = *(LAS f32x4*)(K4 + (2 + w) * 1536) + *(LAS f32x4*)(K4 + (4 + w) * 1536);
            f32x2 t2 = *(LAS f32x2*)(K2 + (2 + w) * 1536) + *(LAS f32x2*)(K2 + (4 + w) * 1536);
            s4 += t4;
            s2 += t2;
            st16(rs, seal(pk2(s4[0], s4[1]), pk2(s4[2], s4[3]), pk2(s2[0], s2[1]), tag), kg * 4096 + m * 16, WS_KVG + (hp * 8 + w * 4) * 4096 + rt * 256);
        }
    }
    STAMP(3);
    const u32 lo = lane * 16;
    u32x4 wff = ld16(rs, WS_WFF + min(lane, 40) * 16, 0);
    flag_wait1(L, 10, 10);
    STAMP(4);
    f32x4 q[2][3];
#pragma unroll
    for (int r = 0; r < 2; ++r)
#pragma unroll
        for (int t = 0; t < 3; ++t) q[r][t] = f32x4{ONE_EPS, ONE_EPS, ONE_EPS, ONE_EPS};
    u32x4 g[8], WF[45];
    const int grp = 9 * (cq >> 1) + 5 * (cq & 1) + w;
    const u32 wf_main = WS_WF + grp * 36864;
    const u32 wf_ext = WS_WF + (((9 * (cq >> 1) + 4) * 18 + 9 * (w >> 1)) * 2 + (cq & 1)) * 1024;
    auto ldwf = [&](int i) { WF[i] = i < 9 ? ld16(rs, lo, wf_ext + i * 2048) : ld16(rs, lo, wf_main + (i - 9) * 1024); };
    {
        lptr QA = L + L_XRING + ra + (lane & 3) * 8, QL = L + L_XRING + ra + 32;
        u32x2 a[2][3][2];
        auto rdq = [&](int k, int t) {
            const int i = k >> 1, o = (i / 3) * 16384 + (i % 3) * 5120 + (k & 1) * 2560;
            lptr p = t == 2 ? QL + o : QA + o + t * 40;
            a[k & 1][t][0] = trread(p);
            a[k & 1][t][1] = trread(p + 80);
        };
        rdq(0, 0); rdq(0, 1); rdq(0, 2);
        SCHED();
#pragma unroll
        for (int k = 0; k < 18; ++k) {
            if (k == 3) {
#pragma nounroll
                for (u32 it = 0; !wf_ok(wff, lane, tag) && it < SPIN_MAX; ++it) {
                    __builtin_amdgcn_s_sleep(8);
                    wff = ld16(rs, WS_WFF + min(lane, 40) * 16, 0);
                }
                CFENCE();
            }
#pragma unroll
            for (int s = 0; s < 6; ++s) {
                const int t = s >> 1, r = s & 1;
                q[r][t] = mfma32(cat(a[k & 1][t][0], a[k & 1][t][1]), X[k][r], q[r][t]);
                if (s < 3 && k < 17) rdq(k + 1, s);
                if ((s == 3 || s == 5) && k >= 4 && k < 17) ldwf(2 * (k - 4) + (s == 5));
                SCHED();
            }
        }
    }
#pragma unroll
    for (int i = 0; i < 8; ++i) g[i] = ld16(rs, lo, WS_KVG + (hp * 8 + 2 * w) * 4096 + i * 1024);
    SCHED();
    STAMP(5);
    u32x4 pq1[2], pq2[2];
#pragma unroll
    for (int r = 0; r < 2; ++r) {
        u32 p[6];
#pragma unroll
        for (int t = 0; t < 3; ++t) {
            p[2 * t] = pk2(phi(q[r][t][0]), phi(q[r][t][1]));
            p[2 * t + 1] = pk2(phi(q[r][t][2]), phi(q[r][t][3]));
        }
        pq1[r] = u32x4{p[0], p[1], p[2], p[3]};
        pq2[r] = u32x4{p[4], p[5], 0, 0};
    }
    u32x4 sel[2];
    {
        const u32 one = (m & 1) ? 0x3f800000u : 0x3f80u;
#pragma unroll
        for (int s = 0; s < 2; ++s)
#pragma unroll
            for (int i = 0; i < 4; ++i) sel[s][i] = (i < 3 && (m >> 1) == 4 * s + i) ? one : 0u;
    }
    const int hd = w >> 1, kgp = 2 * (w & 1) + (kg >> 1);
    int a0, a1;
    {
        const int DUMP = 38 * 144;
        const int rowE = (20 * hd + m) * 144, ex0 = (16 + 2 * hd) * 144, den = (36 + hd) * 144;
        const int cd = 16 * (m >> 2) + 8 * hd + 2 * (m & 3), cx = 64 + 4 * hd;
        if (!(kg & 1)) {
            a0 = rowE + 16 * kgp + 8 * hd; a1 = 0;
        } else if (kgp == 0) {
            a0 = rowE + cx; a1 = a0 + 2;
        } else if (kgp == 1) {
            a0 = ex0 + cd; a1 = a0 + 144;
        } else if (kgp == 2) {
            a0 = (m >> 1) == 2 + hd ? ex0 + (m & 1) * 144 + cx : (m == 12 ? den + cx : DUMP); a1 = a0 + 2;
        } else {
            a0 = den + cd; a1 = DUMP;
        }
    }
    lptr A0, A2;
    {
        const int kr = m >> 2, i = m & 3;
        A0 = L + L_AIMG + (2 * kr + (i & 1) + 8 * (i >> 1)) * 144 + kg * 16;
        A2 = L + L_AIMG + (i == 0 ? 32 + kr : 36 + ((i - 1) & 1)) * 144 + kg * 16;
    }
    a0 += L_AIMG; a1 += L_AIMG;
    u32 vog = lo + WS_OG + ((rt * 4 + w) * 16 + hp) * 3072;
    PIN(pq1[0]); PIN(pq1[1]); PIN(pq2[0]); PIN(pq2[1]); PIN(sel[0]); PIN(sel[1]); PIN(a0); PIN(a1); PIN(A0); PIN(A2); PIN(vog);
    SCHED();
    poll_rows<8>(g, rs, lo, WS_KVG + (hp * 8 + 2 * w) * 4096, 1024, tag);
    STAMP(6);
    {
        f32x4 d0 = {0, 0, 0, 0}, d1 = d0;
#pragma unroll
        for (int i = 0; i < 4; ++i) {
            g[i][3] = 0; g[4 + i][3] = 0;
            d0 = mfma32(sel[0], g[i], d0);
            d1 = mfma32(sel[1], g[4 + i], d1);
        }
        d0 += d1;
        const u32 p01 = pk2(d0[0], d0[1]), p23 = pk2(d0[2], d0[3]);
        lptr AI = L;
        if (!(kg & 1)) {
            *(LAS u32x2*)(AI + a0) = u32x2{p01, p23};
        } else {
            *(LAS u16*)(AI + a0) = (u16)p01;
            *(LAS u16*)(AI + a1) = (u16)(p01 >> 16);
        }
    }
    flag_set(L, w, 21);
    quad_wait(L + L_FLAG, 21);
    {
        u32x4 A[3][2];
        A[2][0] = *(LAS u32x4*)(A2); A[2][1] = *(LAS u32x4*)(A2 + 64);
        A[0][0] = *(LAS u32x4*)(A0); A[0][1] = *(LAS u32x4*)(A0 + 64);
        A[1][0] = *(LAS u32x4*)(A0 + 16 * 144); A[1][1] = *(LAS u32x4*)(A0 + 16 * 144 + 64);
        f32x4 o[2][3];
#pragma unroll
        for (int tt = 0; tt < 3; ++tt)
#pragma unroll
            for (int r = 0; r < 2; ++r) {
                const int t = (tt + 2) % 3;
                o[r][t] = mfma32(A[t][0], pq1[r], t == 2 ? f32x4{0, EPS, EPS, 0} : f32x4{0, 0, 0, 0});
                o[r][t] = mfma32(A[t][1], pq2[r], o[r][t]);
            }
        u32 d[2][4];
        float e[2];
#pragma unroll
        for (int r = 0; r < 2; ++r) {
            const float ra = __builtin_amdgcn_rcpf(o[r][2][1]), rb = __builtin_amdgcn_rcpf(o[r][2][2]), rc = kg ? rb : ra;
            d[r][0] = pk2s(o[r][0][0] * ra, o[r][0][1] * ra);
            d[r][1] = pk2s(o[r][0][2] * ra, o[r][0][3] * ra);
            d[r][2] = pk2s(o[r][1][0] * rc, o[r][1][1] * rc);
            d[r][3] = pk2s(o[r][1][2] * rb, o[r][1][3] * rb);
            e[r] = o[r][2][0] * rb;
        }
        st16(rs, seal(d[0][0], d[0][1], d[0][2], tag), vog, 0);
        st16(rs, seal(d[1][0], d[1][1], d[1][2], tag), vog, 1024);
        st16(rs, seal(d[0][3], pk2s(e[0], e[1]), d[1][3], tag), vog, 2048);
    }
    STAMP(7);
#pragma unroll
    for (int i = 26; i < 45; ++i) ldwf(i);
    const int c8 = 32 * (9 * (cq >> 1) + 4) + 16 * (cq & 1);
    lptr IM = L + L_XRING;
    {
        u32x4 og[12];
        const u32 so = WS_OG + (rb * 16 + 4 * w) * 3072;
#pragma unroll
        for (int i = 0; i < 12; ++i) og[i] = ld16(rs, lo, so + i * 1024);
        STAMP(8);
        poll_rows<12>(og, rs, lo, so, 1024, tag);
        STAMP(9);
        lptr B0 = IM + w * 4608 + m * 16;
#pragma unroll
        for (int h = 0; h < 4; ++h) {
            lptr E = B0 + (h >> 1) * 2304 + ((h & 1) ? 1024 + (kg >> 1) * 256 + ((8 + 4 * kg) & 15) : 4 * kg);
            lptr E2 = B0 + (h >> 1) * 2304 + ((h & 1) ? 2048 + 8 : 1024) + 2 * kg;
#pragma unroll
            for (int i = 0; i < 3; ++i) {
                *(LAS u32*)(E + i * 256) = og[3 * h][i];
                *(LAS u32*)(E + 18432 + i * 256) = og[3 * h + 1][i];
            }
            *(LAS u32*)(E + 768) = og[3 * h + 2][0];
            *(LAS u32*)(E + 18432 + 768) = og[3 * h + 2][2];
            *(LAS u16*)(E2) = (u16)og[3 * h + 2][1];
            *(LAS u16*)(E2 + 18432) = (u16)(og[3 * h + 2][1] >> 16);
        }
    }
    SCHED();
    u32x4 bt[4], b8[1];
    const u32 bs = WS_BT + (rb * 576 + grp * 32) * 128, bs8 = WS_BT + (rb * 576 + c8) * 128 + (w & 1) * 16;
    const u32 bv = (lane >> 2) * 128 + (lane & 3) * 32, gs = WS_G + rb * 256;
    u32x2 g0, g1;
    flag_set(L, w, 22);
    quad_wait(L + L_FLAG, 22);
    STAMP(10); STAMPC(60);
    const f32x4 Z = {0, 0, 0, 0};
    f32x4 o[2][2] = {{Z, Z}, {Z, Z}}, a8 = Z;
    lptr EX = L + L_WRING + (w & 1) * 1024 + lo;
    {
        lptr F = IM + lo, F8 = IM + (w & 1) * 18432 + (w >> 1) * 9216 + lo;
        u32x4 f8[9], f[3][2];
#pragma unroll
        for (int k = 0; k < 9; ++k) f8[k] = *(LAS u32x4*)(F8 + k * 1024);
        auto rdf = [&](int k) { f[k % 3][0] = *(LAS u32x4*)(F + k * 1024); f[k % 3][1] = *(LAS u32x4*)(F + 18432 + k * 1024); };
        SCHED();
#pragma unroll
        for (int k = 0; k < 9; ++k) {
            a8 = mfma32(WF[k], f8[k], a8);
            if (k == 2 || k == 4) rdf(k / 2 - 1);
            SCHED();
        }
#pragma unroll
        for (int k = 0; k < 18; ++k) {
            if (k + 2 < 18) rdf(k + 2);
            if (k >= 8 && k < 12) bt[k - 8] = ld16(rs, lo, bs + (k - 8) * 1024);
            if (k == 12) b8[0] = ld16(rs, bv, bs8);
            if (k == 6) g0 = __builtin_amdgcn_raw_buffer_load_b64(rs, m * 8, gs, AUX_SC1);
            if (k == 7) g1 = __builtin_amdgcn_raw_buffer_load_b64(rs, m * 8, gs + 128, AUX_SC1);
#pragma unroll
            for (int r = 0; r < 2; ++r)
#pragma unroll
                for (int t = 0; t < 2; ++t) o[r][t] = mfma32(WF[9 + 2 * k + t], f[k % 3][r], o[r][t]);
            if (k == 1 && w >= 2) { *(LAS f32x4*)(EX) = a8; }
            if (k == 3 && w >= 2) flag_set(L, w, 23);
            SCHED();
        }
    }
    STAMP(11); STAMPC(61);
    poll_rows<4>(bt, rs, lo, bs, 1024, tag);
    lptr BI = L + L_WRING + 2048 + w * 2560;
#pragma unroll
    for (int i = 0; i < 4; ++i) *(LAS u32x2*)(BI + (lane & 1) * 1024 + i * 256 + (lane >> 1) * 8) = u32x2{bt[i][0], bt[i][1]};
    if (w < 2) {
        poll_rows<1>(b8, rs, bv, bs8, 0, tag);
        *(LAS u32x2*)(BI + 2048 + lane * 8) = u32x2{b8[0][0], b8[0][1]};
    }
#pragma nounroll
    for (u32 it = 0; it < SPIN_MAX && !__all((g0[0] ^ g0[1]) == tag && (g1[0] ^ g1[1]) == tag); ++it) {
        __builtin_amdgcn_s_sleep(2);
        g0 = __builtin_amdgcn_raw_buffer_load_b64(rs, m * 8, gs, AUX_SC1);
        g1 = __builtin_amdgcn_raw_buffer_load_b64(rs, m * 8, gs + 128, AUX_SC1);
        CFENCE();
    }
    CFENCE();
    STAMP(12);
    auto mix = [&](float g, f32x4 a, u32x2 b) {
        const float b0 = lo2f(b[0]), b1 = hi2f(b[0]), b2 = lo2f(b[1]), b3 = hi2f(b[1]);
        return u32x2{pk2s(fmaf(g, a[0] - b0, b0), fmaf(g, a[1] - b1, b1)), pk2s(fmaf(g, a[2] - b2, b2), fmaf(g, a[3] - b3, b3))};
    };
    GAS char* orow = (GAS char*)P.out + (rb * 32 + m) * 1152;
#pragma unroll
    for (int r = 0; r < 2; ++r) {
        const float g = __uint_as_float(r ? g1[0] : g0[0]);
        const u32x2 t0 = trread(BI + r * 1024 + kg * 256 + m * 8), t1 = trread(BI + r * 1024 + kg * 256 + 128 + m * 8);
        *(GAS u32x4*)(orow + r * 18432 + grp * 64 + kg * 16) = cat(mix(g, o[r][0], t0), mix(g, o[r][1], t1));
    }
    if (w < 2) {
        flag_wait1(L, w + 2, 23);
        a8 += *(LAS f32x4*)(EX);
        *(GAS u32x2*)(orow + w * 18432 + c8 * 2 + kg * 8) = mix(__uint_as_float(w ? g1[0] : g0[0]), a8, trread(BI + 2048 + lane * 8));
    }
    STAMP(13);
}

struct cf { float r, i; };
DEV cf cmul(cf a, cf b) { return cf{fmaf(a.r, b.r, -(a.i * b.i)), fmaf(a.r, b.i, a.i * b.r)}; }
DEV cf csq(cf a) { return cf{fmaf(a.r, a.r, -(a.i * a.i)), (a.r + a.r) * a.i}; }
DEV cf cexp(float lr, float t) {
    const float e = __builtin_amdgcn_exp2f(lr), f = __builtin_amdgcn_fractf(t);
    return cf{e * __builtin_amdgcn_cosf(f), e * __builtin_amdgcn_sinf(f)};
}

DEV u32 kgen_role(Params& P, lptr L, int cl, int sg, int lane) {
    const int ch = 3 * sg + cl;
    const int m = lane & 15, kg = lane >> 4;
    lptr C = L + L_CH + cl * CH_BYTES;
    const int n = ch * 64 + lane;
    P.arrive<2>();
    u32 are = ((const GAS u32*)P.Are)[n], aim = ((const GAS u32*)P.Aim)[n], bre = ((const GAS u32*)P.Bre)[n];
    u32 bim = ((const GAS u32*)P.Bim)[n], cre = ((const GAS u32*)P.Cre)[n], cim = ((const GAS u32*)P.Cim)[n];
    const u16 ldt = ((const GAS u16*)P.logdt)[ch], dsk = ((const GAS u16*)P.Dskip)[ch];
    SCHED();
    RAW_BARRIER();
    u32 ga[2][20];
#pragma unroll
    for (int it = 0; it < 2; ++it) {
        const int l0 = (it * 64 + lane) * 16 - 4;
#pragma unroll
        for (int j = 0; j < 10; ++j) {
            const int la = l0 + 2 * j, lb = la + 1;
            const u32 bits = pk2((float)la, (float)lb);
            int ia = (int)(bits & 0xffff) - 0x4280, ib = (int)(bits >> 16) - 0x4280;
            if (it == 0) {
                ia = la < 256 ? (la < 0 ? 641 : la) : ia;
                ib = lb < 256 ? (lb < 0 ? 641 : lb) : ib;
            }
            ga[it][2 * j] = L_KT + cl * 1312 + 2 * ia;
            ga[it][2 * j + 1] = L_KT + cl * 1312 + 2 * ib;
        }
    }
#pragma unroll
    for (int it = 0; it < 2; ++it)
#pragma unroll
        for (int j = 0; j < 20; ++j) asm volatile("" : "+v"(ga[it][j]));
    SCHED();
    const float dt = lo2f(pk2(expf(bf2f(ldt)), 0.f));
    STAMP(1); STAMPC(60);
    f32x4 a0 = {lane == 0 ? bf2f(dsk) : 0.f, 0, 0, 0}, a1 = {0, 0, 0, 0}, a2 = a1;
    LAS u32* T = (LAS u32*)(C + CH_KC + (lane >> 2) * TQ + (lane & 3) * 4);
    lptr R = C + CH_KC + kg * TQ + m * 16;
#pragma nounroll
    for (int u = 0; u < 2; ++u) {
        auto hf = [&](u32& v) { const float r = hi2f(v); v <<= 16; return r; };
        const float lr = dt * hf(are) * LOG2E, t = dt * hf(aim) * 0.15915494309189533577f;
        const cf c = cmul(cf{hf(cre), hf(cim)}, cf{dt * hf(bre), dt * hf(bim)});
        cf p[16], A[9];
        p[1] = cexp(lr, t);
        const cf w16 = cexp(16.f * lr, 16.f * t);
        A[1] = cexp(256.f * lr, 256.f * t);
        auto put = [&](int row, cf v) { if (EXP & 4) { u32 z = pk2s(v.r, v.i); asm volatile("" :: "v"(z)); } else T[row * 4] = pk2s(v.r, v.i); };
        T[0] = 0x3f80u;
        put(1, p[1]);
#pragma unroll
        for (int k = 2; k < 16; ++k) {
            p[k] = (k & 1) ? cmul(p[k - 1], p[1]) : csq(p[k / 2]);
            put(k, p[k]);
        }
        {
            const cf w32 = csq(w16);
            cf h0 = cf{c.r, -c.i}, h1 = cmul(c, w16);
            h1.i = -h1.i;
            const cf wc = cf{w32.r, -w32.i};
#pragma unroll
            for (int k = 0; k < 8; ++k) {
                put(41 + 2 * k, h0);
                put(42 + 2 * k, h1);
                if (k < 7) { h0 = cmul(h0, wc); h1 = cmul(h1, wc); }
            }
        }
        A[2] = csq(A[1]); A[3] = cmul(A[2], A[1]); A[4] = csq(A[2]);
        A[5] = cmul(A[4], A[1]); A[6] = csq(A[3]); A[7] = cmul(A[4], A[3]); A[8] = csq(A[4]);
#pragma unroll
        for (int k = 0; k < 8; ++k) put(16 + k, k ? cmul(A[1], p[2 * k]) : A[1]);
#pragma unroll
        for (int k = 0; k < 8; ++k) put(24 + k, (k & 3) ? cmul(A[2 + (k >> 2)], p[4 * (k & 3)]) : A[2 + (k >> 2)]);
#pragma unroll
        for (int k = 0; k < 8; ++k) put(32 + k, (k & 1) ? cmul(A[4 + (k >> 1)], p[8]) : A[4 + (k >> 1)]);
        put(40, A[8]);
        CFENCE();
        STAMPD(8 + u);
#pragma unroll
        for (int s = 0; s < 4; ++s) {
            const u32x4 H = *(LAS u32x4*)(R + s * 4 * TQ + 41 * 16);
            a0 = mfma32(*(LAS u32x4*)(R + s * 4 * TQ), H, a0);
            a1 = mfma32(*(LAS u32x4*)(R + s * 4 * TQ + 256), H, a1);
            a2 = mfma32(*(LAS u32x4*)(R + s * 4 * TQ + 512), H, a2);
        }
        CFENCE();
        STAMPD(6 + u);
    }
    STAMP(2); STAMPC(61);
    const u32 tag = mk_tag(get_epoch(L));
    u32x4 g[6];
    const u32 xs = WS_XT + sg * 16384 + cl * 5120;
#pragma unroll
    for (int i = 0; i < 6; ++i) g[i] = ld16(P.rs, lane * 16, xs + i * 1024);
    SCHED();
    {
        lptr KT = L + L_KT + cl * 1312;
        *(LAS u32x2*)(KT + 2 * (16 * m + 4 * kg)) = u32x2{pk2(a0[0], a0[1]), pk2(a0[2], a0[3])};
        *(LAS u32x2*)(KT + 2 * (kg < 2 ? 256 + 8 * m + 4 * kg : 256 + 64 * kg + 4 * m)) = u32x2{pk2(a1[0], a1[1]), pk2(a1[2], a1[3])};
        if (kg < 2) {
            *(LAS u32*)(KT + 2 * (512 + 64 * kg + 2 * m)) = pk2(a2[0], a2[1]);
            *(LAS u32*)(KT + 2 * (544 + 64 * kg + 2 * m)) = pk2(a2[2], a2[3]);
        }
        if (lane == 32) *(LAS u32*)(KT + 2 * 640) = pk2(a2[0], 0.f);
        CFENCE();
    }
#pragma unroll
    for (int it = 0; it < 2; ++it) {
        u32 v[20], d[10], e[9];
#pragma unroll
        for (int j = 0; j < 20; ++j) v[j] = *(LAS u16*)(L + ga[it][j]);
#pragma unroll
        for (int j = 1; j < 10; ++j) d[j] = v[2 * j] | (v[2 * j + 1] << 16);
#pragma unroll
        for (int j = 0; j < 9; ++j) e[j] = v[2 * j + 1] | (v[2 * j + 2] << 16);
        lptr W = C + CH_KC + 32 + (it * 64 + lane) * 32;
        *(LAS u32x4*)(W) = u32x4{d[2], d[3], d[4], d[5]};
        *(LAS u32x4*)(W + 16) = u32x4{d[6], d[7], d[8], d[9]};
        *(LAS u32x4*)(W + CH_KSTRIDE) = u32x4{e[1], e[2], e[3], e[4]};
        *(LAS u32x4*)(W + CH_KSTRIDE + 16) = u32x4{e[5], e[6], e[7], e[8]};
        *(LAS u32x4*)(W + 2 * CH_KSTRIDE) = u32x4{d[1], d[2], d[3], d[4]};
        *(LAS u32x4*)(W + 2 * CH_KSTRIDE + 16) = u32x4{d[5], d[6], d[7], d[8]};
        *(LAS u32x4*)(W + 3 * CH_KSTRIDE) = u32x4{e[0], e[1], e[2], e[3]};
        *(LAS u32x4*)(W + 3 * CH_KSTRIDE + 16) = u32x4{e[4], e[5], e[6], e[7]};
    }
    if (lane < 8) *(LAS u32x4*)(C + CH_KC + (lane >> 1) * CH_KSTRIDE + (lane & 1) * 16) = u32x4{0, 0, 0, 0};
    *(LAS u32x2*)(C + CH_XR + 4096 + lane * 8) = u32x2{0, 0};
    SCHED();
    STAMP(10);
    poll_rows<6>(g, P.rs, lane * 16, xs, 1024, tag);
    lptr XR = L + L_CH + CH_XR + cl * 1280 + lane * 4;
#pragma unroll
    for (int i = 0; i < 6; ++i)
#pragma unroll
        for (int c = 0; c < 3; ++c) *(LAS u32*)(XR + c * CH_BYTES + i * 256) = g[i][c];
    return tag;
}

template <int MASK, int NA>
DEV void conv_blocks(rsrc_t rs, lptr C, int ch, int lane, u32 tag) {
    const int m = lane & 15, kg = lane >> 4;
    f32x4 acc[8];
#pragma unroll
    for (int i = 0; i < 8; ++i) acc[i] = f32x4{0, 0, 0, 0};
    lptr KA = C + CH_KC + ((~m) & 3) * CH_KSTRIDE + 2 * (8 * kg + ((m + 4) & ~3));
    lptr XB = C + CH_XR + 2 * (240 - 16 * m + 8 * kg);
    u32x4 A[2][NA], B[2][NA];
    auto rd = [&](int j, int a) {
        A[j & 1][a] = cat(*(volatile LAS u32x2*)(KA + 64 * j + 512 * a), *(volatile LAS u32x2*)(KA + 64 * j + 512 * a + 8));
        B[j & 1][a] = *(LAS u32x4*)(XB + 64 * j + 512 * (7 - a));
    };
#pragma unroll
    for (int a = 0; a < NA; ++a) rd(0, a);
    SCHED();
#pragma unroll
    for (int j = 0; j < 8; ++j) {
        int n = 0;
#pragma unroll
        for (int I = 0; I < 8; ++I)
            if (MASK >> I & 1)
#pragma unroll
                for (int a = 0; a <= I; ++a) {
                    acc[I] = mfma32(A[j & 1][a], B[j & 1][I - a], acc[I]);
                    if (j < 7 && !(n & 1) && (n >> 1) < NA) rd(j + 1, n >> 1);
                    ++n;
                    SCHED();
                }
    }
    const u32 vo = (m >> 1) * 73728 + kg * 32 + (m & 1) * 16;
#pragma unroll
    for (int I = 0; I < 8; ++I)
        if (MASK >> I & 1) st16(rs, seal(pk2(acc[I][0], acc[I][1]), pk2(acc[I][2], acc[I][3]), 0u, tag), vo, WS_BT + I * 8 * 73728 + ch * 128);
}

DEV void gate_role(const Params& P, rsrc_t rs, int lane, int rb, int ps, u32 tag) {
    const GAS char* gw = (const GAS char*)P.gw + (lane & 3) * 16;
    {
        const int row = rb * 32 + ps * 16 + (lane >> 2);
        const GAS char* xr = (const GAS char*)P.x + row * 1152 + (lane & 3) * 16;
        u32x4 xv[18], gv[18];
#pragma unroll
        for (int i = 0; i < 18; ++i) { xv[i] = *(const GAS u32x4*)(xr + i * 64); gv[i] = *(const GAS u32x4*)(gw + i * 64); }
        float s0 = 0.f, s1 = 0.f;
#pragma unroll
        for (int i = 0; i < 18; ++i)
#pragma unroll
            for (int j = 0; j < 4; ++j) {
                s0 = fmaf(lo2f(xv[i][j]), lo2f(gv[i][j]), s0);
                s1 = fmaf(hi2f(xv[i][j]), hi2f(gv[i][j]), s1);
            }
        float s = s0 + s1;
        s += __shfl_xor(s, 1);
        s += __shfl_xor(s, 2);
        const u32 g = __float_as_uint(1.0f / (1.0f + __expf(-s)));
        if ((lane & 3) == 0) __builtin_amdgcn_raw_buffer_store_b64(u32x2{g, g ^ tag}, rs, WS_G + row * 8, 0, AUX_SC1);
    }
}

DEV u32 wf_role(Params& P, lptr L, u32 lbase, int cl, int lane, int unit) {
    const int ks = unit / 9, gp = unit - ks * 9;
    P.arrive<3>();
    const rsrc_t rs = P.rs;
    if (unit < 162) {
        const char* src = (const char*)P.wo + ks * 32 * 1152 + gp * 128;
#pragma unroll
        for (int i = 0; i < 4; ++i) glds16((u32)((lane >> 3) * 1152 + (lane & 7) * 16), src + i * 8 * 1152, lbase + L_CH + cl * CH_BYTES + i * 1040);
    }
    RAW_BARRIER();
    if (unit >= 162) return mk_tag(get_epoch(L));
    WAIT_V(0);
    lptr S = L + L_CH + cl * CH_BYTES + (lane >> 4) * 1040 + ((lane >> 2) & 3) * 128;
#pragma unroll
    for (int h = 0; h < 2; ++h) {
        const int grp = 2 * gp + h;
        const bool sp = grp == 4 || grp == 13;
        lptr p = S + h * 64 + (lane & 3) * (sp ? 8 : 16);
#pragma unroll
        for (int t = 0; t < 2; ++t) {
            lptr q = p + t * (sp ? 32 : 8);
            st16(rs, cat(trread(q), trread(q + 512)), lane * 16, WS_WF + ((grp * 18 + ks) * 2 + t) * 1024);
        }
    }
    SCHED();
    const u32 tag = mk_tag(get_epoch(L));
    WAIT_V(0);
    if (lane == 0) __builtin_amdgcn_raw_buffer_store_b32(tag, rs, WS_WFF + unit * 4, 0, AUX_SC1);
    return tag;
}

__global__ __launch_bounds__(512, 2) void fused(Args A) {
    Params P;
    P.fetch();
    extern __shared__ __attribute__((aligned(64))) char smem[];
    lptr L = (lptr)smem;
    const u32 lbase = (u32)(uintptr_t)L;
    const int tid = threadIdx.x, lane = tid & 63, w = __builtin_amdgcn_readfirstlane(tid >> 6);
    const int bid = blockIdx.x, xcd = bid & 7, slot = bid >> 3;
    if (tid < 16) *(LAS u32*)(L + L_FLAG + tid * 4) = tid == 7;
    const int rt = (xcd >> 1) * 4 + (slot & 3), hp = (xcd & 1) * 8 + (slot >> 2);
    const int rb = 8 * xcd + (slot & 7), cq = slot >> 3, cg = w & 3;
    if (w < 4) {
        gemm_role(P, L, lbase, w, lane, rt, hp, rb, cq);
        if (bid == 0 && tid == 0) {
            const u32 ep = *(volatile LAS u32*)(L + L_FLAG + 32);
            __builtin_amdgcn_raw_buffer_store_b32(ep + 1, P.rs, WS_EPOCH, 0, AUX_SC1);
        }
    } else if (w == 7) {
        const u32 tag = loader_role(P, L, lbase, lane, rt, hp);
        if (slot < 24) {
            const int sg = xcd * 24 + slot;
            quad_wait(L + L_FLAG + 16, 1);
            STAMP(3);
#pragma nounroll
            for (int c = 0; c < 3; ++c) {
                STAMP(4);
                conv_blocks<0x0E, 4>(P.rs, L + L_CH + c * CH_BYTES, 3 * sg + c, lane, tag);
            }
            STAMP(5);
        }
    } else if (slot < 24) {
        const int sg = xcd * 24 + slot;
        const u32 tag = kgen_role(P, L, cg, sg, lane);
        flag_set(L, w, 1);
        STAMP(3);
        quad_wait(L + L_FLAG + 16, 1);
        STAMP(4);
        conv_blocks<0xF1, 8>(P.rs, L + L_CH + cg * CH_BYTES, 3 * sg + cg, lane, tag);
        STAMP(5);
    } else {
        const u32 tag = wf_role(P, L, lbase, cg, lane, ((slot - 24) * 8 + xcd) * 3 + cg);
        STAMP(3);
        if (cg < 2) {
            flag_wait1(L, 10, 10);
            gate_role(P, P.rs, lane, rb, cg, tag);
        }
        STAMP(4);
    }
}

extern "C" void kernel_launch(void* const* d_in, const int* in_sizes, int n_in, void* d_out, int out_size, void* d_ws,
                              size_t ws_size, hipStream_t stream) {
    Args P;
    P.x = (const u16*)d_in[0];
    P.wqkv = (const u16*)d_in[1];
    P.wo = (const u16*)d_in[2];
    P.Are = (const u16*)d_in[4];
    P.Aim = (const u16*)d_in[5];
    P.Bre = (const u16*)d_in[6];
    P.Bim = (const u16*)d_in[7];
    P.Cre = (const u16*)d_in[8];
    P.Cim = (const u16*)d_in[9];
    P.logdt = (const u16*)d_in[10];
    P.Dskip = (const u16*)d_in[11];
    P.gw = (const u16*)d_in[12];
    P.ws = (char*)d_ws;
    P.out = (u16*)d_out;
    (void)hipFuncSetAttribute((const void*)fused, hipFuncAttributeMaxDynamicSharedMemorySize, LDS_BYTES);
    fused<<<256, 512, LDS_BYTES, stream>>>(P);
}
```

```cpp
#define STAMPS 1
#define DIAG 0
#define DIAGB 0
#include <hip/hip_runtime.h>
#include <hip/hip_bf16.h>
#include <stdint.h>

typedef unsigned short u16;
typedef unsigned int u32;
typedef unsigned long long u64;
typedef u32 u32x4 __attribute__((ext_vector_type(4)));
typedef u32 u32x2 __attribute__((ext_vector_type(2)));
typedef float f32x4 __attribute__((ext_vector_type(4)));
typedef float f32x2 __attribute__((ext_vector_type(2)));
typedef __bf16 bf16x8 __attribute__((ext_vector_type(8)));
typedef __bf16 bf16x2 __attribute__((ext_vector_type(2)));
typedef short s16x4 __attribute__((ext_vector_type(4)));
#define LAS __attribute__((address_space(3)))
#define GAS __attribute__((address_space(1)))
#define DEV __device__ __forceinline__
typedef LAS char* lptr;
typedef __amdgpu_buffer_rsrc_t rsrc_t;

constexpr u32 WS_EPOCH = 0;
constexpr u32 WS_WFF = 256;
constexpr u32 WS_G = 4096;
constexpr u32 WS_KVG = 65536;
constexpr u32 WS_WF = WS_KVG + 524288;
constexpr u32 WS_XT = WS_WF + 663552;
constexpr u32 WS_OG = WS_XT + 3145728;
constexpr u32 WS_BT = WS_OG + 3145728;
constexpr u32 WS_DBG = WS_BT + 4718592;
constexpr u32 WS_END = WS_DBG + 1048576;
constexpr int AUX_SC1 = 16;
#define EXP 0
#define SPIN_LOG 17
constexpr u32 SPIN_MAX = 1u << SPIN_LOG;

constexpr u32 L_FLAG = 0;
constexpr u32 CH_KC = 0, CH_KSTRIDE = 4160, CH_XR = 16640, CH_BYTES = 21248;
constexpr u32 L_CH = 64;
constexpr u32 L_KT = L_CH + 3 * CH_BYTES;
constexpr u32 L_XRING = L_KT + 3 * 1312;
constexpr u32 L_WRING = L_XRING + 49152;
constexpr u32 L_AIMG = L_WRING + 30720;
constexpr u32 LDS_BYTES = L_AIMG + 5632;
constexpr u32 L_KVX = L_WRING;
constexpr u32 TQ = 944;
static_assert(LDS_BYTES <= 163840, "lds");
static_assert(L_XRING % 32 == 0, "align");

DEV float bf2f(u16 v) { return __uint_as_float(((u32)v) << 16); }
DEV u32 pk2(float a, float b) {
    f32x2 v = {a, b};
    return __builtin_bit_cast(u32, __builtin_convertvector(v, bf16x2));
}
DEV u32 pk2s(float a, float b) {
    u32 r;
    asm("v_cvt_pk_bf16_f32 %0, %1, %2" : "=v"(r) : "v"(a), "v"(b));
    return r;
}
DEV float lo2f(u32 v) { return __uint_as_float(v << 16); }
DEV float hi2f(u32 v) { return __uint_as_float(v & 0xffff0000u); }
DEV f32x4 mfma32(u32x4 a, u32x4 b, f32x4 c) {
    return __builtin_amdgcn_mfma_f32_16x16x32_bf16(__builtin_bit_cast(bf16x8, a), __builtin_bit_cast(bf16x8, b), c, 0, 0, 0);
}
DEV void glds16(u32 voff, const void* sbase, u32 ldsaddr) {
    asm volatile("s_mov_b32 m0, %2\n\ts_nop 0\n\tglobal_load_lds_dwordx4 %0, %1" ::"v"(voff), "s"(sbase), "s"(ldsaddr) : "memory");
}
DEV void glds16x4(u32 v0, u32 v1, u32 v2, u32 v3, const void* sbase, u32 ldsaddr) {
    asm volatile("s_mov_b32 m0, %5\n\ts_nop 0\n\tglobal_load_lds_dwordx4 %0, %4\n\tglobal_load_lds_dwordx4 %1, %4 offset:1024\n\t"
                 "global_load_lds_dwordx4 %2, %4 offset:2048\n\tglobal_load_lds_dwordx4 %3, %4 offset:3072" ::"v"(v0), "v"(v1), "v"(v2), "v"(v3),
                 "s"(sbase), "s"(ldsaddr) : "memory");
}
DEV u32x2 trread(lptr p) { return __builtin_bit_cast(u32x2, __builtin_amdgcn_ds_read_tr16_b64_v4i16((LAS s16x4*)p)); }
DEV u32x4 cat(u32x2 a, u32x2 b) { return u32x4{a[0], a[1], b[0], b[1]}; }
DEV u32x4 ld16(rsrc_t rs, u32 voff, u32 soff) { return __builtin_amdgcn_raw_buffer_load_b128(rs, voff, soff, AUX_SC1); }
DEV void st16(rsrc_t rs, u32x4 v, u32 voff, u32 soff) { __builtin_amdgcn_raw_buffer_store_b128(v, rs, voff + soff, 0, AUX_SC1); }
DEV u32 xor3(u32 a, u32 b, u32 c) { return __builtin_amdgcn_bitop3_b32(a, b, c, 0x96); }
DEV u32x4 seal(u32 a, u32 b, u32 c, u32 tag) { return u32x4{a, b, c, xor3(a, b, tag) ^ c}; }
DEV u32 unseal(u32x4 g, u32 tag) { return xor3(xor3(g[0], g[1], tag), g[2], g[3]); }
#define WAIT_V(n) asm volatile("s_waitcnt vmcnt(%0)" ::"n"(n) : "memory")
#define WAIT_L(n) asm volatile("s_waitcnt lgkmcnt(%0)" ::"n"(n) : "memory")
#define WAIT_L0() asm volatile("s_waitcnt lgkmcnt(0)" ::: "memory")
#define RAW_BARRIER() asm volatile("s_waitcnt lgkmcnt(0)\n\ts_barrier" ::: "memory")
#define CFENCE() asm volatile("" ::: "memory")
#define SCHED() __builtin_amdgcn_sched_barrier(0)

#define STAMP(i) do { SCHED(); u64 t_; asm volatile("s_memrealtime %0\n\ts_waitcnt lgkmcnt(0)" : "=s"(t_) :: "memory"); asm volatile("v_writelane_b32 %0, %1, %2" : "+v"(P.tsv) : "s"((u32)t_), "n"(i)); SCHED(); } while (0)
#define STAMPC(i) do { SCHED(); u64 t_; asm volatile("s_memtime %0\n\ts_waitcnt lgkmcnt(0)" : "=s"(t_) :: "memory"); asm volatile("v_writelane_b32 %0, %1, %2" : "+v"(P.tsv) : "s"((u32)t_), "n"(i)); SCHED(); } while (0)
#define STAMPD(i) do { SCHED(); u64 t_; asm volatile("s_memrealtime %0\n\ts_waitcnt lgkmcnt(0)" : "=s"(t_) :: "memory"); asm volatile("s_mov_b32 m0, %2\n\ts_nop 0\n\tv_writelane_b32 %0, %1, m0" : "+v"(P.tsv) : "s"((u32)t_), "s"(i)); SCHED(); } while (0)

DEV void flag_set(lptr L, int wave, u32 v) {
    WAIT_L0();
    *(volatile LAS u32*)(L + L_FLAG + wave * 4) = v;
}
DEV void flag_wait1(lptr L, int wave, u32 v) {
#pragma nounroll
    for (u32 it = 0; it < SPIN_MAX * 8; ++it) {
        u32 f = *(volatile LAS u32*)(L + L_FLAG + wave * 4);
        if (__builtin_amdgcn_readfirstlane(f) >= v) break;
        __builtin_amdgcn_s_sleep(1);
    }
    CFENCE();
}
DEV bool quad_ok(u32x4 f, u32 v) {
    u32 m = min(min(f[0], f[1]), min(f[2], f[3]));
    return __builtin_amdgcn_readfirstlane(m) >= v;
}
DEV void quad_wait(lptr Q, u32 v) {
#pragma nounroll
    for (u32 it = 0; it < SPIN_MAX * 8; ++it) {
        u32x4 f = *(volatile LAS u32x4*)(Q);
        if (quad_ok(f, v)) break;
        __builtin_amdgcn_s_sleep(1);
    }
    CFENCE();
}
DEV void quad_spin(lptr Q, u32 v) {
#pragma nounroll
    for (u32 it = 0; it < SPIN_MAX * 16; ++it) {
        u32x4 f = *(volatile LAS u32x4*)(Q);
        if (quad_ok(f, v)) break;
        if (EXP & 8) __builtin_amdgcn_s_sleep(1);
    }
    CFENCE();
}
DEV u32 mk_tag(u32 epoch) { return epoch * 0x9E3779B1u + 0x7F4A7C15u; }
DEV void put_epoch(lptr L, u32 epoch) { *(volatile LAS u32x2*)(L + L_FLAG + 32) = u32x2{epoch, 1u}; }
DEV u32 get_epoch(lptr L) {
    u32x2 v;
#pragma nounroll
    for (u32 it = 0; it < SPIN_MAX * 8; ++it) {
        v = *(volatile LAS u32x2*)(L + L_FLAG + 32);
        if (__builtin_amdgcn_readfirstlane(v[1])) break;
        __builtin_amdgcn_s_sleep(1);
    }
    CFENCE();
    return __builtin_amdgcn_readfirstlane(v[0]);
}
template <int N>
DEV bool rows_bad(u32x4 (&g)[N], u32 tag) {
    u32 bad = 0;
#pragma unroll
    for (int i = 0; i < N; ++i) bad |= unseal(g[i], tag);
    return __any(bad != 0);
}
template <int N>
DEV void poll_rows(u32x4 (&g)[N], rsrc_t rs, u32 voff, u32 soff, u32 stride, u32 tag) {
    bool bad = rows_bad(g, tag);
#pragma nounroll
    for (u32 it = 0; __builtin_expect(bad, 0) && it < SPIN_MAX; ++it) {
#pragma unroll
        for (int i = 0; i < N; ++i)
            if (__any(unseal(g[i], tag) != 0))
                asm volatile("buffer_load_dwordx4 %0, %1, %2, %3 offen sc1" : "+v"(g[i]) : "v"(voff), "s"(rs), "s"(soff + i * stride) : "memory");
        WAIT_V(0);
#pragma unroll
        for (int i = 0; i < N; ++i) asm volatile("" : "+v"(g[i]));
        bad = rows_bad(g, tag);
    }
    CFENCE();
}
#define PIN(x) asm volatile("" : "+v"(x))

struct Args {
    const u16 *x, *wqkv, *wo, *Are, *Aim, *Bre, *Bim, *Cre, *Cim, *logdt, *Dskip, *gw;
    char* ws;
    u16* out;
};
typedef u64 u64x8 __attribute__((ext_vector_type(8)));
typedef u64 u64x4 __attribute__((ext_vector_type(4)));
typedef u64 u64x2 __attribute__((ext_vector_type(2)));
struct Params : Args {
    u64x8 k0; u64x4 k1; u64x2 k2;
    rsrc_t rs;
    mutable u32 tsv;
    DEV void fetch() {
        asm volatile("s_load_dwordx16 %0, %3, 0x0\n\ts_load_dwordx8 %1, %3, 0x40\n\ts_load_dwordx4 %2, %3, 0x60"
                     : "=&s"(k0), "=&s"(k1), "=&s"(k2) : "s"(__builtin_amdgcn_kernarg_segment_ptr()));
    }
    DEV static u64 take(u64 v) {
        u64 r;
        asm volatile("s_mov_b64 %0, %1" : "=s"(r) : "s"(v));
        return r;
    }
    template <int ROLE>
    DEV void arrive() {
        asm volatile("s_waitcnt lgkmcnt(0)" ::"s"(k0), "s"(k1), "s"(k2));
        if (ROLE != 2) x = (const u16*)take(k0[0]);
        if (ROLE < 2) wqkv = (const u16*)take(k0[1]);
        if (ROLE == 3) { wo = (const u16*)take(k0[2]); gw = (const u16*)take(k1[3]); }
        if (ROLE == 2) {
            Are = (const u16*)take(k0[3]); Aim = (const u16*)take(k0[4]); Bre = (const u16*)take(k0[5]); Bim = (const u16*)take(k0[6]);
            Cre = (const u16*)take(k0[7]); Cim = (const u16*)take(k1[0]); logdt = (const u16*)take(k1[1]); Dskip = (const u16*)take(k1[2]);
        }
        ws = (char*)take(k2[0]);
        if (ROLE == 0) out = (u16*)take(k2[1]);
        rs = __builtin_amdgcn_make_buffer_rsrc((void*)ws, 0, (int)WS_END, 0x00020000);
    }
};
DEV void pos5(u32 idx, u32& pos, u32& piece) {
    pos = (idx * 52429u) >> 18;
    piece = idx - 5 * pos;
}
DEV u32 pos2row(u32 pos) { return (pos & 56) | ((pos >> 1) & 3) | ((pos & 1) << 2); }

DEV int first_chunk(int hp) { return (9 * hp) >> 4; }
DEV u32 loader_role(Params& P, lptr L, u32 lbase, int lane, int rt, int hp) {
    u32 wv[5], xv[4];
#pragma unroll
    for (int p = 0; p < 5; ++p) {
        u32 pos, piece;
        pos5(p * 64 + lane, pos, piece);
        wv[p] = pos2row(pos) * 3456 + piece * 16 - (p & 3) * 1024;
    }
#pragma unroll
    for (int j = 0; j < 4; ++j) xv[j] = (u32)((lane >> 3) * 1152 + (((lane & 7) ^ (lane >> 3)) << 4)) + j * 8192;
    int c = first_chunk(hp);
    P.arrive<1>();
    u32 epoch;
    asm volatile("global_load_dword %0, %1, %2 sc1" : "=v"(epoch) : "v"(WS_EPOCH), "s"(P.ws) : "memory");
    const char* xg = (const char*)P.x + rt * 128 * 1152;
    const char* wg = (const char*)P.wqkv + 72 * hp;
    auto five = [&](u32(&v)[5], const char* src, u32 dst) {
        glds16x4(v[0], v[1], v[2], v[3], src, dst);
        glds16(v[4], src, dst + 4096);
    };
    const int c0 = c;
    int k = 0;
    auto next = [&]() {
        k = k == 1 ? 2 + 2 * (rt & 3) : (k == 8 ? 2 : k + 1);
        c = c0 + k >= 9 ? c0 + k - 9 : c0 + k;
    };
    auto ldx = [&](u32 sl) {
#pragma unroll
        for (int w = 0; w < 4; ++w) glds16x4(xv[0], xv[1], xv[2], xv[3], xg + c * 128 + w * 36864, lbase + L_XRING + sl * 16384 + w * 4096);
    };
    auto ldw = [&](u32 sl) {
        five(wv, wg + c * 221184 + 1144, lbase + L_WRING + sl * 10240);
        five(wv, wg + c * 221184 + 2296, lbase + L_WRING + sl * 10240 + 5120);
    };
    volatile LAS u32* FULL = (volatile LAS u32*)(L + L_FLAG + 40);
    next();
    ldx(1);
    ldw(1);
    RAW_BARRIER();
    __builtin_amdgcn_s_setprio(3);
    asm volatile("s_waitcnt vmcnt(26)" : "+v"(epoch)::"memory");
    const u32 ep = __builtin_amdgcn_readfirstlane(epoch);
    put_epoch(L, ep);
    STAMP(1);
    if (EXP & 1) { WAIT_V(0); __builtin_amdgcn_s_setprio(0); STAMP(2); return mk_tag(ep); }
    next();
    ldx(2);
    WAIT_V(16);
    *FULL = 2;
    ldw(2);
    u32 sl = 0;
#pragma nounroll
    for (u32 j = 3; j < 9; ++j) {
        next();
        if (!(EXP & 2)) quad_spin(L + L_FLAG, 2 * j - 4);
        ldx(sl);
        if (j == 3) { WAIT_V(16); *FULL = 3; }
        if (!(EXP & 2)) quad_spin(L + L_FLAG, 2 * j - 3);
        ldw(sl);
        WAIT_V(26);
        *FULL = j;
        sl = sl == 2 ? 0 : sl + 1;
    }
    k = -1;
#pragma nounroll
    for (u32 s = 0; s < 3; ++s) {
        if (!(EXP & 2)) quad_spin(L + L_FLAG, 14 + 2 * s);
#pragma unroll
        for (int i = 0; i < 3; ++i) {
            next();
            five(wv, wg + c * 221184, lbase + L_XRING + s * 16384 + i * 5120);
        }
        if (s == 0) {
            if (!(EXP & 2)) quad_spin(L + L_FLAG, 15);
            WAIT_V(15);
            *FULL = 9;
        }
    }
    WAIT_V(0);
    *FULL = 10;
    __builtin_amdgcn_s_setprio(0);
    STAMP(2);
    return mk_tag(ep);
}

constexpr float LOG2E = 1.44269504088896340736f;
constexpr float EPS = 1e-6f;
constexpr float ONE_EPS = 1.0f + 1e-6f;
DEV float phi(float a) {
    float e = __builtin_amdgcn_exp2f(fmaf(a, LOG2E, -ONE_EPS * LOG2E));
    return __builtin_amdgcn_fmed3f(e + EPS, a, ONE_EPS);
}

DEV bool wf_ok(u32x4 f, int lane, u32 tag) { return __all(f[0] == tag && f[1] == tag && (lane >= 40 || (f[2] == tag && f[3] == tag))); }

DEV void gemm_role(Params& P, lptr L, u32 lbase, int w, int lane, int rt, int hp, int rb, int cq) {
    const int m = lane & 15, kg = lane >> 4;
    {
        const u32 xo = (u32)((lane >> 3) * 1152 + (((lane & 7) ^ (lane >> 3)) << 4));
        u32 wv[3];
#pragma unroll
        for (int j = 0; j < 3; ++j) {
            u32 pos, piece;
            pos5((w + 4 * j) * 64 + lane, pos, piece);
            wv[j] = pos2row(pos) * 3456 + (pos >> 6) * 1152 + piece * 16;
        }
        const int c0 = first_chunk(hp);
        P.arrive<0>();
        STAMP(40);
        glds16x4(xo, xo + 8192, xo + 16384, xo + 24576, (const char*)P.x + (rt * 128 + w * 32) * 1152 + c0 * 128, lbase + L_XRING + w * 4096);
        const char* wsrc = (const char*)P.wqkv + 72 * hp + c0 * 221184 + 1144;
        glds16(wv[0], wsrc, lbase + L_WRING + w * 1024);
        glds16(wv[1], wsrc, lbase + L_WRING + w * 1024 + 4096);
        if (w < 2) glds16(wv[2], wsrc, lbase + L_WRING + w * 1024 + 8192);
    }
    const rsrc_t rs = P.rs;
    STAMP(41);
    RAW_BARRIER();
    STAMP(42);
    if (EXP & 3) return;
    __builtin_amdgcn_s_setprio(2);
    *(LAS u32x4*)(L + L_AIMG + w * 1024 + lane * 16) = u32x4{0, 0, 0, 0};
    if (lane < 48) *(LAS u32x2*)(L + L_AIMG + 4096 + w * 384 + lane * 8) = u32x2{0, 0};
    f32x4 acc[2][5];
    const bool kl = !(m & 4);
#pragma unroll
    for (int ct = 0; ct < 5; ++ct) {
        float iv = ct < 2 ? ONE_EPS : (ct < 4 ? 0.f : (kl ? ONE_EPS : 0.f));
        acc[0][ct] = f32x4{iv, iv, iv, iv};
        acc[1][ct] = acc[0][ct];
    }
    lptr XA0 = L + L_XRING + w * 4096 + m * 128 + ((kg ^ (m & 7)) << 4);
    lptr XA1 = L + L_XRING + w * 4096 + m * 128 + (((4 + kg) ^ (m & 7)) << 4);
    const int ra = (8 * kg + 2 * ((lane >> 2) & 3)) * 80;
    lptr WA = L + L_WRING + ra + (lane & 3) * 8 + 8;
    lptr WL = L + L_WRING + ra + (lane & 1) * 5120 + 40;
    volatile LAS u32* FULL = (volatile LAS u32*)(L + L_FLAG + 40);
    volatile LAS u32* FREE = (volatile LAS u32*)(L + L_FLAG + w * 4);
    u32x4 X[18][2];
    u32x2 B[2][5][2];
    auto rdx = [&](int i, int e) {
        X[2 * i + (e >> 1)][e & 1] = *(LAS u32x4*)(((e >> 1) ? XA1 : XA0) + (i % 3) * 16384 + (e & 1) * 2048);
    };
    auto rdb = [&](int i, int ks, int q) {
        const int o = (i % 3) * 10240 + ks * 2560;
        lptr p = q == 2 ? WL + o : WA + o + (q >= 3 ? 5120 : 0) + ((q == 1 || q == 4) ? 40 : 0);
        B[ks][q][0] = trread(p);
        B[ks][q][1] = trread(p + 80);
    };
    auto mm = [&](int k, int s) {
        constexpr int ord[5] = {0, 1, 4, 2, 3};
        const int q = s >> 1, r = s & 1;
        acc[r][ord[q]] = mfma32(X[k][r], cat(B[k & 1][q][0], B[k & 1][q][1]), acc[r][ord[q]]);
    };
    lptr XTA[3];
    u32 xts = WS_XT + (hp * 12 + min(kg, 2) * 4) * 16384 + (1008 - 64 * rt - 16 * w + m) * 16;
#pragma unroll
    for (int e = 0; e < 3; ++e) {
        const int B = ((72 * hp) & 127) + 24 * min(kg, 2) + 8 * e, r = 30 - 2 * m;
        XTA[e] = L + L_XRING + (B >> 7) * 16384 + w * 4096 + r * 128 + ((((B >> 4) ^ r) & 7) << 4) + (B & 8);
        PIN(XTA[e]);
    }
    PIN(xts);
    u32x2 ev[3], od[3];
    u32 tag = 0;
    WAIT_V(0);
    flag_set(L, w, 1);
    quad_wait(L + L_FLAG, 1);
    STAMP(1);
#pragma unroll
    for (int e = 0; e < 4; ++e) rdx(0, e);
#pragma unroll
    for (int q = 0; q < 5; ++q) rdb(0, 0, q);
    SCHED();
#pragma unroll
    for (int i = 0; i < 9; ++i) {
        u32 f = 0;
#pragma unroll
        for (int s = 0; s < 10; ++s) {
            mm(2 * i, s);
            if (s == 1 && i) { WAIT_L(12); *FREE = 2 * i + 2; }
            if (i == 1 && s >= 5 && s < 9) {
                u32 d[3];
#pragma unroll
                for (int c = 0; c < 3; ++c) {
                    const int q = 3 * (s - 5) + c;
                    d[c] = __builtin_amdgcn_perm(ev[q >> 2][(q >> 1) & 1], od[q >> 2][(q >> 1) & 1], (q & 1) ? 0x07060302u : 0x05040100u);
                }
                st16(rs, seal(d[0], d[1], d[2], tag), xts, (s - 5) * 16384);
            }
            if (s < 5) rdb(i, 1, s);
            if (s == 5 && i < 8) f = *FULL;
            SCHED();
        }
#pragma unroll
        for (int s = 0; s < 10; ++s) {
            mm(2 * i + 1, s);
            if (s == 0 && i < 8) {
                if (i) { WAIT_L0(); *FREE = 2 * i + 3; }
                if (__builtin_amdgcn_readfirstlane(f) < (u32)(i + 2)) flag_wait1(L, 10, i + 2);
                CFENCE();
                if (i == 0) {
#pragma unroll
                    for (int e = 0; e < 3; ++e) { ev[e] = *(LAS u32x2*)(XTA[e]); od[e] = *(LAS u32x2*)((lptr)((u32)(uintptr_t)XTA[e] ^ 16) + 128); }
                    tag = mk_tag(__builtin_amdgcn_readfirstlane(*(volatile LAS u32*)(L + L_FLAG + 32)));
                    WAIT_L0();
                    *FREE = 3;
                }
            }
            if (i < 8) {
                if (s == 1 || s == 2) { rdx(i + 1, 2 * s - 2); rdx(i + 1, 2 * s - 1); }
                if (s >= 3 && s < 8) rdb(i + 1, 0, s - 3);
            }
            SCHED();
        }
        STAMP(16 + i);
    }
    STAMP(2);
    {
        u32x4 op[5];
        const float ca = kl ? EPS : __builtin_inff(), cb = kl ? ONE_EPS : -__builtin_inff();
#pragma unroll
        for (int ct = 0; ct < 5; ++ct) {
            f32x4 v[2];
#pragma unroll
            for (int r = 0; r < 2; ++r)
#pragma unroll
                for (int i = 0; i < 4; ++i) {
                    float a = acc[r][ct][i];
                    if (ct < 2) a = phi(a);
                    if (ct == 4) a = __builtin_amdgcn_fmed3f(__builtin_amdgcn_exp2f(fmaf(a, LOG2E, -ONE_EPS * LOG2E)) + ca, a, cb);
                    v[r][i] = a;
                }
            op[ct] = u32x4{pk2(v[0][0], v[0][1]), pk2(v[0][2], v[0][3]), pk2(v[1][0], v[1][1]), pk2(v[1][2], v[1][3])};
        }
#pragma unroll
        for (int i = 0; i < 4; ++i) op[4][i] = m >= 12 ? 0x3f803f80u : op[4][i];
        const f32x4 Z = {0, 0, 0, 0};
        f32x4 j1[2], j3[2], j5[2], j7;
        j3[0] = mfma32(op[4], op[0], Z);
        j3[1] = mfma32(op[4], op[1], Z);
        j7 = mfma32(op[4], op[4], Z);
        j5[0] = mfma32(op[4], op[2], Z);
        j5[1] = mfma32(op[4], op[3], Z);
        j1[0] = mfma32(op[0], op[2], Z);
        j1[1] = mfma32(op[1], op[3], Z);
        f32x2 ex[2];
#pragma unroll
        for (int h = 0; h < 2; ++h)
#pragma unroll
            for (int i = 0; i < 2; ++i) ex[h][i] = (kg & 1) ? j3[h][2 * h + i] : (kg ? j7[2 * h + i] : j5[h][2 * h + i]);
        lptr K4 = L + L_KVX + lane * 16, K2 = L + L_KVX + 1024 + lane * 8;
        if (w >= 2) {
#pragma unroll
            for (int h = 0; h < 2; ++h) {
                *(LAS f32x4*)(K4 + (2 * w - 2 + h) * 1536) = j1[h];
                *(LAS f32x2*)(K2 + (2 * w - 2 + h) * 1536) = ex[h];
            }
            flag_set(L, w, 20);
        } else {
            *(LAS f32x4*)(K4 + w * 1536) = w ? j1[0] : j1[1];
            *(LAS f32x2*)(K2 + w * 1536) = w ? ex[0] : ex[1];
            flag_set(L, w, 20);
            f32x4 s4 = w ? j1[1] : j1[0];
            f32x2 s2 = w ? ex[1] : ex[0];
            quad_wait(L + L_FLAG, 20);
            s4 += *(LAS f32x4*)(K4 + (1 - w) * 1536);
            s2 += *(LAS f32x2*)(K2 + (1 - w) * 1536);
            f32x4 t4 = *(LAS f32x4*)(K4 + (2 + w) * 1536) + *(LAS f32x4*)(K4 + (4 + w) * 1536);
            f32x2 t2 = *(LAS f32x2*)(K2 + (2 + w) * 1536) + *(LAS f32x2*)(K2 + (4 + w) * 1536);
            s4 += t4;
            s2 += t2;
            st16(rs, seal(pk2(s4[0], s4[1]), pk2(s4[2], s4[3]), pk2(s2[0], s2[1]), tag), kg * 4096 + m * 16, WS_KVG + (hp * 8 + w * 4) * 4096 + rt * 256);
        }
    }
    STAMP(3);
    const u32 lo = lane * 16;
    u32x4 wff = ld16(rs, WS_WFF + min(lane, 40) * 16, 0);
    flag_wait1(L, 10, 10);
    STAMP(4);
    f32x4 q[2][3];
#pragma unroll
    for (int r = 0; r < 2; ++r)
#pragma unroll
        for (int t = 0; t < 3; ++t) q[r][t] = f32x4{ONE_EPS, ONE_EPS, ONE_EPS, ONE_EPS};
    u32x4 g[8], WF[45];
    const int grp = 9 * (cq >> 1) + 5 * (cq & 1) + w;
    const u32 wf_main = WS_WF + grp * 36864;
    const u32 wf_ext = WS_WF + (((9 * (cq >> 1) + 4) * 18 + 9 * (w >> 1)) * 2 + (cq & 1)) * 1024;
    auto ldwf = [&](int i) { WF[i] = i < 9 ? ld16(rs, lo, wf_ext + i * 2048) : ld16(rs, lo, wf_main + (i - 9) * 1024); };
    {
        lptr QA = L + L_XRING + ra + (lane & 3) * 8, QL = L + L_XRING + ra + 32;
        u32x2 a[2][3][2];
        auto rdq = [&](int k, int t) {
            const int i = k >> 1, o = (i / 3) * 16384 + (i % 3) * 5120 + (k & 1) * 2560;
            lptr p = t == 2 ? QL + o : QA + o + t * 40;
            a[k & 1][t][0] = trread(p);
            a[k & 1][t][1] = trread(p + 80);
        };
        rdq(0, 0); rdq(0, 1); rdq(0, 2);
        SCHED();
#pragma unroll
        for (int k = 0; k < 18; ++k) {
            if (k == 3) {
#pragma nounroll
                for (u32 it = 0; !wf_ok(wff, lane, tag) && it < SPIN_MAX; ++it) {
                    __builtin_amdgcn_s_sleep(8);
                    wff = ld16(rs, WS_WFF + min(lane, 40) * 16, 0);
                }
                CFENCE();
            }
#pragma unroll
            for (int s = 0; s < 6; ++s) {
                const int t = s >> 1, r = s & 1;
                q[r][t] = mfma32(cat(a[k & 1][t][0], a[k & 1][t][1]), X[k][r], q[r][t]);
                if (s < 3 && k < 17) rdq(k + 1, s);
                if ((s == 3 || s == 5) && k >= 4 && k < 17) ldwf(2 * (k - 4) + (s == 5));
                SCHED();
            }
        }
    }
#pragma unroll
    for (int i = 0; i < 8; ++i) g[i] = ld16(rs, lo, WS_KVG + (hp * 8 + 2 * w) * 4096 + i * 1024);
    SCHED();
    STAMP(5);
    u32x4 pq1[2], pq2[2];
#pragma unroll
    for (int r = 0; r < 2; ++r) {
        u32 p[6];
#pragma unroll
        for (int t = 0; t < 3; ++t) {
            p[2 * t] = pk2(phi(q[r][t][0]), phi(q[r][t][1]));
            p[2 * t + 1] = pk2(phi(q[r][t][2]), phi(q[r][t][3]));
        }
        pq1[r] = u32x4{p[0], p[1], p[2], p[3]};
        pq2[r] = u32x4{p[4], p[5], 0, 0};
    }
    u32x4 sel[2];
    {
        const u32 one = (m & 1) ? 0x3f800000u : 0x3f80u;
#pragma unroll
        for (int s = 0; s < 2; ++s)
#pragma unroll
            for (int i = 0; i < 4; ++i) sel[s][i] = (i < 3 && (m >> 1) == 4 * s + i) ? one : 0u;
    }
    const int hd = w >> 1, kgp = 2 * (w & 1) + (kg >> 1);
    int a0, a1;
    {
        const int DUMP = 38 * 144;
        const int rowE = (20 * hd + m) * 144, ex0 = (16 + 2 * hd) * 144, den = (36 + hd) * 144;
        const int cd = 16 * (m >> 2) + 8 * hd + 2 * (m & 3), cx = 64 + 4 * hd;
        if (!(kg & 1)) {
            a0 = rowE + 16 * kgp + 8 * hd; a1 = 0;
        } else if (kgp == 0) {
            a0 = rowE + cx; a1 = a0 + 2;
        } else if (kgp == 1) {
            a0 = ex0 + cd; a1 = a0 + 144;
        } else if (kgp == 2) {
            a0 = (m >> 1) == 2 + hd ? ex0 + (m & 1) * 144 + cx : (m == 12 ? den + cx : DUMP); a1 = a0 + 2;
        } else {
            a0 = den + cd; a1 = DUMP;
        }
    }
    lptr A0, A2;
    {
        const int kr = m >> 2, i = m & 3;
        A0 = L + L_AIMG + (2 * kr + (i & 1) + 8 * (i >> 1)) * 144 + kg * 16;
        A2 = L + L_AIMG + (i == 0 ? 32 + kr : 36 + ((i - 1) & 1)) * 144 + kg * 16;
    }
    a0 += L_AIMG; a1 += L_AIMG;
    u32 vog = lo + WS_OG + ((rt * 4 + w) * 16 + hp) * 3072;
    PIN(pq1[0]); PIN(pq1[1]); PIN(pq2[0]); PIN(pq2[1]); PIN(sel[0]); PIN(sel[1]); PIN(a0); PIN(a1); PIN(A0); PIN(A2); PIN(vog);
    SCHED();
    poll_rows<8>(g, rs, lo, WS_KVG + (hp * 8 + 2 * w) * 4096, 1024, tag);
    STAMP(6);
    {
        f32x4 d0 = {0, 0, 0, 0}, d1 = d0;
#pragma unroll
        for (int i = 0; i < 4; ++i) {
            g[i][3] = 0; g[4 + i][3] = 0;
            d0 = mfma32(sel[0], g[i], d0);
            d1 = mfma32(sel[1], g[4 + i], d1);
        }
        d0 += d1;
        const u32 p01 = pk2(d0[0], d0[1]), p23 = pk2(d0[2], d0[3]);
        lptr AI = L;
        if (!(kg & 1)) {
            *(LAS u32x2*)(AI + a0) = u32x2{p01, p23};
        } else {
            *(LAS u16*)(AI + a0) = (u16)p01;
            *(LAS u16*)(AI + a1) = (u16)(p01 >> 16);
        }
    }
    flag_set(L, w, 21);
    quad_wait(L + L_FLAG, 21);
    {
        u32x4 A[3][2];
        A[2][0] = *(LAS u32x4*)(A2); A[2][1] = *(LAS u32x4*)(A2 + 64);
        A[0][0] = *(LAS u32x4*)(A0); A[0][1] = *(LAS u32x4*)(A0 + 64);
        A[1][0] = *(LAS u32x4*)(A0 + 16 * 144); A[1][1] = *(LAS u32x4*)(A0 + 16 * 144 + 64);
        f32x4 o[2][3];
#pragma unroll
        for (int tt = 0; tt < 3; ++tt)
#pragma unroll
            for (int r = 0; r < 2; ++r) {
                const int t = (tt + 2) % 3;
                o[r][t] = mfma32(A[t][0], pq1[r], t == 2 ? f32x4{0, EPS, EPS, 0} : f32x4{0, 0, 0, 0});
                o[r][t] = mfma32(A[t][1], pq2[r], o[r][t]);
            }
        u32 d[2][4];
        float e[2];
#pragma unroll
        for (int r = 0; r < 2; ++r) {
            const float ra = __builtin_amdgcn_rcpf(o[r][2][1]), rb = __builtin_amdgcn_rcpf(o[r][2][2]), rc = kg ? rb : ra;
            d[r][0] = pk2s(o[r][0][0] * ra, o[r][0][1] * ra);
            d[r][1] = pk2s(o[r][0][2] * ra, o[r][0][3] * ra);
            d[r][2] = pk2s(o[r][1][0] * rc, o[r][1][1] * rc);
            d[r][3] = pk2s(o[r][1][2] * rb, o[r][1][3] * rb);
            e[r] = o[r][2][0] * rb;
        }
        st16(rs, seal(d[0][0], d[0][1], d[0][2], tag), vog, 0);
        st16(rs, seal(d[1][0], d[1][1], d[1][2], tag), vog, 1024);
        st16(rs, seal(d[0][3], pk2s(e[0], e[1]), d[1][3], tag), vog, 2048);
    }
    STAMP(7);
#pragma unroll
    for (int i = 26; i < 45; ++i) ldwf(i);
    const int c8 = 32 * (9 * (cq >> 1) + 4) + 16 * (cq & 1);
    lptr IM = L + L_XRING;
    {
        u32x4 og[12];
        const u32 so = WS_OG + (rb * 16 + 4 * w) * 3072;
#pragma unroll
        for (int i = 0; i < 12; ++i) og[i] = ld16(rs, lo, so + i * 1024);
        STAMP(8);
        poll_rows<12>(og, rs, lo, so, 1024, tag);
        STAMP(9);
        lptr B0 = IM + w * 4608 + m * 16;
#pragma unroll
        for (int h = 0; h < 4; ++h) {
            lptr E = B0 + (h >> 1) * 2304 + ((h & 1) ? 1024 + (kg >> 1) * 256 + ((8 + 4 * kg) & 15) : 4 * kg);
            lptr E2 = B0 + (h >> 1) * 2304 + ((h & 1) ? 2048 + 8 : 1024) + 2 * kg;
#pragma unroll
            for (int i = 0; i < 3; ++i) {
                *(LAS u32*)(E + i * 256) = og[3 * h][i];
                *(LAS u32*)(E + 18432 + i * 256) = og[3 * h + 1][i];
            }
            *(LAS u32*)(E + 768) = og[3 * h + 2][0];
            *(LAS u32*)(E + 18432 + 768) = og[3 * h + 2][2];
            *(LAS u16*)(E2) = (u16)og[3 * h + 2][1];
            *(LAS u16*)(E2 + 18432) = (u16)(og[3 * h + 2][1] >> 16);
        }
    }
    SCHED();
    u32x4 bt[4], b8[1];
    const u32 bs = WS_BT + (rb * 576 + grp * 32) * 128, bs8 = WS_BT + (rb * 576 + c8) * 128 + (w & 1) * 16;
    const u32 bv = (lane >> 2) * 128 + (lane & 3) * 32, gs = WS_G + rb * 256;
    u32x2 g0, g1;
    flag_set(L, w, 22);
    quad_wait(L + L_FLAG, 22);
    STAMP(10); STAMPC(60);
    const f32x4 Z = {0, 0, 0, 0};
    f32x4 o[2][2] = {{Z, Z}, {Z, Z}}, a8 = Z;
    lptr EX = L + L_WRING + (w & 1) * 1024 + lo;
    {
        lptr F = IM + lo, F8 = IM + (w & 1) * 18432 + (w >> 1) * 9216 + lo;
        u32x4 f8[9], f[3][2];
#pragma unroll
        for (int k = 0; k < 9; ++k) f8[k] = *(LAS u32x4*)(F8 + k * 1024);
        auto rdf = [&](int k) { f[k % 3][0] = *(LAS u32x4*)(F + k * 1024); f[k % 3][1] = *(LAS u32x4*)(F + 18432 + k * 1024); };
        SCHED();
#pragma unroll
        for (int k = 0; k < 9; ++k) {
            a8 = mfma32(WF[k], f8[k], a8);
            if (k == 2 || k == 4) rdf(k / 2 - 1);
            SCHED();
        }
#pragma unroll
        for (int k = 0; k < 18; ++k) {
            if (k + 2 < 18) rdf(k + 2);
            if (k >= 8 && k < 12) bt[k - 8] = ld16(rs, lo, bs + (k - 8) * 1024);
            if (k == 12) b8[0] = ld16(rs, bv, bs8);
            if (k == 6) g0 = __builtin_amdgcn_raw_buffer_load_b64(rs, m * 8, gs, AUX_SC1);
            if (k == 7) g1 = __builtin_amdgcn_raw_buffer_load_b64(rs, m * 8, gs + 128, AUX_SC1);
#pragma unroll
            for (int r = 0; r < 2; ++r)
#pragma unroll
                for (int t = 0; t < 2; ++t) o[r][t] = mfma32(WF[9 + 2 * k + t], f[k % 3][r], o[r][t]);
            if (k == 1 && w >= 2) { *(LAS f32x4*)(EX) = a8; }
            if (k == 3 && w >= 2) flag_set(L, w, 23);
            SCHED();
        }
    }
    STAMP(11); STAMPC(61);
    poll_rows<4>(bt, rs, lo, bs, 1024, tag);
    lptr BI = L + L_WRING + 2048 + w * 2560;
#pragma unroll
    for (int i = 0; i < 4; ++i) *(LAS u32x2*)(BI + (lane & 1) * 1024 + i * 256 + (lane >> 1) * 8) = u32x2{bt[i][0], bt[i][1]};
    if (w < 2) {
        poll_rows<1>(b8, rs, bv, bs8, 0, tag);
        *(LAS u32x2*)(BI + 2048 + lane * 8) = u32x2{b8[0][0], b8[0][1]};
    }
#pragma nounroll
    for (u32 it = 0; it < SPIN_MAX && !__all((g0[0] ^ g0[1]) == tag && (g1[0] ^ g1[1]) == tag); ++it) {
        __builtin_amdgcn_s_sleep(2);
        g0 = __builtin_amdgcn_raw_buffer_load_b64(rs, m * 8, gs, AUX_SC1);
        g1 = __builtin_amdgcn_raw_buffer_load_b64(rs, m * 8, gs + 128, AUX_SC1);
        CFENCE();
    }
    CFENCE();
    STAMP(12);
    auto mix = [&](float g, f32x4 a, u32x2 b) {
        const float b0 = lo2f(b[0]), b1 = hi2f(b[0]), b2 = lo2f(b[1]), b3 = hi2f(b[1]);
        return u32x2{pk2s(fmaf(g, a[0] - b0, b0), fmaf(g, a[1] - b1, b1)), pk2s(fmaf(g, a[2] - b2, b2), fmaf(g, a[3] - b3, b3))};
    };
    GAS char* orow = (GAS char*)P.out + (rb * 32 + m) * 1152;
#pragma unroll
    for (int r = 0; r < 2; ++r) {
        const float g = __uint_as_float(r ? g1[0] : g0[0]);
        const u32x2 t0 = trread(BI + r * 1024 + kg * 256 + m * 8), t1 = trread(BI + r * 1024 + kg * 256 + 128 + m * 8);
        *(GAS u32x4*)(orow + r * 18432 + grp * 64 + kg * 16) = cat(mix(g, o[r][0], t0), mix(g, o[r][1], t1));
    }
    if (w < 2) {
        flag_wait1(L, w + 2, 23);
        a8 += *(LAS f32x4*)(EX);
        *(GAS u32x2*)(orow + w * 18432 + c8 * 2 + kg * 8) = mix(__uint_as_float(w ? g1[0] : g0[0]), a8, trread(BI + 2048 + lane * 8));
    }
    STAMP(13);
}

struct cf { float r, i; };
DEV cf cmul(cf a, cf b) { return cf{fmaf(a.r, b.r, -(a.i * b.i)), fmaf(a.r, b.i, a.i * b.r)}; }
DEV cf csq(cf a) { return cf{fmaf(a.r, a.r, -(a.i * a.i)), (a.r + a.r) * a.i}; }
DEV cf cexp(float lr, float t) {
    const float e = __builtin_amdgcn_exp2f(lr), f = __builtin_amdgcn_fractf(t);
    return cf{e * __builtin_amdgcn_cosf(f), e * __builtin_amdgcn_sinf(f)};
}

DEV u32 kgen_role(Params& P, lptr L, int cl, int sg, int lane) {
    const int ch = 3 * sg + cl;
    const int m = lane & 15, kg = lane >> 4;
    lptr C = L + L_CH + cl * CH_BYTES;
    const int n = ch * 64 + lane;
    P.arrive<2>();
    u32 are = ((const GAS u32*)P.Are)[n], aim = ((const GAS u32*)P.Aim)[n], bre = ((const GAS u32*)P.Bre)[n];
    u32 bim = ((const GAS u32*)P.Bim)[n], cre = ((const GAS u32*)P.Cre)[n], cim = ((const GAS u32*)P.Cim)[n];
    const u16 ldt = ((const GAS u16*)P.logdt)[ch], dsk = ((const GAS u16*)P.Dskip)[ch];
    SCHED();
    RAW_BARRIER();
    u32 ga[2][20];
#pragma unroll
    for (int it = 0; it < 2; ++it) {
        const int l0 = (it * 64 + lane) * 16 - 4;
#pragma unroll
        for (int j = 0; j < 10; ++j) {
            const int la = l0 + 2 * j, lb = la + 1;
            const u32 bits = pk2((float)la, (float)lb);
            int ia = (int)(bits & 0xffff) - 0x4280, ib = (int)(bits >> 16) - 0x4280;
            if (it == 0) {
                ia = la < 256 ? (la < 0 ? 641 : la) : ia;
                ib = lb < 256 ? (lb < 0 ? 641 : lb) : ib;
            }
            ga[it][2 * j] = L_KT + cl * 1312 + 2 * ia;
            ga[it][2 * j + 1] = L_KT + cl * 1312 + 2 * ib;
        }
    }
#pragma unroll
    for (int it = 0; it < 2; ++it)
#pragma unroll
        for (int j = 0; j < 20; ++j) asm volatile("" : "+v"(ga[it][j]));
    SCHED();
    const float dt = lo2f(pk2(expf(bf2f(ldt)), 0.f));
    STAMP(1); STAMPC(60);
    f32x4 a0 = {lane == 0 ? bf2f(dsk) : 0.f, 0, 0, 0}, a1 = {0, 0, 0, 0}, a2 = a1;
    LAS u32* T = (LAS u32*)(C + CH_KC + (lane >> 2) * TQ + (lane & 3) * 4);
    lptr R = C + CH_KC + kg * TQ + m * 16;
#pragma nounroll
    for (int u = 0; u < 2; ++u) {
        auto hf = [&](u32& v) { const float r = hi2f(v); v <<= 16; return r; };
        const float lr = dt * hf(are) * LOG2E, t = dt * hf(aim) * 0.15915494309189533577f;
        const cf c = cmul(cf{hf(cre), hf(cim)}, cf{dt * hf(bre), dt * hf(bim)});
        cf p[16], A[9];
        p[1] = cexp(lr, t);
        const cf w16 = cexp(16.f * lr, 16.f * t);
        A[1] = cexp(256.f * lr, 256.f * t);
        auto put = [&](int row, cf v) { if (EXP & 4) { u32 z = pk2s(v.r, v.i); asm volatile("" :: "v"(z)); } else T[row * 4] = pk2s(v.r, v.i); };
        T[0] = 0x3f80u;
        put(1, p[1]);
#pragma unroll
        for (int k = 2; k < 16; ++k) {
            p[k] = (k & 1) ? cmul(p[k - 1], p[1]) : csq(p[k / 2]);
            put(k, p[k]);
        }
        {
            const cf w32 = csq(w16);
            cf h0 = cf{c.r, -c.i}, h1 = cmul(c, w16);
            h1.i = -h1.i;
            const cf wc = cf{w32.r, -w32.i};
#pragma unroll
            for (int k = 0; k < 8; ++k) {
                put(41 + 2 * k, h0);
                put(42 + 2 * k, h1);
                if (k < 7) { h0 = cmul(h0, wc); h1 = cmul(h1, wc); }
            }
        }
        A[2] = csq(A[1]); A[3] = cmul(A[2], A[1]); A[4] = csq(A[2]);
        A[5] = cmul(A[4], A[1]); A[6] = csq(A[3]); A[7] = cmul(A[4], A[3]); A[8] = csq(A[4]);
#pragma unroll
        for (int k = 0; k < 8; ++k) put(16 + k, k ? cmul(A[1], p[2 * k]) : A[1]);
#pragma unroll
        for (int k = 0; k < 8; ++k) put(24 + k, (k & 3) ? cmul(A[2 + (k >> 2)], p[4 * (k & 3)]) : A[2 + (k >> 2)]);
#pragma unroll
        for (int k = 0; k < 8; ++k) put(32 + k, (k & 1) ? cmul(A[4 + (k >> 1)], p[8]) : A[4 + (k >> 1)]);
        put(40, A[8]);
        CFENCE();
        STAMPD(8 + u);
#pragma unroll
        for (int s = 0; s < 4; ++s) {
            const u32x4 H = *(LAS u32x4*)(R + s * 4 * TQ + 41 * 16);
            a0 = mfma32(*(LAS u32x4*)(R + s * 4 * TQ), H, a0);
            a1 = mfma32(*(LAS u32x4*)(R + s * 4 * TQ + 256), H, a1);
            a2 = mfma32(*(LAS u32x4*)(R + s * 4 * TQ + 512), H, a2);
        }
        CFENCE();
        STAMPD(6 + u);
    }
    STAMP(2); STAMPC(61);
    const u32 tag = mk_tag(get_epoch(L));
    u32x4 g[6];
    const u32 xs = WS_XT + sg * 16384 + cl * 5120;
#pragma unroll
    for (int i = 0; i < 6; ++i) g[i] = ld16(P.rs, lane * 16, xs + i * 1024);
    SCHED();
    {
        lptr KT = L + L_KT + cl * 1312;
        *(LAS u32x2*)(KT + 2 * (16 * m + 4 * kg)) = u32x2{pk2(a0[0], a0[1]), pk2(a0[2], a0[3])};
        *(LAS u32x2*)(KT + 2 * (kg < 2 ? 256 + 8 * m + 4 * kg : 256 + 64 * kg + 4 * m)) = u32x2{pk2(a1[0], a1[1]), pk2(a1[2], a1[3])};
        if (kg < 2) {
            *(LAS u32*)(KT + 2 * (512 + 64 * kg + 2 * m)) = pk2(a2[0], a2[1]);
            *(LAS u32*)(KT + 2 * (544 + 64 * kg + 2 * m)) = pk2(a2[2], a2[3]);
        }
        if (lane == 32) *(LAS u32*)(KT + 2 * 640) = pk2(a2[0], 0.f);
        CFENCE();
    }
#pragma unroll
    for (int it = 0; it < 2; ++it) {
        u32 v[20], d[10], e[9];
#pragma unroll
        for (int j = 0; j < 20; ++j) v[j] = *(LAS u16*)(L + ga[it][j]);
#pragma unroll
        for (int j = 1; j < 10; ++j) d[j] = v[2 * j] | (v[2 * j + 1] << 16);
#pragma unroll
        for (int j = 0; j < 9; ++j) e[j] = v[2 * j + 1] | (v[2 * j + 2] << 16);
        lptr W = C + CH_KC + 32 + (it * 64 + lane) * 32;
        *(LAS u32x4*)(W) = u32x4{d[2], d[3], d[4], d[5]};
        *(LAS u32x4*)(W + 16) = u32x4{d[6], d[7], d[8], d[9]};
        *(LAS u32x4*)(W + CH_KSTRIDE) = u32x4{e[1], e[2], e[3], e[4]};
        *(LAS u32x4*)(W + CH_KSTRIDE + 16) = u32x4{e[5], e[6], e[7], e[8]};
        *(LAS u32x4*)(W + 2 * CH_KSTRIDE) = u32x4{d[1], d[2], d[3], d[4]};
        *(LAS u32x4*)(W + 2 * CH_KSTRIDE + 16) = u32x4{d[5], d[6], d[7], d[8]};
        *(LAS u32x4*)(W + 3 * CH_KSTRIDE) = u32x4{e[0], e[1], e[2], e[3]};
        *(LAS u32x4*)(W + 3 * CH_KSTRIDE + 16) = u32x4{e[4], e[5], e[6], e[7]};
    }
    if (lane < 8) *(LAS u32x4*)(C + CH_KC + (lane >> 1) * CH_KSTRIDE + (lane & 1) * 16) = u32x4{0, 0, 0, 0};
    *(LAS u32x2*)(C + CH_XR + 4096 + lane * 8) = u32x2{0, 0};
    SCHED();
    STAMP(10);
    poll_rows<6>(g, P.rs, lane * 16, xs, 1024, tag);
    lptr XR = L + L_CH + CH_XR + cl * 1280 + lane * 4;
#pragma unroll
    for (int i = 0; i < 6; ++i)
#pragma unroll
        for (int c = 0; c < 3; ++c) *(LAS u32*)(XR + c * CH_BYTES + i * 256) = g[i][c];
    return tag;
}

template <int MASK, int NA>
DEV void conv_blocks(rsrc_t rs, lptr C, int ch, int lane, u32 tag) {
    const int m = lane & 15, kg = lane >> 4;
    f32x4 acc[8];
#pragma unroll
    for (int i = 0; i < 8; ++i) acc[i] = f32x4{0, 0, 0, 0};
    lptr KA = C + CH_KC + ((~m) & 3) * CH_KSTRIDE + 2 * (8 * kg + ((m + 4) & ~3));
    lptr XB = C + CH_XR + 2 * (240 - 16 * m + 8 * kg);
    u32x4 A[2][NA], B[2][NA];
    auto rd = [&](int j, int a) {
        A[j & 1][a] = cat(*(volatile LAS u32x2*)(KA + 64 * j + 512 * a), *(volatile LAS u32x2*)(KA + 64 * j + 512 * a + 8));
        B[j & 1][a] = *(LAS u32x4*)(XB + 64 * j + 512 * (7 - a));
    };
#pragma unroll
    for (int a = 0; a < NA; ++a) rd(0, a);
    SCHED();
#pragma unroll
    for (int j = 0; j < 8; ++j) {
        int n = 0;
#pragma unroll
        for (int I = 0; I < 8; ++I)
            if (MASK >> I & 1)
#pragma unroll
                for (int a = 0; a <= I; ++a) {
                    acc[I] = mfma32(A[j & 1][a], B[j & 1][I - a], acc[I]);
                    if (j < 7 && !(n & 1) && (n >> 1) < NA) rd(j + 1, n >> 1);
                    ++n;
                    SCHED();
                }
    }
    const u32 vo = (m >> 1) * 73728 + kg * 32 + (m & 1) * 16;
#pragma unroll
    for (int I = 0; I < 8; ++I)
        if (MASK >> I & 1) st16(rs, seal(pk2(acc[I][0], acc[I][1]), pk2(acc[I][2], acc[I][3]), 0u, tag), vo, WS_BT + I * 8 * 73728 + ch * 128);
}

DEV void gate_role(const Params& P, rsrc_t rs, int lane, int rb, int ps, u32 tag) {
    const GAS char* gw = (const GAS char*)P.gw + (lane & 3) * 16;
    {
        const int row = rb * 32 + ps * 16 + (lane >> 2);
        const GAS char* xr = (const GAS char*)P.x + row * 1152 + (lane & 3) * 16;
        u32x4 xv[18], gv[18];
#pragma unroll
        for (int i = 0; i < 18; ++i) { xv[i] = *(const GAS u32x4*)(xr + i * 64); gv[i] = *(const GAS u32x4*)(gw + i * 64); }
        float s0 = 0.f, s1 = 0.f;
#pragma unroll
        for (int i = 0; i < 18; ++i)
#pragma unroll
            for (int j = 0; j < 4; ++j) {
                s0 = fmaf(lo2f(xv[i][j]), lo2f(gv[i][j]), s0);
                s1 = fmaf(hi2f(xv[i][j]), hi2f(gv[i][j]), s1);
            }
        float s = s0 + s1;
        s += __shfl_xor(s, 1);
        s += __shfl_xor(s, 2);
        const u32 g = __float_as_uint(1.0f / (1.0f + __expf(-s)));
        if ((lane & 3) == 0) __builtin_amdgcn_raw_buffer_store_b64(u32x2{g, g ^ tag}, rs, WS_G + row * 8, 0, AUX_SC1);
    }
}

DEV u32 wf_role(Params& P, lptr L, u32 lbase, int cl, int lane, int unit) {
    const int ks = unit / 9, gp = unit - ks * 9;
    P.arrive<3>();
    const rsrc_t rs = P.rs;
    if (unit < 162) {
        const char* src = (const char*)P.wo + ks * 32 * 1152 + gp * 128;
#pragma unroll
        for (int i = 0; i < 4; ++i) glds16((u32)((lane >> 3) * 1152 + (lane & 7) * 16), src + i * 8 * 1152, lbase + L_CH + cl * CH_BYTES + i * 1040);
    }
    RAW_BARRIER();
    if (unit >= 162) return mk_tag(get_epoch(L));
    WAIT_V(0);
    lptr S = L + L_CH + cl * CH_BYTES + (lane >> 4) * 1040 + ((lane >> 2) & 3) * 128;
#pragma unroll
    for (int h = 0; h < 2; ++h) {
        const int grp = 2 * gp + h;
        const bool sp = grp == 4 || grp == 13;
        lptr p = S + h * 64 + (lane & 3) * (sp ? 8 : 16);
#pragma unroll
        for (int t = 0; t < 2; ++t) {
            lptr q = p + t * (sp ? 32 : 8);
            st16(rs, cat(trread(q), trread(q + 512)), lane * 16, WS_WF + ((grp * 18 + ks) * 2 + t) * 1024);
        }
    }
    SCHED();
    const u32 tag = mk_tag(get_epoch(L));
    WAIT_V(0);
    if (lane == 0) __builtin_amdgcn_raw_buffer_store_b32(tag, rs, WS_WFF + unit * 4, 0, AUX_SC1);
    return tag;
}

__global__ __launch_bounds__(512, 2) void fused(Args A) {
    Params P;
    P.fetch();
    P.tsv = 0;
    { u64 t_ = __builtin_amdgcn_s_memrealtime(); P.tsv = threadIdx.x & 63 ? 0u : (u32)t_; }
    extern __shared__ __attribute__((aligned(64))) char smem[];
    lptr L = (lptr)smem;
    const u32 lbase = (u32)(uintptr_t)L;
    const int tid = threadIdx.x, lane = tid & 63, w = __builtin_amdgcn_readfirstlane(tid >> 6);
    const int bid = blockIdx.x, xcd = bid & 7, slot = bid >> 3;
    if (tid < 16) *(LAS u32*)(L + L_FLAG + tid * 4) = tid == 7;
    const int rt = (xcd >> 1) * 4 + (slot & 3), hp = (xcd & 1) * 8 + (slot >> 2);
    const int rb = 8 * xcd + (slot & 7), cq = slot >> 3, cg = w & 3;
    if (w < 4) {
        gemm_role(P, L, lbase, w, lane, rt, hp, rb, cq);
        if (bid == 0 && tid == 0) {
            const u32 ep = *(volatile LAS u32*)(L + L_FLAG + 32);
            __builtin_amdgcn_raw_buffer_store_b32(ep + 1, P.rs, WS_EPOCH, 0, AUX_SC1);
        }
    } else if (w == 7) {
        const u32 tag = loader_role(P, L, lbase, lane, rt, hp);
        if (slot < 24) {
            const int sg = xcd * 24 + slot;
            quad_wait(L + L_FLAG + 16, 1);
            STAMP(3);
#pragma nounroll
            for (int c = 0; c < 3; ++c) {
                STAMP(4);
                conv_blocks<0x0E, 4>(P.rs, L + L_CH + c * CH_BYTES, 3 * sg + c, lane, tag);
            }
            STAMP(5);
        }
    } else if (slot < 24) {
        const int sg = xcd * 24 + slot;
        const u32 tag = kgen_role(P, L, cg, sg, lane);
        flag_set(L, w, 1);
        STAMP(3);
        quad_wait(L + L_FLAG + 16, 1);
        STAMP(4);
        conv_blocks<0xF1, 8>(P.rs, L + L_CH + cg * CH_BYTES, 3 * sg + cg, lane, tag);
        STAMP(5);
    } else {
        const u32 tag = wf_role(P, L, lbase, cg, lane, ((slot - 24) * 8 + xcd) * 3 + cg);
        STAMP(3);
        if (cg < 2) {
            flag_wait1(L, 10, 10);
            gate_role(P, P.rs, lane, rb, cg, tag);
        }
        STAMP(4);
    }
    WAIT_V(0);
    STAMP(14);
    ((GAS u32*)(P.ws + WS_DBG))[(bid * 8 + w) * 64 + lane] = P.tsv;

    if (bid == DIAGB && w == 0) {
        const u32 t0 = __builtin_amdgcn_readlane(P.tsv, 0), tk = __builtin_amdgcn_readlane(P.tsv, DIAG);
        u32 iv = tk - t0;
        if (iv > 5000) iv = 5000;
        const u32 end = 3000 + 4 * iv;
        for (int it = 0; it < 1000000; ++it) { if ((u32)__builtin_amdgcn_s_memrealtime() - t0 >= end) break; __builtin_amdgcn_s_sleep(1); }
    }
}

extern "C" void kernel_launch(void* const* d_in, const int* in_sizes, int n_in, void* d_out, int out_size, void* d_ws,
                              size_t ws_size, hipStream_t stream) {
    Args P;
    P.x = (const u16*)d_in[0];
    P.wqkv = (const u16*)d_in[1];
    P.wo = (const u16*)d_in[2];
    P.Are = (const u16*)d_in[4];
    P.Aim = (const u16*)d_in[5];
    P.Bre = (const u16*)d_in[6];
    P.Bim = (const u16*)d_in[7];
    P.Cre = (const u16*)d_in[8];
    P.Cim = (const u16*)d_in[9];
    P.logdt = (const u16*)d_in[10];
    P.Dskip = (const u16*)d_in[11];
    P.gw = (const u16*)d_in[12];
    P.ws = (char*)d_ws;
    P.out = (u16*)d_out;
    (void)hipFuncSetAttribute((const void*)fused, hipFuncAttributeMaxDynamicSharedMemorySize, LDS_BYTES);
    fused<<<256, 512, LDS_BYTES, stream>>>(P);
}
```

```cpp
#include <hip/hip_runtime.h>
#include <hip/hip_bf16.h>
#include <stdint.h>

typedef unsigned short u16;
typedef unsigned int u32;
typedef unsigned long long u64;
typedef u32 u32x4 __attribute__((ext_vector_type(4)));
typedef u32 u32x2 __attribute__((ext_vector_type(2)));
typedef float f32x4 __attribute__((ext_vector_type(4)));
typedef float f32x2 __attribute__((ext_vector_type(2)));
typedef __bf16 bf16x8 __attribute__((ext_vector_type(8)));
typedef __bf16 bf16x2 __attribute__((ext_vector_type(2)));
typedef short s16x4 __attribute__((ext_vector_type(4)));
#define LAS __attribute__((address_space(3)))
#define GAS __attribute__((address_space(1)))
#define DEV __device__ __forceinline__
typedef LAS char* lptr;
typedef __amdgpu_buffer_rsrc_t rsrc_t;

constexpr u32 WS_EPOCH = 0;
constexpr u32 WS_WFF = 256;
constexpr u32 WS_G = 4096;
constexpr u32 WS_KVG = 65536;
constexpr u32 WS_WF = WS_KVG + 524288;
constexpr u32 WS_XT = WS_WF + 663552;
constexpr u32 WS_OG = WS_XT + 3145728;
constexpr u32 WS_BT = WS_OG + 3145728;
constexpr u32 WS_DBG = WS_BT + 4718592;
constexpr u32 WS_END = WS_DBG + 1048576;
constexpr int AUX_SC1 = 16;
#define EXP 0
#define SPIN_LOG 17
constexpr u32 SPIN_MAX = 1u << SPIN_LOG;

constexpr u32 L_FLAG = 0;
constexpr u32 CH_KC = 0, CH_KSTRIDE = 4160, CH_XR = 16640, CH_BYTES = 21248;
constexpr u32 L_CH = 64;
constexpr u32 L_KT = L_CH + 3 * CH_BYTES;
constexpr u32 L_XRING = L_KT + 3 * 1312;
constexpr u32 L_WRING = L_XRING + 49152;
constexpr u32 L_AIMG = L_WRING + 30720;
constexpr u32 LDS_BYTES = L_AIMG + 5632;
constexpr u32 L_KVX = L_WRING;
constexpr u32 TQ = 944;
static_assert(LDS_BYTES <= 163840, "lds");
static_assert(L_XRING % 32 == 0, "align");

DEV float bf2f(u16 v) { return __uint_as_float(((u32)v) << 16); }
DEV u32 pk2(float a, float b) {
    f32x2 v = {a, b};
    return __builtin_bit_cast(u32, __builtin_convertvector(v, bf16x2));
}
DEV u32 pk2s(float a, float b) {
    u32 r;
    asm("v_cvt_pk_bf16_f32 %0, %1, %2" : "=v"(r) : "v"(a), "v"(b));
    return r;
}
DEV float lo2f(u32 v) { return __uint_as_float(v << 16); }
DEV float hi2f(u32 v) { return __uint_as_float(v & 0xffff0000u); }
DEV f32x4 mfma32(u32x4 a, u32x4 b, f32x4 c) {
    return __builtin_amdgcn_mfma_f32_16x16x32_bf16(__builtin_bit_cast(bf16x8, a), __builtin_bit_cast(bf16x8, b), c, 0, 0, 0);
}
DEV void glds16(u32 voff, const void* sbase, u32 ldsaddr) {
    asm volatile("s_mov_b32 m0, %2\n\ts_nop 0\n\tglobal_load_lds_dwordx4 %0, %1" ::"v"(voff), "s"(sbase), "s"(ldsaddr) : "memory");
}
DEV void glds16x4(u32 v0, u32 v1, u32 v2, u32 v3, const void* sbase, u32 ldsaddr) {
    asm volatile("s_mov_b32 m0, %5\n\ts_nop 0\n\tglobal_load_lds_dwordx4 %0, %4\n\tglobal_load_lds_dwordx4 %1, %4 offset:1024\n\t"
                 "global_load_lds_dwordx4 %2, %4 offset:2048\n\tglobal_load_lds_dwordx4 %3, %4 offset:3072" ::"v"(v0), "v"(v1), "v"(v2), "v"(v3),
                 "s"(sbase), "s"(ldsaddr) : "memory");
}
DEV u32x2 trread(lptr p) { return __builtin_bit_cast(u32x2, __builtin_amdgcn_ds_read_tr16_b64_v4i16((LAS s16x4*)p)); }
DEV u32x4 cat(u32x2 a, u32x2 b) { return u32x4{a[0], a[1], b[0], b[1]}; }
DEV u32x4 ld16(rsrc_t rs, u32 voff, u32 soff) { return __builtin_amdgcn_raw_buffer_load_b128(rs, voff, soff, AUX_SC1); }
DEV void st16(rsrc_t rs, u32x4 v, u32 voff, u32 soff) { __builtin_amdgcn_raw_buffer_store_b128(v, rs, voff + soff, 0, AUX_SC1); }
DEV u32 xor3(u32 a, u32 b, u32 c) { return __builtin_amdgcn_bitop3_b32(a, b, c, 0x96); }
DEV u32x4 seal(u32 a, u32 b, u32 c, u32 tag) { return u32x4{a, b, c, xor3(a, b, tag) ^ c}; }
DEV u32 unseal(u32x4 g, u32 tag) { return xor3(xor3(g[0], g[1], tag), g[2], g[3]); }
#define WAIT_V(n) asm volatile("s_waitcnt vmcnt(%0)" ::"n"(n) : "memory")
#define WAIT_L(n) asm volatile("s_waitcnt lgkmcnt(%0)" ::"n"(n) : "memory")
#define WAIT_L0() asm volatile("s_waitcnt lgkmcnt(0)" ::: "memory")
#define RAW_BARRIER() asm volatile("s_waitcnt lgkmcnt(0)\n\ts_barrier" ::: "memory")
#define CFENCE() asm volatile("" ::: "memory")
#define SCHED() __builtin_amdgcn_sched_barrier(0)

#define STAMP(i) do {} while (0)
#define STAMPD(i) do {} while (0)
#define STAMPC(i) do {} while (0)

DEV void flag_set(lptr L, int wave, u32 v) {
    WAIT_L0();
    *(volatile LAS u32*)(L + L_FLAG + wave * 4) = v;
}
DEV void flag_wait1(lptr L, int wave, u32 v) {
#pragma nounroll
    for (u32 it = 0; it < SPIN_MAX * 8; ++it) {
        u32 f = *(volatile LAS u32*)(L + L_FLAG + wave * 4);
        if (__builtin_amdgcn_readfirstlane(f) >= v) break;
        __builtin_amdgcn_s_sleep(1);
    }
    CFENCE();
}
DEV bool quad_ok(u32x4 f, u32 v) {
    u32 m = min(min(f[0], f[1]), min(f[2], f[3]));
    return __builtin_amdgcn_readfirstlane(m) >= v;
}
DEV void quad_wait(lptr Q, u32 v) {
#pragma nounroll
    for (u32 it = 0; it < SPIN_MAX * 8; ++it) {
        u32x4 f = *(volatile LAS u32x4*)(Q);
        if (quad_ok(f, v)) break;
        __builtin_amdgcn_s_sleep(1);
    }
    CFENCE();
}
DEV void quad_spin(lptr Q, u32 v) {
#pragma nounroll
    for (u32 it = 0; it < SPIN_MAX * 16; ++it) {
        u32x4 f = *(volatile LAS u32x4*)(Q);
        if (quad_ok(f, v)) break;
        if (EXP & 8) __builtin_amdgcn_s_sleep(1);
    }
    CFENCE();
}
DEV u32 mk_tag(u32 epoch) { return epoch * 0x9E3779B1u + 0x7F4A7C15u; }
DEV void put_epoch(lptr L, u32 epoch) { *(volatile LAS u32x2*)(L + L_FLAG + 32) = u32x2{epoch, 1u}; }
DEV u32 get_epoch(lptr L) {
    u32x2 v;
#pragma nounroll
    for (u32 it = 0; it < SPIN_MAX * 8; ++it) {
        v = *(volatile LAS u32x2*)(L + L_FLAG + 32);
        if (__builtin_amdgcn_readfirstlane(v[1])) break;
        __builtin_amdgcn_s_sleep(1);
    }
    CFENCE();
    return __builtin_amdgcn_readfirstlane(v[0]);
}
template <int N>
DEV bool rows_bad(u32x4 (&g)[N], u32 tag) {
    u32 bad = 0;
#pragma unroll
    for (int i = 0; i < N; ++i) bad |= unseal(g[i], tag);
    return __any(bad != 0);
}
template <int N>
DEV void poll_rows(u32x4 (&g)[N], rsrc_t rs, u32 voff, u32 soff, u32 stride, u32 tag) {
    bool bad = rows_bad(g, tag);
#pragma nounroll
    for (u32 it = 0; __builtin_expect(bad, 0) && it < SPIN_MAX; ++it) {
#pragma unroll
        for (int i = 0; i < N; ++i)
            if (__any(unseal(g[i], tag) != 0))
                asm volatile("buffer_load_dwordx4 %0, %1, %2, %3 offen sc1" : "+v"(g[i]) : "v"(voff), "s"(rs), "s"(soff + i * stride) : "memory");
        WAIT_V(0);
#pragma unroll
        for (int i = 0; i < N; ++i) asm volatile("" : "+v"(g[i]));
        bad = rows_bad(g, tag);
    }
    CFENCE();
}
#define PIN(x) asm volatile("" : "+v"(x))

struct Args {
    const u16 *x, *wqkv, *wo, *Are, *Aim, *Bre, *Bim, *Cre, *Cim, *logdt, *Dskip, *gw;
    char* ws;
    u16* out;
};
typedef u64 u64x8 __attribute__((ext_vector_type(8)));
typedef u64 u64x4 __attribute__((ext_vector_type(4)));
typedef u64 u64x2 __attribute__((ext_vector_type(2)));
struct Params : Args {
    u64x8 k0; u64x4 k1; u64x2 k2;
    rsrc_t rs;
    DEV void fetch() {
        asm volatile("s_load_dwordx16 %0, %3, 0x0\n\ts_load_dwordx8 %1, %3, 0x40\n\ts_load_dwordx4 %2, %3, 0x60"
                     : "=&s"(k0), "=&s"(k1), "=&s"(k2) : "s"(__builtin_amdgcn_kernarg_segment_ptr()));
    }
    DEV static u64 take(u64 v) {
        u64 r;
        asm volatile("s_mov_b64 %0, %1" : "=s"(r) : "s"(v));
        return r;
    }
    template <int ROLE>
    DEV void arrive() {
        asm volatile("s_waitcnt lgkmcnt(0)" ::"s"(k0), "s"(k1), "s"(k2));
        if (ROLE != 2) x = (const u16*)take(k0[0]);
        if (ROLE < 2) wqkv = (const u16*)take(k0[1]);
        if (ROLE == 3) { wo = (const u16*)take(k0[2]); gw = (const u16*)take(k1[3]); }
        if (ROLE == 2) {
            Are = (const u16*)take(k0[3]); Aim = (const u16*)take(k0[4]); Bre = (const u16*)take(k0[5]); Bim = (const u16*)take(k0[6]);
            Cre = (const u16*)take(k0[7]); Cim = (const u16*)take(k1[0]); logdt = (const u16*)take(k1[1]); Dskip = (const u16*)take(k1[2]);
        }
        ws = (char*)take(k2[0]);
        if (ROLE == 0) out = (u16*)take(k2[1]);
        rs = __builtin_amdgcn_make_buffer_rsrc((void*)ws, 0, (int)WS_END, 0x00020000);
    }
};
DEV void pos5(u32 idx, u32& pos, u32& piece) {
    pos = (idx * 52429u) >> 18;
    piece = idx - 5 * pos;
}
DEV u32 pos2row(u32 pos) { return (pos & 56) | ((pos >> 1) & 3) | ((pos & 1) << 2); }

DEV int first_chunk(int hp) { return (9 * hp) >> 4; }
DEV u32 loader_role(Params& P, lptr L, u32 lbase, int lane, int rt, int hp) {
    u32 wv[5], xv[4];
#pragma unroll
    for (int p = 0; p < 5; ++p) {
        u32 pos, piece;
        pos5(p * 64 + lane, pos, piece);
        wv[p] = pos2row(pos) * 3456 + piece * 16 - (p & 3) * 1024;
    }
#pragma unroll
    for (int j = 0; j < 4; ++j) xv[j] = (u32)((lane >> 3) * 1152 + (((lane & 7) ^ (lane >> 3)) << 4)) + j * 8192;
    int c = first_chunk(hp);
    P.arrive<1>();
    u32 epoch;
    asm volatile("global_load_dword %0, %1, %2 sc1" : "=v"(epoch) : "v"(WS_EPOCH), "s"(P.ws) : "memory");
    const char* xg = (const char*)P.x + rt * 128 * 1152;
    const char* wg = (const char*)P.wqkv + 72 * hp;
    auto five = [&](u32(&v)[5], const char* src, u32 dst) {
        glds16x4(v[0], v[1], v[2], v[3], src, dst);
        glds16(v[4], src, dst + 4096);
    };
    const int c0 = c;
    int k = 0;
    auto next = [&]() {
        k = k == 1 ? 2 + 2 * (rt & 3) : (k == 8 ? 2 : k + 1);
        c = c0 + k >= 9 ? c0 + k - 9 : c0 + k;
    };
    auto ldx = [&](u32 sl) {
#pragma unroll
        for (int w = 0; w < 4; ++w) glds16x4(xv[0], xv[1], xv[2], xv[3], xg + c * 128 + w * 36864, lbase + L_XRING + sl * 16384 + w * 4096);
    };
    auto ldw = [&](u32 sl) {
        five(wv, wg + c * 221184 + 1144, lbase + L_WRING + sl * 10240);
        five(wv, wg + c * 221184 + 2296, lbase + L_WRING + sl * 10240 + 5120);
    };
    volatile LAS u32* FULL = (volatile LAS u32*)(L + L_FLAG + 40);
    next();
    ldx(1);
    ldw(1);
    RAW_BARRIER();
    __builtin_amdgcn_s_setprio(3);
    asm volatile("s_waitcnt vmcnt(26)" : "+v"(epoch)::"memory");
    const u32 ep = __builtin_amdgcn_readfirstlane(epoch);
    put_epoch(L, ep);
    STAMP(1);
    if (EXP & 1) { WAIT_V(0); __builtin_amdgcn_s_setprio(0); STAMP(2); return mk_tag(ep); }
    next();
    ldx(2);
    WAIT_V(16);
    *FULL = 2;
    ldw(2);
    u32 sl = 0;
#pragma nounroll
    for (u32 j = 3; j < 9; ++j) {
        next();
        if (!(EXP & 2)) quad_spin(L + L_FLAG, 2 * j - 4);
        ldx(sl);
        if (j == 3) { WAIT_V(16); *FULL = 3; }
        if (!(EXP & 2)) quad_spin(L + L_FLAG, 2 * j - 3);
        ldw(sl);
        WAIT_V(26);
        *FULL = j;
        sl = sl == 2 ? 0 : sl + 1;
    }
    k = -1;
#pragma nounroll
    for (u32 s = 0; s < 3; ++s) {
        if (!(EXP & 2)) quad_spin(L + L_FLAG, 14 + 2 * s);
#pragma unroll
        for (int i = 0; i < 3; ++i) {
            next();
            five(wv, wg + c * 221184, lbase + L_XRING + s * 16384 + i * 5120);
        }
        if (s == 0) {
            if (!(EXP & 2)) quad_spin(L + L_FLAG, 15);
            WAIT_V(15);
            *FULL = 9;
        }
    }
    WAIT_V(0);
    *FULL = 10;
    __builtin_amdgcn_s_setprio(0);
    STAMP(2);
    return mk_tag(ep);
}

constexpr float LOG2E = 1.44269504088896340736f;
constexpr float EPS = 1e-6f;
constexpr float ONE_EPS = 1.0f + 1e-6f;
DEV float phi(float a) {
    float e = __builtin_amdgcn_exp2f(fmaf(a, LOG2E, -ONE_EPS * LOG2E));
    return __builtin_amdgcn_fmed3f(e + EPS, a, ONE_EPS);
}

DEV bool wf_ok(u32x4 f, int lane, u32 tag) { return __all(f[0] == tag && f[1] == tag && (lane >= 40 || (f[2] == tag && f[3] == tag))); }

DEV void gemm_role(Params& P, lptr L, u32 lbase, int w, int lane, int rt, int hp, int rb, int cq) {
    const int m = lane & 15, kg = lane >> 4;
    {
        const u32 xo = (u32)((lane >> 3) * 1152 + (((lane & 7) ^ (lane >> 3)) << 4));
        u32 wv[3];
#pragma unroll
        for (int j = 0; j < 3; ++j) {
            u32 pos, piece;
            pos5((w + 4 * j) * 64 + lane, pos, piece);
            wv[j] = pos2row(pos) * 3456 + (pos >> 6) * 1152 + piece * 16;
        }
        const int c0 = first_chunk(hp);
        P.arrive<0>();
        STAMP(40);
        glds16x4(xo, xo + 8192, xo + 16384, xo + 24576, (const char*)P.x + (rt * 128 + w * 32) * 1152 + c0 * 128, lbase + L_XRING + w * 4096);
        const char* wsrc = (const char*)P.wqkv + 72 * hp + c0 * 221184 + 1144;
        glds16(wv[0], wsrc, lbase + L_WRING + w * 1024);
        glds16(wv[1], wsrc, lbase + L_WRING + w * 1024 + 4096);
        if (w < 2) glds16(wv[2], wsrc, lbase + L_WRING + w * 1024 + 8192);
    }
    const rsrc_t rs = P.rs;
    STAMP(41);
    RAW_BARRIER();
    STAMP(42);
    if (EXP & 3) return;
    __builtin_amdgcn_s_setprio(2);
    *(LAS u32x4*)(L + L_AIMG + w * 1024 + lane * 16) = u32x4{0, 0, 0, 0};
    if (lane < 48) *(LAS u32x2*)(L + L_AIMG + 4096 + w * 384 + lane * 8) = u32x2{0, 0};
    f32x4 acc[2][5];
    const bool kl = !(m & 4);
#pragma unroll
    for (int ct = 0; ct < 5; ++ct) {
        float iv = ct < 2 ? ONE_EPS : (ct < 4 ? 0.f : (kl ? ONE_EPS : 0.f));
        acc[0][ct] = f32x4{iv, iv, iv, iv};
        acc[1][ct] = acc[0][ct];
    }
    lptr XA0 = L + L_XRING + w * 4096 + m * 128 + ((kg ^ (m & 7)) << 4);
    lptr XA1 = L + L_XRING + w * 4096 + m * 128 + (((4 + kg) ^ (m & 7)) << 4);
    const int ra = (8 * kg + 2 * ((lane >> 2) & 3)) * 80;
    lptr WA = L + L_WRING + ra + (lane & 3) * 8 + 8;
    lptr WL = L + L_WRING + ra + (lane & 1) * 5120 + 40;
    volatile LAS u32* FULL = (volatile LAS u32*)(L + L_FLAG + 40);
    volatile LAS u32* FREE = (volatile LAS u32*)(L + L_FLAG + w * 4);
    u32x4 X[18][2];
    u32x2 B[2][5][2];
    auto rdx = [&](int i, int e) {
        X[2 * i + (e >> 1)][e & 1] = *(LAS u32x4*)(((e >> 1) ? XA1 : XA0) + (i % 3) * 16384 + (e & 1) * 2048);
    };
    auto rdb = [&](int i, int ks, int q) {
        const int o = (i % 3) * 10240 + ks * 2560;
        lptr p = q == 2 ? WL + o : WA + o + (q >= 3 ? 5120 : 0) + ((q == 1 || q == 4) ? 40 : 0);
        B[ks][q][0] = trread(p);
        B[ks][q][1] = trread(p + 80);
    };
    auto mm = [&](int k, int s) {
        constexpr int ord[5] = {0, 1, 4, 2, 3};
        const int q = s >> 1, r = s & 1;
        acc[r][ord[q]] = mfma32(X[k][r], cat(B[k & 1][q][0], B[k & 1][q][1]), acc[r][ord[q]]);
    };
    lptr XTA[3];
    u32 xts = WS_XT + (hp * 12 + min(kg, 2) * 4) * 16384 + (1008 - 64 * rt - 16 * w + m) * 16;
#pragma unroll
    for (int e = 0; e < 3; ++e) {
        const int B = ((72 * hp) & 127) + 24 * min(kg, 2) + 8 * e, r = 30 - 2 * m;
        XTA[e] = L + L_XRING + (B >> 7) * 16384 + w * 4096 + r * 128 + ((((B >> 4) ^ r) & 7) << 4) + (B & 8);
        PIN(XTA[e]);
    }
    PIN(xts);
    u32x2 ev[3], od[3];
    u32 tag = 0;
    WAIT_V(0);
    flag_set(L, w, 1);
    quad_wait(L + L_FLAG, 1);
    STAMP(1);
#pragma unroll
    for (int e = 0; e < 4; ++e) rdx(0, e);
#pragma unroll
    for (int q = 0; q < 5; ++q) rdb(0, 0, q);
    SCHED();
#pragma unroll
    for (int i = 0; i < 9; ++i) {
        u32 f = 0;
#pragma unroll
        for (int s = 0; s < 10; ++s) {
            mm(2 * i, s);
            if (s == 1 && i) { WAIT_L(12); *FREE = 2 * i + 2; }
            if (i == 1 && s >= 5 && s < 9) {
                u32 d[3];
#pragma unroll
                for (int c = 0; c < 3; ++c) {
                    const int q = 3 * (s - 5) + c;
                    d[c] = __builtin_amdgcn_perm(ev[q >> 2][(q >> 1) & 1], od[q >> 2][(q >> 1) & 1], (q & 1) ? 0x07060302u : 0x05040100u);
                }
                st16(rs, seal(d[0], d[1], d[2], tag), xts, (s - 5) * 16384);
            }
            if (s < 5) rdb(i, 1, s);
            if (s == 5 && i < 8) f = *FULL;
            SCHED();
        }
#pragma unroll
        for (int s = 0; s < 10; ++s) {
            mm(2 * i + 1, s);
            if (s == 0 && i < 8) {
                if (i) { WAIT_L0(); *FREE = 2 * i + 3; }
                if (__builtin_amdgcn_readfirstlane(f) < (u32)(i + 2)) flag_wait1(L, 10, i + 2);
                CFENCE();
                if (i == 0) {
#pragma unroll
                    for (int e = 0; e < 3; ++e) { ev[e] = *(LAS u32x2*)(XTA[e]); od[e] = *(LAS u32x2*)((lptr)((u32)(uintptr_t)XTA[e] ^ 16) + 128); }
                    tag = mk_tag(__builtin_amdgcn_readfirstlane(*(volatile LAS u32*)(L + L_FLAG + 32)));
                    WAIT_L0();
                    *FREE = 3;
                }
            }
            if (i < 8) {
                if (s == 1 || s == 2) { rdx(i + 1, 2 * s - 2); rdx(i + 1, 2 * s - 1); }
                if (s >= 3 && s < 8) rdb(i + 1, 0, s - 3);
            }
            SCHED();
        }
        STAMP(16 + i);
    }
    STAMP(2);
    {
        u32x4 op[5];
        const float ca = kl ? EPS : __builtin_inff(), cb = kl ? ONE_EPS : -__builtin_inff();
#pragma unroll
        for (int ct = 0; ct < 5; ++ct) {
            f32x4 v[2];
#pragma unroll
            for (int r = 0; r < 2; ++r)
#pragma unroll
                for (int i = 0; i < 4; ++i) {
                    float a = acc[r][ct][i];
                    if (ct < 2) a = phi(a);
                    if (ct == 4) a = __builtin_amdgcn_fmed3f(__builtin_amdgcn_exp2f(fmaf(a, LOG2E, -ONE_EPS * LOG2E)) + ca, a, cb);
                    v[r][i] = a;
                }
            op[ct] = u32x4{pk2(v[0][0], v[0][1]), pk2(v[0][2], v[0][3]), pk2(v[1][0], v[1][1]), pk2(v[1][2], v[1][3])};
        }
#pragma unroll
        for (int i = 0; i < 4; ++i) op[4][i] = m >= 12 ? 0x3f803f80u : op[4][i];
        const f32x4 Z = {0, 0, 0, 0};
        f32x4 j1[2], j3[2], j5[2], j7;
        j3[0] = mfma32(op[4], op[0], Z);
        j3[1] = mfma32(op[4], op[1], Z);
        j7 = mfma32(op[4], op[4], Z);
        j5[0] = mfma32(op[4], op[2], Z);
        j5[1] = mfma32(op[4], op[3], Z);
        j1[0] = mfma32(op[0], op[2], Z);
        j1[1] = mfma32(op[1], op[3], Z);
        f32x2 ex[2];
#pragma unroll
        for (int h = 0; h < 2; ++h)
#pragma unroll
            for (int i = 0; i < 2; ++i) ex[h][i] = (kg & 1) ? j3[h][2 * h + i] : (kg ? j7[2 * h + i] : j5[h][2 * h + i]);
        lptr K4 = L + L_KVX + lane * 16, K2 = L + L_KVX + 1024 + lane * 8;
        if (w >= 2) {
#pragma unroll
            for (int h = 0; h < 2; ++h) {
                *(LAS f32x4*)(K4 + (2 * w - 2 + h) * 1536) = j1[h];
                *(LAS f32x2*)(K2 + (2 * w - 2 + h) * 1536) = ex[h];
            }
            flag_set(L, w, 20);
        } else {
            *(LAS f32x4*)(K4 + w * 1536) = w ? j1[0] : j1[1];
            *(LAS f32x2*)(K2 + w * 1536) = w ? ex[0] : ex[1];
            flag_set(L, w, 20);
            f32x4 s4 = w ? j1[1] : j1[0];
            f32x2 s2 = w ? ex[1] : ex[0];
            quad_wait(L + L_FLAG, 20);
            s4 += *(LAS f32x4*)(K4 + (1 - w) * 1536);
            s2 += *(LAS f32x2*)(K2 + (1 - w) * 1536);
            f32x4 t4 = *(LAS f32x4*)(K4 + (2 + w) * 1536) + *(LAS f32x4*)(K4 + (4 + w) * 1536);
            f32x2 t2 = *(LAS f32x2*)(K2 + (2 + w) * 1536) + *(LAS f32x2*)(K2 + (4 + w) * 1536);
            s4 += t4;
            s2 += t2;
            st16(rs, seal(pk2(s4[0], s4[1]), pk2(s4[2], s4[3]), pk2(s2[0], s2[1]), tag), kg * 4096 + m * 16, WS_KVG + (hp * 8 + w * 4) * 4096 + rt * 256);
        }
    }
    STAMP(3);
    const u32 lo = lane * 16;
    u32x4 wff = ld16(rs, WS_WFF + min(lane, 40) * 16, 0);
    flag_wait1(L, 10, 10);
    STAMP(4);
    f32x4 q[2][3];
#pragma unroll
    for (int r = 0; r < 2; ++r)
#pragma unroll
        for (int t = 0; t < 3; ++t) q[r][t] = f32x4{ONE_EPS, ONE_EPS, ONE_EPS, ONE_EPS};
    u32x4 g[8], WF[45];
    const int grp = 9 * (cq >> 1) + 5 * (cq & 1) + w;
    const u32 wf_main = WS_WF + grp * 36864;
    const u32 wf_ext = WS_WF + (((9 * (cq >> 1) + 4) * 18 + 9 * (w >> 1)) * 2 + (cq & 1)) * 1024;
    auto ldwf = [&](int i) { WF[i] = i < 9 ? ld16(rs, lo, wf_ext + i * 2048) : ld16(rs, lo, wf_main + (i - 9) * 1024); };
    {
        lptr QA = L + L_XRING + ra + (lane & 3) * 8, QL = L + L_XRING + ra + 32;
        u32x2 a[2][3][2];
        auto rdq = [&](int k, int t) {
            const int i = k >> 1, o = (i / 3) * 16384 + (i % 3) * 5120 + (k & 1) * 2560;
            lptr p = t == 2 ? QL + o : QA + o + t * 40;
            a[k & 1][t][0] = trread(p);
            a[k & 1][t][1] = trread(p + 80);
        };
        rdq(0, 0); rdq(0, 1); rdq(0, 2);
        SCHED();
#pragma unroll
        for (int k = 0; k < 18; ++k) {
            if (k == 3) {
#pragma nounroll
                for (u32 it = 0; !wf_ok(wff, lane, tag) && it < SPIN_MAX; ++it) {
                    __builtin_amdgcn_s_sleep(8);
                    wff = ld16(rs, WS_WFF + min(lane, 40) * 16, 0);
                }
                CFENCE();
            }
#pragma unroll
            for (int s = 0; s < 6; ++s) {
                const int t = s >> 1, r = s & 1;
                q[r][t] = mfma32(cat(a[k & 1][t][0], a[k & 1][t][1]), X[k][r], q[r][t]);
                if (s < 3 && k < 17) rdq(k + 1, s);
                if ((s == 3 || s == 5) && k >= 4 && k < 17) ldwf(2 * (k - 4) + (s == 5));
                SCHED();
            }
        }
    }
#pragma unroll
    for (int i = 0; i < 8; ++i) g[i] = ld16(rs, lo, WS_KVG + (hp * 8 + 2 * w) * 4096 + i * 1024);
    SCHED();
    STAMP(5);
    u32x4 pq1[2], pq2[2];
#pragma unroll
    for (int r = 0; r < 2; ++r) {
        u32 p[6];
#pragma unroll
        for (int t = 0; t < 3; ++t) {
            p[2 * t] = pk2(phi(q[r][t][0]), phi(q[r][t][1]));
            p[2 * t + 1] = pk2(phi(q[r][t][2]), phi(q[r][t][3]));
        }
        pq1[r] = u32x4{p[0], p[1], p[2], p[3]};
        pq2[r] = u32x4{p[4], p[5], 0, 0};
    }
    u32x4 sel[2];
    {
        const u32 one = (m & 1) ? 0x3f800000u : 0x3f80u;
#pragma unroll
        for (int s = 0; s < 2; ++s)
#pragma unroll
            for (int i = 0; i < 4; ++i) sel[s][i] = (i < 3 && (m >> 1) == 4 * s + i) ? one : 0u;
    }
    const int hd = w >> 1, kgp = 2 * (w & 1) + (kg >> 1);
    int a0, a1;
    {
        const int DUMP = 38 * 144;
        const int rowE = (20 * hd + m) * 144, ex0 = (16 + 2 * hd) * 144, den = (36 + hd) * 144;
        const int cd = 16 * (m >> 2) + 8 * hd + 2 * (m & 3), cx = 64 + 4 * hd;
        if (!(kg & 1)) {
            a0 = rowE + 16 * kgp + 8 * hd; a1 = 0;
        } else if (kgp == 0) {
            a0 = rowE + cx; a1 = a0 + 2;
        } else if (kgp == 1) {
            a0 = ex0 + cd; a1 = a0 + 144;
        } else if (kgp == 2) {
            a0 = (m >> 1) == 2 + hd ? ex0 + (m & 1) * 144 + cx : (m == 12 ? den + cx : DUMP); a1 = a0 + 2;
        } else {
            a0 = den + cd; a1 = DUMP;
        }
    }
    lptr A0, A2;
    {
        const int kr = m >> 2, i = m & 3;
        A0 = L + L_AIMG + (2 * kr + (i & 1) + 8 * (i >> 1)) * 144 + kg * 16;
        A2 = L + L_AIMG + (i == 0 ? 32 + kr : 36 + ((i - 1) & 1)) * 144 + kg * 16;
    }
    a0 += L_AIMG; a1 += L_AIMG;
    u32 vog = lo + WS_OG + ((rt * 4 + w) * 16 + hp) * 3072;
    PIN(pq1[0]); PIN(pq1[1]); PIN(pq2[0]); PIN(pq2[1]); PIN(sel[0]); PIN(sel[1]); PIN(a0); PIN(a1); PIN(A0); PIN(A2); PIN(vog);
    SCHED();
    poll_rows<8>(g, rs, lo, WS_KVG + (hp * 8 + 2 * w) * 4096, 1024, tag);
    STAMP(6);
    {
        f32x4 d0 = {0, 0, 0, 0}, d1 = d0;
#pragma unroll
        for (int i = 0; i < 4; ++i) {
            g[i][3] = 0; g[4 + i][3] = 0;
            d0 = mfma32(sel[0], g[i], d0);
            d1 = mfma32(sel[1], g[4 + i], d1);
        }
        d0 += d1;
        const u32 p01 = pk2(d0[0], d0[1]), p23 = pk2(d0[2], d0[3]);
        lptr AI = L;
        if (!(kg & 1)) {
            *(LAS u32x2*)(AI + a0) = u32x2{p01, p23};
        } else {
            *(LAS u16*)(AI + a0) = (u16)p01;
            *(LAS u16*)(AI + a1) = (u16)(p01 >> 16);
        }
    }
    flag_set(L, w, 21);
    quad_wait(L + L_FLAG, 21);
    {
        u32x4 A[3][2];
        A[2][0] = *(LAS u32x4*)(A2); A[2][1] = *(LAS u32x4*)(A2 + 64);
        A[0][0] = *(LAS u32x4*)(A0); A[0][1] = *(LAS u32x4*)(A0 + 64);
        A[1][0] = *(LAS u32x4*)(A0 + 16 * 144); A[1][1] = *(LAS u32x4*)(A0 + 16 * 144 + 64);
        f32x4 o[2][3];
#pragma unroll
        for (int tt = 0; tt < 3; ++tt)
#pragma unroll
            for (int r = 0; r < 2; ++r) {
                const int t = (tt + 2) % 3;
                o[r][t] = mfma32(A[t][0], pq1[r], t == 2 ? f32x4{0, EPS, EPS, 0} : f32x4{0, 0, 0, 0});
                o[r][t] = mfma32(A[t][1], pq2[r], o[r][t]);
            }
        u32 d[2][4];
        float e[2];
#pragma unroll
        for (int r = 0; r < 2; ++r) {
            const float ra = __builtin_amdgcn_rcpf(o[r][2][1]), rb = __builtin_amdgcn_rcpf(o[r][2][2]), rc = kg ? rb : ra;
            d[r][0] = pk2s(o[r][0][0] * ra, o[r][0][1] * ra);
            d[r][1] = pk2s(o[r][0][2] * ra, o[r][0][3] * ra);
            d[r][2] = pk2s(o[r][1][0] * rc, o[r][1][1] * rc);
            d[r][3] = pk2s(o[r][1][2] * rb, o[r][1][3] * rb);
            e[r] = o[r][2][0] * rb;
        }
        st16(rs, seal(d[0][0], d[0][1], d[0][2], tag), vog, 0);
        st16(rs, seal(d[1][0], d[1][1], d[1][2], tag), vog, 1024);
        st16(rs, seal(d[0][3], pk2s(e[0], e[1]), d[1][3], tag), vog, 2048);
    }
    STAMP(7);
#pragma unroll
    for (int i = 26; i < 45; ++i) ldwf(i);
    const int c8 = 32 * (9 * (cq >> 1) + 4) + 16 * (cq & 1);
    lptr IM = L + L_XRING;
    {
        u32x4 og[12];
        const u32 so = WS_OG + (rb * 16 + 4 * w) * 3072;
#pragma unroll
        for (int i = 0; i < 12; ++i) og[i] = ld16(rs, lo, so + i * 1024);
        STAMP(8);
        poll_rows<12>(og, rs, lo, so, 1024, tag);
        STAMP(9);
        lptr B0 = IM + w * 4608 + m * 16;
#pragma unroll
        for (int h = 0; h < 4; ++h) {
            lptr E = B0 + (h >> 1) * 2304 + ((h & 1) ? 1024 + (kg >> 1) * 256 + ((8 + 4 * kg) & 15) : 4 * kg);
            lptr E2 = B0 + (h >> 1) * 2304 + ((h & 1) ? 2048 + 8 : 1024) + 2 * kg;
#pragma unroll
            for (int i = 0; i < 3; ++i) {
                *(LAS u32*)(E + i * 256) = og[3 * h][i];
                *(LAS u32*)(E + 18432 + i * 256) = og[3 * h + 1][i];
            }
            *(LAS u32*)(E + 768) = og[3 * h + 2][0];
            *(LAS u32*)(E + 18432 + 768) = og[3 * h + 2][2];
            *(LAS u16*)(E2) = (u16)og[3 * h + 2][1];
            *(LAS u16*)(E2 + 18432) = (u16)(og[3 * h + 2][1] >> 16);
        }
    }
    SCHED();
    u32x4 bt[4], b8[1];
    const u32 bs = WS_BT + (rb * 576 + grp * 32) * 128, bs8 = WS_BT + (rb * 576 + c8) * 128 + (w & 1) * 16;
    const u32 bv = (lane >> 2) * 128 + (lane & 3) * 32, gs = WS_G + rb * 256;
    u32x2 g0, g1;
    flag_set(L, w, 22);
    quad_wait(L + L_FLAG, 22);
    STAMP(10); STAMPC(60);
    const f32x4 Z = {0, 0, 0, 0};
    f32x4 o[2][2] = {{Z, Z}, {Z, Z}}, a8 = Z;
    lptr EX = L + L_WRING + (w & 1) * 1024 + lo;
    {
        lptr F = IM + lo, F8 = IM + (w & 1) * 18432 + (w >> 1) * 9216 + lo;
        u32x4 f8[9], f[3][2];
#pragma unroll
        for (int k = 0; k < 9; ++k) f8[k] = *(LAS u32x4*)(F8 + k * 1024);
        auto rdf = [&](int k) { f[k % 3][0] = *(LAS u32x4*)(F + k * 1024); f[k % 3][1] = *(LAS u32x4*)(F + 18432 + k * 1024); };
        SCHED();
#pragma unroll
        for (int k = 0; k < 9; ++k) {
            a8 = mfma32(WF[k], f8[k], a8);
            if (k == 2 || k == 4) rdf(k / 2 - 1);
            SCHED();
        }
#pragma unroll
        for (int k = 0; k < 18; ++k) {
            if (k + 2 < 18) rdf(k + 2);
            if (k >= 8 && k < 12) bt[k - 8] = ld16(rs, lo, bs + (k - 8) * 1024);
            if (k == 12) b8[0] = ld16(rs, bv, bs8);
            if (k == 6) g0 = __builtin_amdgcn_raw_buffer_load_b64(rs, m * 8, gs, AUX_SC1);
            if (k == 7) g1 = __builtin_amdgcn_raw_buffer_load_b64(rs, m * 8, gs + 128, AUX_SC1);
#pragma unroll
            for (int r = 0; r < 2; ++r)
#pragma unroll
                for (int t = 0; t < 2; ++t) o[r][t] = mfma32(WF[9 + 2 * k + t], f[k % 3][r], o[r][t]);
            if (k == 1 && w >= 2) { *(LAS f32x4*)(EX) = a8; }
            if (k == 3 && w >= 2) flag_set(L, w, 23);
            SCHED();
        }
    }
    STAMP(11); STAMPC(61);
    poll_rows<4>(bt, rs, lo, bs, 1024, tag);
    lptr BI = L + L_WRING + 2048 + w * 2560;
#pragma unroll
    for (int i = 0; i < 4; ++i) *(LAS u32x2*)(BI + (lane & 1) * 1024 + i * 256 + (lane >> 1) * 8) = u32x2{bt[i][0], bt[i][1]};
    if (w < 2) {
        poll_rows<1>(b8, rs, bv, bs8, 0, tag);
        *(LAS u32x2*)(BI + 2048 + lane * 8) = u32x2{b8[0][0], b8[0][1]};
    }
#pragma nounroll
    for (u32 it = 0; it < SPIN_MAX && !__all((g0[0] ^ g0[1]) == tag && (g1[0] ^ g1[1]) == tag); ++it) {
        __builtin_amdgcn_s_sleep(2);
        g0 = __builtin_amdgcn_raw_buffer_load_b64(rs, m * 8, gs, AUX_SC1);
        g1 = __builtin_amdgcn_raw_buffer_load_b64(rs, m * 8, gs + 128, AUX_SC1);
        CFENCE();
    }
    CFENCE();
    STAMP(12);
    auto mix = [&](float g, f32x4 a, u32x2 b) {
        const float b0 = lo2f(b[0]), b1 = hi2f(b[0]), b2 = lo2f(b[1]), b3 = hi2f(b[1]);
        return u32x2{pk2s(fmaf(g, a[0] - b0, b0), fmaf(g, a[1] - b1, b1)), pk2s(fmaf(g, a[2] - b2, b2), fmaf(g, a[3] - b3, b3))};
    };
    GAS char* orow = (GAS char*)P.out + (rb * 32 + m) * 1152;
#pragma unroll
    for (int r = 0; r < 2; ++r) {
        const float g = __uint_as_float(r ? g1[0] : g0[0]);
        const u32x2 t0 = trread(BI + r * 1024 + kg * 256 + m * 8), t1 = trread(BI + r * 1024 + kg * 256 + 128 + m * 8);
        { u32x4 vv = cat(mix(g, o[r][0], t0), mix(g, o[r][1], t1)); asm volatile("global_store_dwordx4 %0, %1, off sc1\n\ts_nop 1" :: "v"(orow + r * 18432 + grp * 64 + kg * 16), "v"(vv) : "memory"); }
    }
    if (w < 2) {
        flag_wait1(L, w + 2, 23);
        a8 += *(LAS f32x4*)(EX);
        { u32x2 vv = mix(__uint_as_float(w ? g1[0] : g0[0]), a8, trread(BI + 2048 + lane * 8)); asm volatile("global_store_dwordx2 %0, %1, off sc1" :: "v"(orow + w * 18432 + c8 * 2 + kg * 8), "v"(vv) : "memory"); }
    }
    STAMP(13);
}

struct cf { float r, i; };
DEV cf cmul(cf a, cf b) { return cf{fmaf(a.r, b.r, -(a.i * b.i)), fmaf(a.r, b.i, a.i * b.r)}; }
DEV cf csq(cf a) { return cf{fmaf(a.r, a.r, -(a.i * a.i)), (a.r + a.r) * a.i}; }
DEV cf cexp(float lr, float t) {
    const float e = __builtin_amdgcn_exp2f(lr), f = __builtin_amdgcn_fractf(t);
    return cf{e * __builtin_amdgcn_cosf(f), e * __builtin_amdgcn_sinf(f)};
}

DEV u32 kgen_role(Params& P, lptr L, int cl, int sg, int lane) {
    const int ch = 3 * sg + cl;
    const int m = lane & 15, kg = lane >> 4;
    lptr C = L + L_CH + cl * CH_BYTES;
    const int n = ch * 64 + lane;
    P.arrive<2>();
    u32 are = ((const GAS u32*)P.Are)[n], aim = ((const GAS u32*)P.Aim)[n], bre = ((const GAS u32*)P.Bre)[n];
    u32 bim = ((const GAS u32*)P.Bim)[n], cre = ((const GAS u32*)P.Cre)[n], cim = ((const GAS u32*)P.Cim)[n];
    const u16 ldt = ((const GAS u16*)P.logdt)[ch], dsk = ((const GAS u16*)P.Dskip)[ch];
    SCHED();
    RAW_BARRIER();
    u32 ga[2][20];
#pragma unroll
    for (int it = 0; it < 2; ++it) {
        const int l0 = (it * 64 + lane) * 16 - 4;
#pragma unroll
        for (int j = 0; j < 10; ++j) {
            const int la = l0 + 2 * j, lb = la + 1;
            const u32 bits = pk2((float)la, (float)lb);
            int ia = (int)(bits & 0xffff) - 0x4280, ib = (int)(bits >> 16) - 0x4280;
            if (it == 0) {
                ia = la < 256 ? (la < 0 ? 641 : la) : ia;
                ib = lb < 256 ? (lb < 0 ? 641 : lb) : ib;
            }
            ga[it][2 * j] = L_KT + cl * 1312 + 2 * ia;
            ga[it][2 * j + 1] = L_KT + cl * 1312 + 2 * ib;
        }
    }
#pragma unroll
    for (int it = 0; it < 2; ++it)
#pragma unroll
        for (int j = 0; j < 20; ++j) asm volatile("" : "+v"(ga[it][j]));
    SCHED();
    const float dt = lo2f(pk2(expf(bf2f(ldt)), 0.f));
    STAMP(1); STAMPC(60);
    f32x4 a0 = {lane == 0 ? bf2f(dsk) : 0.f, 0, 0, 0}, a1 = {0, 0, 0, 0}, a2 = a1;
    LAS u32* T = (LAS u32*)(C + CH_KC + (lane >> 2) * TQ + (lane & 3) * 4);
    lptr R = C + CH_KC + kg * TQ + m * 16;
#pragma nounroll
    for (int u = 0; u < 2; ++u) {
        auto hf = [&](u32& v) { const float r = hi2f(v); v <<= 16; return r; };
        const float lr = dt * hf(are) * LOG2E, t = dt * hf(aim) * 0.15915494309189533577f;
        const cf c = cmul(cf{hf(cre), hf(cim)}, cf{dt * hf(bre), dt * hf(bim)});
        cf p[16], A[9];
        p[1] = cexp(lr, t);
        const cf w16 = cexp(16.f * lr, 16.f * t);
        A[1] = cexp(256.f * lr, 256.f * t);
        auto put = [&](int row, cf v) { if (EXP & 4) { u32 z = pk2s(v.r, v.i); asm volatile("" :: "v"(z)); } else T[row * 4] = pk2s(v.r, v.i); };
        T[0] = 0x3f80u;
        put(1, p[1]);
#pragma unroll
        for (int k = 2; k < 16; ++k) {
            p[k] = (k & 1) ? cmul(p[k - 1], p[1]) : csq(p[k / 2]);
            put(k, p[k]);
        }
        {
            const cf w32 = csq(w16);
            cf h0 = cf{c.r, -c.i}, h1 = cmul(c, w16);
            h1.i = -h1.i;
            const cf wc = cf{w32.r, -w32.i};
#pragma unroll
            for (int k = 0; k < 8; ++k) {
                put(41 + 2 * k, h0);
                put(42 + 2 * k, h1);
                if (k < 7) { h0 = cmul(h0, wc); h1 = cmul(h1, wc); }
            }
        }
        A[2] = csq(A[1]); A[3] = cmul(A[2], A[1]); A[4] = csq(A[2]);
        A[5] = cmul(A[4], A[1]); A[6] = csq(A[3]); A[7] = cmul(A[4], A[3]); A[8] = csq(A[4]);
#pragma unroll
        for (int k = 0; k < 8; ++k) put(16 + k, k ? cmul(A[1], p[2 * k]) : A[1]);
#pragma unroll
        for (int k = 0; k < 8; ++k) put(24 + k, (k & 3) ? cmul(A[2 + (k >> 2)], p[4 * (k & 3)]) : A[2 + (k >> 2)]);
#pragma unroll
        for (int k = 0; k < 8; ++k) put(32 + k, (k & 1) ? cmul(A[4 + (k >> 1)], p[8]) : A[4 + (k >> 1)]);
        put(40, A[8]);
        CFENCE();
        STAMPD(8 + u);
#pragma unroll
        for (int s = 0; s < 4; ++s) {
            const u32x4 H = *(LAS u32x4*)(R + s * 4 * TQ + 41 * 16);
            a0 = mfma32(*(LAS u32x4*)(R + s * 4 * TQ), H, a0);
            a1 = mfma32(*(LAS u32x4*)(R + s * 4 * TQ + 256), H, a1);
            a2 = mfma32(*(LAS u32x4*)(R + s * 4 * TQ + 512), H, a2);
        }
        CFENCE();
        STAMPD(6 + u);
    }
    STAMP(2); STAMPC(61);
    const u32 tag = mk_tag(get_epoch(L));
    u32x4 g[6];
    const u32 xs = WS_XT + sg * 16384 + cl * 5120;
#pragma unroll
    for (int i = 0; i < 6; ++i) g[i] = ld16(P.rs, lane * 16, xs + i * 1024);
    SCHED();
    {
        lptr KT = L + L_KT + cl * 1312;
        *(LAS u32x2*)(KT + 2 * (16 * m + 4 * kg)) = u32x2{pk2(a0[0], a0[1]), pk2(a0[2], a0[3])};
        *(LAS u32x2*)(KT + 2 * (kg < 2 ? 256 + 8 * m + 4 * kg : 256 + 64 * kg + 4 * m)) = u32x2{pk2(a1[0], a1[1]), pk2(a1[2], a1[3])};
        if (kg < 2) {
            *(LAS u32*)(KT + 2 * (512 + 64 * kg + 2 * m)) = pk2(a2[0], a2[1]);
            *(LAS u32*)(KT + 2 * (544 + 64 * kg + 2 * m)) = pk2(a2[2], a2[3]);
        }
        if (lane == 32) *(LAS u32*)(KT + 2 * 640) = pk2(a2[0], 0.f);
        CFENCE();
    }
#pragma unroll
    for (int it = 0; it < 2; ++it) {
        u32 v[20], d[10], e[9];
#pragma unroll
        for (int j = 0; j < 20; ++j) v[j] = *(LAS u16*)(L + ga[it][j]);
#pragma unroll
        for (int j = 1; j < 10; ++j) d[j] = v[2 * j] | (v[2 * j + 1] << 16);
#pragma unroll
        for (int j = 0; j < 9; ++j) e[j] = v[2 * j + 1] | (v[2 * j + 2] << 16);
        lptr W = C + CH_KC + 32 + (it * 64 + lane) * 32;
        *(LAS u32x4*)(W) = u32x4{d[2], d[3], d[4], d[5]};
        *(LAS u32x4*)(W + 16) = u32x4{d[6], d[7], d[8], d[9]};
        *(LAS u32x4*)(W + CH_KSTRIDE) = u32x4{e[1], e[2], e[3], e[4]};
        *(LAS u32x4*)(W + CH_KSTRIDE + 16) = u32x4{e[5], e[6], e[7], e[8]};
        *(LAS u32x4*)(W + 2 * CH_KSTRIDE) = u32x4{d[1], d[2], d[3], d[4]};
        *(LAS u32x4*)(W + 2 * CH_KSTRIDE + 16) = u32x4{d[5], d[6], d[7], d[8]};
        *(LAS u32x4*)(W + 3 * CH_KSTRIDE) = u32x4{e[0], e[1], e[2], e[3]};
        *(LAS u32x4*)(W + 3 * CH_KSTRIDE + 16) = u32x4{e[4], e[5], e[6], e[7]};
    }
    if (lane < 8) *(LAS u32x4*)(C + CH_KC + (lane >> 1) * CH_KSTRIDE + (lane & 1) * 16) = u32x4{0, 0, 0, 0};
    *(LAS u32x2*)(C + CH_XR + 4096 + lane * 8) = u32x2{0, 0};
    SCHED();
    STAMP(10);
    poll_rows<6>(g, P.rs, lane * 16, xs, 1024, tag);
    lptr XR = L + L_CH + CH_XR + cl * 1280 + lane * 4;
#pragma unroll
    for (int i = 0; i < 6; ++i)
#pragma unroll
        for (int c = 0; c < 3; ++c) *(LAS u32*)(XR + c * CH_BYTES + i * 256) = g[i][c];
    return tag;
}

template <int MASK, int NA>
DEV void conv_blocks(rsrc_t rs, lptr C, int ch, int lane, u32 tag) {
    const int m = lane & 15, kg = lane >> 4;
    f32x4 acc[8];
#pragma unroll
    for (int i = 0; i < 8; ++i) acc[i] = f32x4{0, 0, 0, 0};
    lptr KA = C + CH_KC + ((~m) & 3) * CH_KSTRIDE + 2 * (8 * kg + ((m + 4) & ~3));
    lptr XB = C + CH_XR + 2 * (240 - 16 * m + 8 * kg);
    u32x4 A[2][NA], B[2][NA];
    auto rd = [&](int j, int a) {
        A[j & 1][a] = cat(*(volatile LAS u32x2*)(KA + 64 * j + 512 * a), *(volatile LAS u32x2*)(KA + 64 * j + 512 * a + 8));
        B[j & 1][a] = *(LAS u32x4*)(XB + 64 * j + 512 * (7 - a));
    };
#pragma unroll
    for (int a = 0; a < NA; ++a) rd(0, a);
    SCHED();
#pragma unroll
    for (int j = 0; j < 8; ++j) {
        int n = 0;
#pragma unroll
        for (int I = 0; I < 8; ++I)
            if (MASK >> I & 1)
#pragma unroll
                for (int a = 0; a <= I; ++a) {
                    acc[I] = mfma32(A[j & 1][a], B[j & 1][I - a], acc[I]);
                    if (j < 7 && !(n & 1) && (n >> 1) < NA) rd(j + 1, n >> 1);
                    ++n;
                    SCHED();
                }
    }
    const u32 vo = (m >> 1) * 73728 + kg * 32 + (m & 1) * 16;
#pragma unroll
    for (int I = 0; I < 8; ++I)
        if (MASK >> I & 1) st16(rs, seal(pk2(acc[I][0], acc[I][1]), pk2(acc[I][2], acc[I][3]), 0u, tag), vo, WS_BT + I * 8 * 73728 + ch * 128);
}

DEV void gate_role(const Params& P, rsrc_t rs, int lane, int rb, int ps, u32 tag) {
    const GAS char* gw = (const GAS char*)P.gw + (lane & 3) * 16;
    {
        const int row = rb * 32 + ps * 16 + (lane >> 2);
        const GAS char* xr = (const GAS char*)P.x + row * 1152 + (lane & 3) * 16;
        u32x4 xv[18], gv[18];
#pragma unroll
        for (int i = 0; i < 18; ++i) { xv[i] = *(const GAS u32x4*)(xr + i * 64); gv[i] = *(const GAS u32x4*)(gw + i * 64); }
        float s0 = 0.f, s1 = 0.f;
#pragma unroll
        for (int i = 0; i < 18; ++i)
#pragma unroll
            for (int j = 0; j < 4; ++j) {
                s0 = fmaf(lo2f(xv[i][j]), lo2f(gv[i][j]), s0);
                s1 = fmaf(hi2f(xv[i][j]), hi2f(gv[i][j]), s1);
            }
        float s = s0 + s1;
        s += __shfl_xor(s, 1);
        s += __shfl_xor(s, 2);
        const u32 g = __float_as_uint(1.0f / (1.0f + __expf(-s)));
        if ((lane & 3) == 0) __builtin_amdgcn_raw_buffer_store_b64(u32x2{g, g ^ tag}, rs, WS_G + row * 8, 0, AUX_SC1);
    }
}

DEV u32 wf_role(Params& P, lptr L, u32 lbase, int cl, int lane, int unit) {
    const int ks = unit / 9, gp = unit - ks * 9;
    P.arrive<3>();
    const rsrc_t rs = P.rs;
    if (unit < 162) {
        const char* src = (const char*)P.wo + ks * 32 * 1152 + gp * 128;
#pragma unroll
        for (int i = 0; i < 4; ++i) glds16((u32)((lane >> 3) * 1152 + (lane & 7) * 16), src + i * 8 * 1152, lbase + L_CH + cl * CH_BYTES + i * 1040);
    }
    RAW_BARRIER();
    if (unit >= 162) return mk_tag(get_epoch(L));
    WAIT_V(0);
    lptr S = L + L_CH + cl * CH_BYTES + (lane >> 4) * 1040 + ((lane >> 2) & 3) * 128;
#pragma unroll
    for (int h = 0; h < 2; ++h) {
        const int grp = 2 * gp + h;
        const bool sp = grp == 4 || grp == 13;
        lptr p = S + h * 64 + (lane & 3) * (sp ? 8 : 16);
#pragma unroll
        for (int t = 0; t < 2; ++t) {
            lptr q = p + t * (sp ? 32 : 8);
            st16(rs, cat(trread(q), trread(q + 512)), lane * 16, WS_WF + ((grp * 18 + ks) * 2 + t) * 1024);
        }
    }
    SCHED();
    const u32 tag = mk_tag(get_epoch(L));
    WAIT_V(0);
    if (lane == 0) __builtin_amdgcn_raw_buffer_store_b32(tag, rs, WS_WFF + unit * 4, 0, AUX_SC1);
    return tag;
}

__global__ __launch_bounds__(512, 2) void fused(Args A) {
    Params P;
    P.fetch();
    extern __shared__ __attribute__((aligned(64))) char smem[];
    lptr L = (lptr)smem;
    const u32 lbase = (u32)(uintptr_t)L;
    const int tid = threadIdx.x, lane = tid & 63, w = __builtin_amdgcn_readfirstlane(tid >> 6);
    const int bid = blockIdx.x, xcd = bid & 7, slot = bid >> 3;
    if (tid < 16) *(LAS u32*)(L + L_FLAG + tid * 4) = tid == 7;
    const int rt = (xcd >> 1) * 4 + (slot & 3), hp = (xcd & 1) * 8 + (slot >> 2);
    const int rb = 8 * xcd + (slot & 7), cq = slot >> 3, cg = w & 3;
    if (w < 4) {
        gemm_role(P, L, lbase, w, lane, rt, hp, rb, cq);
        if (bid == 0 && tid == 0) {
            const u32 ep = *(volatile LAS u32*)(L + L_FLAG + 32);
            __builtin_amdgcn_raw_buffer_store_b32(ep + 1, P.rs, WS_EPOCH, 0, AUX_SC1);
        }
    } else if (w == 7) {
        const u32 tag = loader_role(P, L, lbase, lane, rt, hp);
        if (slot < 24) {
            const int sg = xcd * 24 + slot;
            quad_wait(L + L_FLAG + 16, 1);
            STAMP(3);
#pragma nounroll
            for (int c = 0; c < 3; ++c) {
                STAMP(4);
                conv_blocks<0x0E, 4>(P.rs, L + L_CH + c * CH_BYTES, 3 * sg + c, lane, tag);
            }
            STAMP(5);
        }
    } else if (slot < 24) {
        const int sg = xcd * 24 + slot;
        const u32 tag = kgen_role(P, L, cg, sg, lane);
        flag_set(L, w, 1);
        STAMP(3);
        quad_wait(L + L_FLAG + 16, 1);
        STAMP(4);
        conv_blocks<0xF1, 8>(P.rs, L + L_CH + cg * CH_BYTES, 3 * sg + cg, lane, tag);
        STAMP(5);
    } else {
        const u32 tag = wf_role(P, L, lbase, cg, lane, ((slot - 24) * 8 + xcd) * 3 + cg);
        STAMP(3);
        if (cg < 2) {
            flag_wait1(L, 10, 10);
            gate_role(P, P.rs, lane, rb, cg, tag);
        }
        STAMP(4);
    }
}

extern "C" void kernel_launch(void* const* d_in, const int* in_sizes, int n_in, void* d_out, int out_size, void* d_ws,
                              size_t ws_size, hipStream_t stream) {
    Args P;
    P.x = (const u16*)d_in[0];
    P.wqkv = (const u16*)d_in[1];
    P.wo = (const u16*)d_in[2];
    P.Are = (const u16*)d_in[4];
    P.Aim = (const u16*)d_in[5];
    P.Bre = (const u16*)d_in[6];
    P.Bim = (const u16*)d_in[7];
    P.Cre = (const u16*)d_in[8];
    P.Cim = (const u16*)d_in[9];
    P.logdt = (const u16*)d_in[10];
    P.Dskip = (const u16*)d_in[11];
    P.gw = (const u16*)d_in[12];
    P.ws = (char*)d_ws;
    P.out = (u16*)d_out;
    (void)hipFuncSetAttribute((const void*)fused, hipFuncAttributeMaxDynamicSharedMemorySize, LDS_BYTES);
    fused<<<256, 512, LDS_BYTES, stream>>>(P);
}
```

```cpp
#include <hip/hip_runtime.h>
#include <hip/hip_bf16.h>
#include <stdint.h>

typedef unsigned short u16;
typedef unsigned int u32;
typedef unsigned long long u64;
typedef u32 u32x4 __attribute__((ext_vector_type(4)));
typedef u32 u32x2 __attribute__((ext_vector_type(2)));
typedef float f32x4 __attribute__((ext_vector_type(4)));
typedef float f32x2 __attribute__((ext_vector_type(2)));
typedef __bf16 bf16x8 __attribute__((ext_vector_type(8)));
typedef __bf16 bf16x2 __attribute__((ext_vector_type(2)));
typedef short s16x4 __attribute__((ext_vector_type(4)));
#define LAS __attribute__((address_space(3)))
#define GAS __attribute__((address_space(1)))
#define DEV __device__ __forceinline__
typedef LAS char* lptr;
typedef __amdgpu_buffer_rsrc_t rsrc_t;

constexpr u32 WS_EPOCH = 0;
constexpr u32 WS_WFF = 256;
constexpr u32 WS_G = 4096;
constexpr u32 WS_KVG = 65536;
constexpr u32 WS_WF = WS_KVG + 524288;
constexpr u32 WS_XT = WS_WF + 663552;
constexpr u32 WS_OG = WS_XT + 3145728;
constexpr u32 WS_BT = WS_OG + 3145728;
constexpr u32 WS_DBG = WS_BT + 4718592;
constexpr u32 WS_END = WS_DBG + 1048576;
constexpr int AUX_SC1 = 16;
#define EXP 0
#define SPIN_LOG 17
constexpr u32 SPIN_MAX = 1u << SPIN_LOG;

constexpr u32 L_FLAG = 0;
constexpr u32 CH_KC = 0, CH_KSTRIDE = 4160, CH_XR = 16640, CH_BYTES = 21248;
constexpr u32 L_CH = 64;
constexpr u32 L_KT = L_CH + 3 * CH_BYTES;
constexpr u32 L_XRING = L_KT + 3 * 1312;
constexpr u32 L_WRING = L_XRING + 49152;
constexpr u32 L_AIMG = L_WRING + 40960;
constexpr u32 LDS_BYTES = L_AIMG + 5632;
constexpr u32 L_KVX = L_WRING + 10240;
constexpr u32 TQ = 944;
static_assert(LDS_BYTES <= 163840, "lds");
static_assert(L_XRING % 32 == 0, "align");

DEV float bf2f(u16 v) { return __uint_as_float(((u32)v) << 16); }
DEV u32 pk2(float a, float b) {
    f32x2 v = {a, b};
    return __builtin_bit_cast(u32, __builtin_convertvector(v, bf16x2));
}
DEV u32 pk2s(float a, float b) {
    u32 r;
    asm("v_cvt_pk_bf16_f32 %0, %1, %2" : "=v"(r) : "v"(a), "v"(b));
    return r;
}
DEV float lo2f(u32 v) { return __uint_as_float(v << 16); }
DEV float hi2f(u32 v) { return __uint_as_float(v & 0xffff0000u); }
DEV f32x4 mfma32(u32x4 a, u32x4 b, f32x4 c) {
    return __builtin_amdgcn_mfma_f32_16x16x32_bf16(__builtin_bit_cast(bf16x8, a), __builtin_bit_cast(bf16x8, b), c, 0, 0, 0);
}
DEV void glds16(u32 voff, const void* sbase, u32 ldsaddr) {
    asm volatile("s_mov_b32 m0, %2\n\ts_nop 0\n\tglobal_load_lds_dwordx4 %0, %1" ::"v"(voff), "s"(sbase), "s"(ldsaddr) : "memory");
}
DEV void glds16x4(u32 v0, u32 v1, u32 v2, u32 v3, const void* sbase, u32 ldsaddr) {
    asm volatile("s_mov_b32 m0, %5\n\ts_nop 0\n\tglobal_load_lds_dwordx4 %0, %4\n\tglobal_load_lds_dwordx4 %1, %4 offset:1024\n\t"
                 "global_load_lds_dwordx4 %2, %4 offset:2048\n\tglobal_load_lds_dwordx4 %3, %4 offset:3072" ::"v"(v0), "v"(v1), "v"(v2), "v"(v3),
                 "s"(sbase), "s"(ldsaddr) : "memory");
}
DEV void glds_word(u32 voff, const void* sbase, u32 ldsaddr) {
    asm volatile("s_mov_b32 m0, %2\n\ts_mov_b64 exec, 1\n\tglobal_load_lds_dword %0, %1\n\ts_mov_b64 exec, -1" ::"v"(voff), "s"(sbase), "s"(ldsaddr) : "memory");
}
DEV u32x2 trread(lptr p) { return __builtin_bit_cast(u32x2, __builtin_amdgcn_ds_read_tr16_b64_v4i16((LAS s16x4*)p)); }
DEV u32x4 cat(u32x2 a, u32x2 b) { return u32x4{a[0], a[1], b[0], b[1]}; }
DEV u32x4 ld16(rsrc_t rs, u32 voff, u32 soff) { return __builtin_amdgcn_raw_buffer_load_b128(rs, voff, soff, AUX_SC1); }
DEV void st16(rsrc_t rs, u32x4 v, u32 voff, u32 soff) { __builtin_amdgcn_raw_buffer_store_b128(v, rs, voff + soff, 0, AUX_SC1); }
DEV u32 xor3(u32 a, u32 b, u32 c) { return __builtin_amdgcn_bitop3_b32(a, b, c, 0x96); }
DEV u32x4 seal(u32 a, u32 b, u32 c, u32 tag) { return u32x4{a, b, c, xor3(a, b, tag) ^ c}; }
DEV u32 unseal(u32x4 g, u32 tag) { return xor3(xor3(g[0], g[1], tag), g[2], g[3]); }
#define WAIT_V(n) asm volatile("s_waitcnt vmcnt(%0)" ::"n"(n) : "memory")
#define WAIT_L(n) asm volatile("s_waitcnt lgkmcnt(%0)" ::"n"(n) : "memory")
#define WAIT_L0() asm volatile("s_waitcnt lgkmcnt(0)" ::: "memory")
#define RAW_BARRIER() asm volatile("s_waitcnt lgkmcnt(0)\n\ts_barrier" ::: "memory")
#define CFENCE() asm volatile("" ::: "memory")
#define SCHED() __builtin_amdgcn_sched_barrier(0)

#define STAMP(i) do {} while (0)
#define STAMPD(i) do {} while (0)
#define STAMPC(i) do {} while (0)

#define STAMPXD(i) do {} while (0)
#define STAMPX(i) do {} while (0)
DEV void flag_set(lptr L, int wave, u32 v) {
    WAIT_L0();
    *(volatile LAS u32*)(L + L_FLAG + wave * 4) = v;
}
DEV void flag_wait1(lptr L, int wave, u32 v) {
#pragma nounroll
    for (u32 it = 0; it < SPIN_MAX * 8; ++it) {
        u32 f = *(volatile LAS u32*)(L + L_FLAG + wave * 4);
        if (__builtin_amdgcn_readfirstlane(f) >= v) break;
        __builtin_amdgcn_s_sleep(1);
    }
    CFENCE();
}
DEV bool quad_ok(u32x4 f, u32 v) {
    u32 m = min(min(f[0], f[1]), min(f[2], f[3]));
    return __builtin_amdgcn_readfirstlane(m) >= v;
}
DEV void quad_wait(lptr Q, u32 v) {
#pragma nounroll
    for (u32 it = 0; it < SPIN_MAX * 8; ++it) {
        u32x4 f = *(volatile LAS u32x4*)(Q);
        if (quad_ok(f, v)) break;
        __builtin_amdgcn_s_sleep(1);
    }
    CFENCE();
}
DEV void quad_spin(lptr Q, u32 v) {
#pragma nounroll
    for (u32 it = 0; it < SPIN_MAX * 16; ++it) {
        u32x4 f = *(volatile LAS u32x4*)(Q);
        if (quad_ok(f, v)) break;
        if (EXP & 8) __builtin_amdgcn_s_sleep(1);
    }
    CFENCE();
}
DEV u32 mk_tag(u32 epoch) { return epoch * 0x9E3779B1u + 0x7F4A7C15u; }
DEV u32 get_epoch(lptr L) {
    u32x2 v;
#pragma nounroll
    for (u32 it = 0; it < SPIN_MAX * 8; ++it) {
        v = *(volatile LAS u32x2*)(L + L_FLAG + 32);
        if (__builtin_amdgcn_readfirstlane(v[1])) break;
        __builtin_amdgcn_s_sleep(1);
    }
    CFENCE();
    return __builtin_amdgcn_readfirstlane(v[0]);
}
template <int N>
DEV bool rows_bad(u32x4 (&g)[N], u32 tag) {
    u32 bad = 0;
#pragma unroll
    for (int i = 0; i < N; ++i) bad |= unseal(g[i], tag);
    return __any(bad != 0);
}
template <int N>
DEV void poll_rows(u32x4 (&g)[N], rsrc_t rs, u32 voff, u32 soff, u32 stride, u32 tag) {
    bool bad = rows_bad(g, tag);
#pragma nounroll
    for (u32 it = 0; __builtin_expect(bad, 0) && it < SPIN_MAX; ++it) {
#pragma unroll
        for (int i = 0; i < N; ++i)
            if (__any(unseal(g[i], tag) != 0))
                asm volatile("buffer_load_dwordx4 %0, %1, %2, %3 offen sc1" : "+v"(g[i]) : "v"(voff), "s"(rs), "s"(soff + i * stride) : "memory");
        WAIT_V(0);
#pragma unroll
        for (int i = 0; i < N; ++i) asm volatile("" : "+v"(g[i]));
        bad = rows_bad(g, tag);
    }
    CFENCE();
}
#define PIN(x) asm volatile("" : "+v"(x))
#define PINS(x) asm volatile("" : "+s"(x))

struct Args {
    const u16 *x, *wqkv, *wo, *Are, *Aim, *Bre, *Bim, *Cre, *Cim, *logdt, *Dskip, *gw;
    char* ws;
    u16* out;
};
typedef u64 u64x8 __attribute__((ext_vector_type(8)));
typedef u64 u64x4 __attribute__((ext_vector_type(4)));
typedef u64 u64x2 __attribute__((ext_vector_type(2)));
struct Params : Args {
    u64x8 k0; u64x4 k1; u64x2 k2;
    rsrc_t rs;
    DEV void fetch() {
        asm volatile("s_load_dwordx16 %0, %3, 0x0\n\ts_load_dwordx8 %1, %3, 0x40\n\ts_load_dwordx4 %2, %3, 0x60"
                     : "=&s"(k0), "=&s"(k1), "=&s"(k2) : "s"(__builtin_amdgcn_kernarg_segment_ptr()));
    }
    DEV static u64 take(u64 v) {
        u64 r;
        asm volatile("s_mov_b64 %0, %1" : "=s"(r) : "s"(v));
        return r;
    }
    template <int ROLE>
    DEV void arrive() {
        asm volatile("s_waitcnt lgkmcnt(0)" ::"s"(k0), "s"(k1), "s"(k2));
        if (ROLE != 2) x = (const u16*)take(k0[0]);
        if (ROLE < 2) wqkv = (const u16*)take(k0[1]);
        if (ROLE == 3) { wo = (const u16*)take(k0[2]); gw = (const u16*)take(k1[3]); }
        if (ROLE == 2) {
            Are = (const u16*)take(k0[3]); Aim = (const u16*)take(k0[4]); Bre = (const u16*)take(k0[5]); Bim = (const u16*)take(k0[6]);
            Cre = (const u16*)take(k0[7]); Cim = (const u16*)take(k1[0]); logdt = (const u16*)take(k1[1]); Dskip = (const u16*)take(k1[2]);
        }
        ws = (char*)take(k2[0]);
        if (ROLE == 0) out = (u16*)take(k2[1]);
        rs = __builtin_amdgcn_make_buffer_rsrc((void*)ws, 0, (int)WS_END, 0x00020000);
    }
};
DEV void pos5(u32 idx, u32& pos, u32& piece) {
    pos = (idx * 52429u) >> 18;
    piece = idx - 5 * pos;
}
DEV u32 pos2row(u32 pos) { return (pos & 56) | ((pos >> 1) & 3) | ((pos & 1) << 2); }

DEV int first_chunk(int hp) { return (9 * hp) >> 4; }
DEV int kv_lead(int hp) { return hp ? 8 : 0; }
DEV int q_lead(int hp) { return hp == 15 ? 8 : 0; }
DEV float add1(float a, float b) { asm("" : "+v"(a)); float r = a + b; asm("" : "+v"(r)); return r; }
DEV f32x4 add4(f32x4 a, f32x4 b) { return f32x4{add1(a[0], b[0]), add1(a[1], b[1]), add1(a[2], b[2]), add1(a[3], b[3])}; }
DEV bool wf_ok(u32x4 f, int lane, u32 tag) { return __all(f[0] == tag && f[1] == tag && (lane >= 40 || (f[2] == tag && f[3] == tag))); }
DEV u32 loader_role(Params& P, lptr L, u32 lbase, int lane, int rt, int hp) {
    u32 wv[5], xv[4];
#pragma unroll
    for (int p = 0; p < 5; ++p) {
        u32 pos, piece;
        pos5(p * 64 + lane, pos, piece);
        wv[p] = pos2row(pos) * 3456 + piece * 16 - (p & 3) * 1024;
    }
#pragma unroll
    for (int j = 0; j < 4; ++j) xv[j] = (u32)((lane >> 3) * 1152 + (((lane & 7) ^ (lane >> 3)) << 4)) + j * 8192;
    const char* seq;
    asm volatile("s_getpc_b64 %0\n\ts_branch 0f\n\t.long 1, 2, 3, 4, 5, 6, 7, 8, 9, 10, 11, 12\n0:" : "=s"(seq));
    u32 nv = 4;
    u64 ord = 0;
    {
        const u64 ks = (((0x87654328765432ull >> (8 * (rt & 3))) & 0xfffffffull) << 8) | 0x10;
        const u32 c0 = first_chunk(hp);
#pragma unroll
        for (int i = 0; i < 9; ++i) {
            const u32 c = c0 + ((u32)(ks >> (4 * i)) & 15);
            ord |= (u64)min(c, c - 9) << (4 * i);
        }
    }
    PINS(ord);
    u32 fo = WS_WFF + min(lane, 40) * 16, vk = (lane >> 4) * 4096 + (lane & 15) * 16 + WS_KVG + hp * 32768 + rt * 256;
    PIN(fo); PIN(vk);
    P.arrive<1>();
    glds_word(WS_EPOCH, P.ws, lbase + L_FLAG + 32);
    glds_word(nv, seq, lbase + L_FLAG + 36);
    const char* xg = (const char*)P.x + rt * 128 * 1152;
    const char* wg = (const char*)P.wqkv + 72 * hp + 1152 - kv_lead(hp);
    const char* qg = (const char*)P.wqkv + 72 * (hp ^ 1) - q_lead(hp ^ 1);
    auto five = [&](const char* src, u32 dst) {
        glds16x4(wv[0], wv[1], wv[2], wv[3], src, dst);
        glds16(wv[4], src, dst + 4096);
    };
    auto full = [&]() { nv += 4; glds_word(nv, seq, lbase + L_FLAG + 40); };
    u32x4 fl = {0, 0, 0, 0};
    auto room = [&](int v) {
        if (v > 0 && !quad_ok(fl, v)) quad_spin(L + L_FLAG, v);
        fl = *(volatile LAS u32x4*)(L + L_FLAG);
    };
    u32 xs = lbase + L_XRING + 16384;
    u64 o = ord;
#pragma nounroll
    for (int j = 1; j < 9; ++j) {
        o >>= 4;
        const u32 c = (u32)o & 15, ws = lbase + L_WRING + (j & 3) * 10240;
        room(j - 3);
        STAMPXD(16 + 2 * j);
        five(wg + c * 221184, ws);
        five(wg + c * 221184 + 1152, ws + 5120);
        if (j == 3) { STAMP(1); room(1); }
#pragma unroll
        for (int w = 0; w < 4; ++w) glds16x4(xv[0], xv[1], xv[2], xv[3], xg + c * 128 + w * 36864, xs + w * 4096);
        full();
        STAMPXD(17 + 2 * j);
        __builtin_amdgcn_s_setprio(3);
        xs = xs == lbase + L_XRING + 32768 ? lbase + L_XRING : xs + 16384;
    }
    u32x4 wff;
    asm volatile("buffer_load_dwordx4 %0, %1, %2, 0 offen sc1" : "=v"(wff) : "v"(fo), "s"(P.rs) : "memory");
    const u32 ep = *(volatile LAS u32*)(L + L_FLAG + 32);
    o = ord;
#pragma nounroll
    for (int s = 0; s < 3; ++s) {
        room(6 + s);
        STAMPXD(34 + 2 * s);
        if (s) full();
#pragma unroll
        for (int i = 0; i < 3; ++i) {
            five(qg + ((u32)o & 15) * 221184, lbase + L_XRING + s * 16384 + i * 5120);
            o >>= 4;
        }
        STAMPXD(35 + 2 * s);
    }
    const u32 tag = mk_tag(__builtin_amdgcn_readfirstlane(ep));
    asm volatile("s_waitcnt vmcnt(47)" : "+v"(wff)::"memory");
#pragma nounroll
    for (u32 it = 0; __builtin_expect(!wf_ok(wff, lane, tag), 0) && it < SPIN_MAX; ++it) {
        __builtin_amdgcn_s_sleep(8);
        asm volatile("buffer_load_dwordx4 %0, %1, %2, 0 offen sc1\n\ts_waitcnt vmcnt(0)" : "=v"(wff) : "v"(fo), "s"(P.rs) : "memory");
    }
    full();
    STAMP(6);
    quad_spin(L + L_FLAG, 20);
    STAMPX(8);
    {
        lptr K4 = L + L_KVX + lane * 16, K2 = L + L_KVX + 1024 + lane * 8;
        f32x4 r4[2][4];
        f32x2 r2[2][4];
#pragma unroll
        for (int h = 0; h < 2; ++h)
#pragma unroll
            for (int w = 0; w < 4; ++w) { r4[h][w] = *(LAS f32x4*)(K4 + w * 3072 + h * 1536); r2[h][w] = *(LAS f32x2*)(K2 + w * 3072 + h * 1536); }
        SCHED();
#pragma unroll
        for (int h = 0; h < 2; ++h) {
            const f32x4 s4 = add4(add4(r4[h][0], r4[h][1]), add4(r4[h][2], r4[h][3]));
            float s2[2];
#pragma unroll
            for (int i = 0; i < 2; ++i) s2[i] = add1(add1(r2[h][0][i], r2[h][1][i]), add1(r2[h][2][i], r2[h][3][i]));
            st16(P.rs, seal(pk2(s4[0], s4[1]), pk2(s4[2], s4[3]), pk2(s2[0], s2[1]), tag), vk, h * 16384);
        }
    }
    __builtin_amdgcn_s_setprio(0);
    STAMP(7);
    WAIT_V(2);
    STAMP(2);
    return tag;
}
#define WA_ 16
#define WB_ 26
#define WC_ 45

constexpr float LOG2E = 1.44269504088896340736f;
constexpr float EPS = 1e-6f;
constexpr float ONE_EPS = 1.0f + 1e-6f;
DEV float phi(float a) {
    float e = __builtin_amdgcn_exp2f(fmaf(a, LOG2E, -ONE_EPS * LOG2E));
    return __builtin_amdgcn_fmed3f(e + EPS, a, ONE_EPS);
}

DEV void gemm_role(Params& P, lptr L, u32 lbase, int w, int lane, int rt, int hp, int rb, int cq) {
    const int m = lane & 15, kg = lane >> 4;
    {
        const u32 xo = (u32)((lane >> 3) * 1152 + (((lane & 7) ^ (lane >> 3)) << 4));
        u32 wv[3];
#pragma unroll
        for (int j = 0; j < 3; ++j) {
            u32 pos, piece;
            pos5((w + 4 * j) * 64 + lane, pos, piece);
            wv[j] = pos2row(pos) * 3456 + (pos >> 6) * 1152 + piece * 16;
        }
        const int c0 = first_chunk(hp);
        P.arrive<0>();
        glds16x4(xo, xo + 8192, xo + 16384, xo + 24576, (const char*)P.x + (rt * 128 + w * 32) * 1152 + c0 * 128, lbase + L_XRING + w * 4096);
        const char* wsrc = (const char*)P.wqkv + 72 * hp + c0 * 221184 + 1152 - kv_lead(hp);
        glds16(wv[0], wsrc, lbase + L_WRING + w * 1024);
        glds16(wv[1], wsrc, lbase + L_WRING + w * 1024 + 4096);
        if (w < 2) glds16(wv[2], wsrc, lbase + L_WRING + w * 1024 + 8192);
    }
    const rsrc_t rs = P.rs;
    __builtin_amdgcn_s_setprio(2);
    *(LAS u32x4*)(L + L_AIMG + w * 1024 + lane * 16) = u32x4{0, 0, 0, 0};
    if (lane < 48) *(LAS u32x2*)(L + L_AIMG + 4096 + w * 384 + lane * 8) = u32x2{0, 0};
    f32x4 acc[2][5];
    const bool kl = !(m & 4);
#pragma unroll
    for (int ct = 0; ct < 5; ++ct) {
        float iv = ct < 2 ? ONE_EPS : (ct < 4 ? 0.f : (kl ? ONE_EPS : 0.f));
        acc[0][ct] = f32x4{iv, iv, iv, iv};
        acc[1][ct] = acc[0][ct];
    }
    lptr XA0 = L + L_XRING + w * 4096 + m * 128 + ((kg ^ (m & 7)) << 4);
    lptr XA1 = L + L_XRING + w * 4096 + m * 128 + (((4 + kg) ^ (m & 7)) << 4);
    const int ra = (8 * kg + 2 * ((lane >> 2) & 3)) * 80;
    lptr WA = L + L_WRING + ra + (lane & 3) * 8 + kv_lead(hp);
    lptr WL = L + L_WRING + ra + (lane & 1) * 5120 + 32 + kv_lead(hp);
    volatile LAS u32* FULL = (volatile LAS u32*)(L + L_FLAG + 40);
    volatile LAS u32* FREE = (volatile LAS u32*)(L + L_FLAG + w * 4);
    u32x4 X[18][2];
    u32x2 B[2][5][2];
    auto rdx = [&](int i, int e) {
        X[2 * i + (e >> 1)][e & 1] = *(LAS u32x4*)(((e >> 1) ? XA1 : XA0) + (i % 3) * 16384 + (e & 1) * 2048);
    };
    auto rdb = [&](int i, int ks, int q) {
        const int o = (i & 3) * 10240 + ks * 2560;
        lptr p = q == 2 ? WL + o : WA + o + (q >= 3 ? 5120 : 0) + ((q == 1 || q == 4) ? 40 : 0);
        B[ks][q][0] = trread(p);
        B[ks][q][1] = trread(p + 80);
    };
    auto mm = [&](int k, int s) {
        constexpr int ord[5] = {0, 1, 4, 2, 3};
        const int q = s >> 1, r = s & 1;
        acc[r][ord[q]] = mfma32(X[k][r], cat(B[k & 1][q][0], B[k & 1][q][1]), acc[r][ord[q]]);
    };
    lptr XTA[3];
    u32 xts = WS_XT + (hp * 12 + min(kg, 2) * 4) * 16384 + (1008 - 64 * rt - 16 * w + m) * 16;
#pragma unroll
    for (int e = 0; e < 3; ++e) {
        const int B = ((72 * hp) & 127) + 24 * min(kg, 2) + 8 * e, r = 30 - 2 * m;
        XTA[e] = L + L_XRING + (B >> 7) * 16384 + w * 4096 + r * 128 + ((((B >> 4) ^ r) & 7) << 4) + (B & 8);
        PIN(XTA[e]);
    }
    PIN(xts);
    u32x2 ev[3], od[3];
    u32 tag = 0, f = 0;
    u32x4 fq;
    WAIT_V(0);
    flag_set(L, 12 + w, 1);
    quad_wait(L + L_FLAG + 48, 1);
    STAMP(1);
#pragma unroll
    for (int e = 0; e < 4; ++e) rdx(0, e);
#pragma unroll
    for (int q = 0; q < 5; ++q) rdb(0, 0, q);
    SCHED();
#pragma unroll
    for (int i = 0; i < 9; ++i) {
#pragma unroll
        for (int s = 0; s < 10; ++s) {
            mm(2 * i, s);
            if (i == 1 && s >= 5 && s < 9) {
                u32 d[3];
#pragma unroll
                for (int c = 0; c < 3; ++c) {
                    const int q = 3 * (s - 5) + c;
                    d[c] = __builtin_amdgcn_perm(ev[q >> 2][(q >> 1) & 1], od[q >> 2][(q >> 1) & 1], (q & 1) ? 0x07060302u : 0x05040100u);
                }
                st16(rs, seal(d[0], d[1], d[2], tag), xts, (s - 5) * 16384);
            }
            if (s < 5) rdb(i, 1, s);
            if (s == 5 && i < 8) f = *FULL;
            SCHED();
        }
#pragma unroll
        for (int s = 0; s < 10; ++s) {
            mm(2 * i + 1, s);
            if (s == 0 && i < 8) {
                if (__builtin_amdgcn_readfirstlane(f) < (u32)(i + 2)) flag_wait1(L, 10, i + 2);
                CFENCE();
                if (i == 0) {
#pragma unroll
                    for (int e = 0; e < 3; ++e) { ev[e] = *(LAS u32x2*)(XTA[e]); od[e] = *(LAS u32x2*)((lptr)((u32)(uintptr_t)XTA[e] ^ 16) + 128); }
                    tag = mk_tag(__builtin_amdgcn_readfirstlane(*(volatile LAS u32*)(L + L_FLAG + 32)));
                }
            }
            if (i == 8 && s == 8) { fq = *(volatile LAS u32x4*)(L + L_FLAG); f = *FULL; }
            if (i < 8) {
                if (s < 2) { rdx(i + 1, 2 * s); rdx(i + 1, 2 * s + 1); }
                if (s >= 2 && s < 7) rdb(i + 1, 0, s - 2);
                if (s == 4) { WAIT_L(6); *FREE = i + 1; }
            }
            SCHED();
        }
        STAMP(16 + i);
    }
    STAMP(2);
    {
        u32x4 op[5];
        const float ca = kl ? EPS : __builtin_inff(), cb = kl ? ONE_EPS : -__builtin_inff();
#pragma unroll
        for (int ct = 0; ct < 5; ++ct) {
            f32x4 v[2];
#pragma unroll
            for (int r = 0; r < 2; ++r)
#pragma unroll
                for (int i = 0; i < 4; ++i) {
                    float a = acc[r][ct][i];
                    if (ct < 2) a = phi(a);
                    if (ct == 4) a = __builtin_amdgcn_fmed3f(__builtin_amdgcn_exp2f(fmaf(a, LOG2E, -ONE_EPS * LOG2E)) + ca, a, cb);
                    v[r][i] = a;
                }
            op[ct] = u32x4{pk2(v[0][0], v[0][1]), pk2(v[0][2], v[0][3]), pk2(v[1][0], v[1][1]), pk2(v[1][2], v[1][3])};
        }
#pragma unroll
        for (int i = 0; i < 4; ++i) op[4][i] = m >= 12 ? 0x3f803f80u : op[4][i];
        const f32x4 Z = {0, 0, 0, 0};
        f32x4 j1[2], j3[2], j5[2], j7;
        j3[0] = mfma32(op[4], op[0], Z);
        j3[1] = mfma32(op[4], op[1], Z);
        j7 = mfma32(op[4], op[4], Z);
        j5[0] = mfma32(op[4], op[2], Z);
        j5[1] = mfma32(op[4], op[3], Z);
        j1[0] = mfma32(op[0], op[2], Z);
        j1[1] = mfma32(op[1], op[3], Z);
        f32x2 ex[2];
#pragma unroll
        for (int h = 0; h < 2; ++h)
#pragma unroll
            for (int i = 0; i < 2; ++i) ex[h][i] = (kg & 1) ? j3[h][2 * h + i] : (kg ? j7[2 * h + i] : j5[h][2 * h + i]);
        if (!quad_ok(fq, 7)) quad_wait(L + L_FLAG, 7);
        lptr K4 = L + L_KVX + w * 3072 + lane * 16, K2 = L + L_KVX + w * 3072 + 1024 + lane * 8;
#pragma unroll
        for (int h = 0; h < 2; ++h) {
            *(LAS f32x4*)(K4 + h * 1536) = j1[h];
            *(LAS f32x2*)(K2 + h * 1536) = ex[h];
        }
        flag_set(L, w, 20);
    }
    STAMP(3);
    const u32 lo = lane * 16;
    const int hq = hp ^ 1;
    if (__builtin_amdgcn_readfirstlane(f) < 10) flag_wait1(L, 10, 10);
    STAMP(4);
    f32x4 q[2][3];
#pragma unroll
    for (int r = 0; r < 2; ++r)
#pragma unroll
        for (int t = 0; t < 3; ++t) q[r][t] = f32x4{ONE_EPS, ONE_EPS, ONE_EPS, ONE_EPS};
    u32x4 g[8], WF[45];
    const int grp = 9 * (cq >> 1) + 5 * (cq & 1) + w;
    const u32 wf_main = WS_WF + grp * 36864;
    const u32 wf_ext = WS_WF + (((9 * (cq >> 1) + 4) * 18 + 9 * (w >> 1)) * 2 + (cq & 1)) * 1024;
    auto ldwf = [&](int i) { WF[i] = i < 9 ? ld16(rs, lo, wf_ext + i * 2048) : ld16(rs, lo, wf_main + (i - 9) * 1024); };
    {
        lptr QA = L + L_XRING + ra + (lane & 3) * 8 + q_lead(hq), QL = L + L_XRING + ra + 32 + q_lead(hq);
        u32x2 a[3][3][2];
        auto rdq = [&](int k, int t) {
            const int i = k >> 1, o = (i / 3) * 16384 + (i % 3) * 5120 + (k & 1) * 2560;
            lptr p = t == 2 ? QL + o : QA + o + t * 40;
            a[k % 3][t][0] = trread(p);
            a[k % 3][t][1] = trread(p + 80);
        };
#pragma unroll
        for (int t = 0; t < 6; ++t) rdq(t / 3, t % 3);
        SCHED();
#pragma unroll
        for (int k = 0; k < 18; ++k) {
            if (k == 4 || k == 10) {
                if (__builtin_amdgcn_readfirstlane(f) < (k == 4 ? 11u : 12u)) flag_wait1(L, 10, k == 4 ? 11 : 12);
                CFENCE();
            }
#pragma unroll
            for (int s = 0; s < 6; ++s) {
                const int t = s >> 1, r = s & 1;
                q[r][t] = mfma32(cat(a[k % 3][t][0], a[k % 3][t][1]), X[k][r], q[r][t]);
                if (s == 0 && (k == 3 || k == 9)) f = *FULL;
                if ((s & 1) && k < 16) rdq(k + 2, s >> 1);
                if (!(s & 1) && k >= 10 && 3 * (k - 10) + (s >> 1) < WA_) ldwf(3 * (k - 10) + (s >> 1));
                SCHED();
            }
        }
    }
    STAMPX(53);
#pragma unroll
    for (int i = 0; i < 8; ++i) g[i] = ld16(rs, lo, WS_KVG + (hq * 8 + 2 * w) * 4096 + i * 1024);
    SCHED();
    STAMP(5);
    u32x4 pq1[2], pq2[2];
#pragma unroll
    for (int r = 0; r < 2; ++r) {
        u32 p[6];
#pragma unroll
        for (int t = 0; t < 3; ++t) {
            p[2 * t] = pk2(phi(q[r][t][0]), phi(q[r][t][1]));
            p[2 * t + 1] = pk2(phi(q[r][t][2]), phi(q[r][t][3]));
        }
        pq1[r] = u32x4{p[0], p[1], p[2], p[3]};
        pq2[r] = u32x4{p[4], p[5], 0, 0};
    }
    u32x4 sel[2];
    {
        const u32 one = (m & 1) ? 0x3f800000u : 0x3f80u;
#pragma unroll
        for (int s = 0; s < 2; ++s)
#pragma unroll
            for (int i = 0; i < 4; ++i) sel[s][i] = (i < 3 && (m >> 1) == 4 * s + i) ? one : 0u;
    }
    const int hd = w >> 1, kgp = 2 * (w & 1) + (kg >> 1);
    int a0, a1;
    {
        const int DUMP = 38 * 144;
        const int rowE = (20 * hd + m) * 144, ex0 = (16 + 2 * hd) * 144, den = (36 + hd) * 144;
        const int cd = 16 * (m >> 2) + 8 * hd + 2 * (m & 3), cx = 64 + 4 * hd;
        if (!(kg & 1)) {
            a0 = rowE + 16 * kgp + 8 * hd; a1 = 0;
        } else if (kgp == 0) {
            a0 = rowE + cx; a1 = a0 + 2;
        } else if (kgp == 1) {
            a0 = ex0 + cd; a1 = a0 + 144;
        } else if (kgp == 2) {
            a0 = (m >> 1) == 2 + hd ? ex0 + (m & 1) * 144 + cx : (m == 12 ? den + cx : DUMP); a1 = a0 + 2;
        } else {
            a0 = den + cd; a1 = DUMP;
        }
    }
    lptr A0, A2;
    {
        const int kr = m >> 2, i = m & 3;
        A0 = L + L_AIMG + (2 * kr + (i & 1) + 8 * (i >> 1)) * 144 + kg * 16;
        A2 = L + L_AIMG + (i == 0 ? 32 + kr : 36 + ((i - 1) & 1)) * 144 + kg * 16;
    }
    a0 += L_AIMG; a1 += L_AIMG;
    u32 vog = lo + WS_OG + ((rt * 4 + w) * 16 + hq) * 3072;
    PIN(pq1[0]); PIN(pq1[1]); PIN(pq2[0]); PIN(pq2[1]); PIN(sel[0]); PIN(sel[1]); PIN(a0); PIN(a1); PIN(A0); PIN(A2); PIN(vog);
    SCHED();
#pragma unroll
    for (int i = WA_; i < WB_; ++i) ldwf(i);
    SCHED();
    poll_rows<8>(g, rs, lo, WS_KVG + (hq * 8 + 2 * w) * 4096, 1024, tag);
    STAMP(6);
    {
        f32x4 d0 = {0, 0, 0, 0}, d1 = d0;
#pragma unroll
        for (int i = 0; i < 4; ++i) {
            g[i][3] = 0; g[4 + i][3] = 0;
            d0 = mfma32(sel[0], g[i], d0);
            d1 = mfma32(sel[1], g[4 + i], d1);
        }
        d0 = add4(d0, d1);
        const u32 p01 = pk2(d0[0], d0[1]), p23 = pk2(d0[2], d0[3]);
        lptr AI = L;
        if (!(kg & 1)) {
            *(LAS u32x2*)(AI + a0) = u32x2{p01, p23};
        } else {
            *(LAS u16*)(AI + a0) = (u16)p01;
            *(LAS u16*)(AI + a1) = (u16)(p01 >> 16);
        }
    }
    flag_set(L, w, 22);
    quad_wait(L + L_FLAG, 22);
    {
        u32x4 A[3][2];
        A[2][0] = *(LAS u32x4*)(A2); A[2][1] = *(LAS u32x4*)(A2 + 64);
        A[0][0] = *(LAS u32x4*)(A0); A[0][1] = *(LAS u32x4*)(A0 + 64);
        A[1][0] = *(LAS u32x4*)(A0 + 16 * 144); A[1][1] = *(LAS u32x4*)(A0 + 16 * 144 + 64);
        f32x4 o[2][3];
#pragma unroll
        for (int tt = 0; tt < 3; ++tt)
#pragma unroll
            for (int r = 0; r < 2; ++r) {
                const int t = (tt + 2) % 3;
                o[r][t] = mfma32(A[t][0], pq1[r], t == 2 ? f32x4{0, EPS, EPS, 0} : f32x4{0, 0, 0, 0});
                o[r][t] = mfma32(A[t][1], pq2[r], o[r][t]);
            }
        u32 d[2][4];
        float e[2];
#pragma unroll
        for (int r = 0; r < 2; ++r) {
            const float ra = __builtin_amdgcn_rcpf(o[r][2][1]), rb = __builtin_amdgcn_rcpf(o[r][2][2]), rc = kg ? rb : ra;
            d[r][0] = pk2s(o[r][0][0] * ra, o[r][0][1] * ra);
            d[r][1] = pk2s(o[r][0][2] * ra, o[r][0][3] * ra);
            d[r][2] = pk2s(o[r][1][0] * rc, o[r][1][1] * rc);
            d[r][3] = pk2s(o[r][1][2] * rb, o[r][1][3] * rb);
            e[r] = o[r][2][0] * rb;
        }
        st16(rs, seal(d[0][0], d[0][1], d[0][2], tag), vog, 0);
        st16(rs, seal(d[1][0], d[1][1], d[1][2], tag), vog, 1024);
        st16(rs, seal(d[0][3], pk2s(e[0], e[1]), d[1][3], tag), vog, 2048);
    }
    STAMP(7);
#pragma unroll
    for (int i = WB_; i < WC_; ++i) ldwf(i);
    SCHED();
    const int c8 = 32 * (9 * (cq >> 1) + 4) + 16 * (cq & 1);
    lptr IM = L + L_XRING;
    {
        u32x4 og[12];
        const u32 so = WS_OG + (rb * 16 + 4 * w) * 3072;
#pragma unroll
        for (int i = 0; i < 12; ++i) og[i] = ld16(rs, lo, so + i * 1024);
        SCHED();
#pragma unroll
        for (int i = WC_; i < 45; ++i) ldwf(i);
        SCHED();
        STAMP(8);
        poll_rows<12>(og, rs, lo, so, 1024, tag);
        STAMP(9);
        lptr B0 = IM + w * 4608 + m * 16;
#pragma unroll
        for (int h = 0; h < 4; ++h) {
            lptr E = B0 + (h >> 1) * 2304 + ((h & 1) ? 1024 + (kg >> 1) * 256 + ((8 + 4 * kg) & 15) : 4 * kg);
            lptr E2 = B0 + (h >> 1) * 2304 + ((h & 1) ? 2048 + 8 : 1024) + 2 * kg;
#pragma unroll
            for (int i = 0; i < 3; ++i) {
                *(LAS u32*)(E + i * 256) = og[3 * h][i];
                *(LAS u32*)(E + 18432 + i * 256) = og[3 * h + 1][i];
            }
            *(LAS u32*)(E + 768) = og[3 * h + 2][0];
            *(LAS u32*)(E + 18432 + 768) = og[3 * h + 2][2];
            *(LAS u16*)(E2) = (u16)og[3 * h + 2][1];
            *(LAS u16*)(E2 + 18432) = (u16)(og[3 * h + 2][1] >> 16);
        }
    }
    SCHED();
    u32x4 bt[4], b8[1];
    const u32 bs = WS_BT + (rb * 576 + grp * 32) * 128, bs8 = WS_BT + (rb * 576 + c8) * 128 + (w & 1) * 16;
    const u32 bv = (lane >> 2) * 128 + (lane & 3) * 32, gs = WS_G + rb * 256;
    u32x2 g0, g1;
    flag_set(L, w, 23);
    quad_wait(L + L_FLAG, 23);
    STAMP(10); STAMPC(60);
    const f32x4 Z = {0, 0, 0, 0};
    f32x4 o[2][2] = {{Z, Z}, {Z, Z}}, a8 = Z;
    lptr EX = L + L_WRING + (w & 1) * 1024 + lo;
    {
        lptr F = IM + lo, F8 = IM + (w & 1) * 18432 + (w >> 1) * 9216 + lo;
        u32x4 f8[9], f[3][2];
#pragma unroll
        for (int k = 0; k < 9; ++k) f8[k] = *(LAS u32x4*)(F8 + k * 1024);
        auto rdf = [&](int k) { f[k % 3][0] = *(LAS u32x4*)(F + k * 1024); f[k % 3][1] = *(LAS u32x4*)(F + 18432 + k * 1024); };
        SCHED();
#pragma unroll
        for (int k = 0; k < 9; ++k) {
            a8 = mfma32(WF[k], f8[k], a8);
            if (k == 2 || k == 4) rdf(k / 2 - 1);
            SCHED();
        }
#pragma unroll
        for (int k = 0; k < 18; ++k) {
            if (k + 2 < 18) rdf(k + 2);
            if (k >= 8 && k < 12) bt[k - 8] = ld16(rs, lo, bs + (k - 8) * 1024);
            if (k == 12) b8[0] = ld16(rs, bv, bs8);
            if (k == 6) g0 = __builtin_amdgcn_raw_buffer_load_b64(rs, m * 8, gs, AUX_SC1);
            if (k == 7) g1 = __builtin_amdgcn_raw_buffer_load_b64(rs, m * 8, gs + 128, AUX_SC1);
#pragma unroll
            for (int r = 0; r < 2; ++r)
#pragma unroll
                for (int t = 0; t < 2; ++t) o[r][t] = mfma32(WF[9 + 2 * k + t], f[k % 3][r], o[r][t]);
            if (k == 1 && w >= 2) { *(LAS f32x4*)(EX) = a8; }
            if (k == 3 && w >= 2) flag_set(L, w, 24);
            SCHED();
        }
    }
    STAMP(11); STAMPC(61);
    if (!(EXP & 16)) poll_rows<4>(bt, rs, lo, bs, 1024, tag);
    lptr BI = L + L_WRING + 2048 + w * 2560;
#pragma unroll
    for (int i = 0; i < 4; ++i) *(LAS u32x2*)(BI + (lane & 1) * 1024 + i * 256 + (lane >> 1) * 8) = u32x2{bt[i][0], bt[i][1]};
    if (w < 2) {
        if (!(EXP & 16)) poll_rows<1>(b8, rs, bv, bs8, 0, tag);
        *(LAS u32x2*)(BI + 2048 + lane * 8) = u32x2{b8[0][0], b8[0][1]};
    }
#pragma nounroll
    for (u32 it = 0; it < SPIN_MAX && !__all((g0[0] ^ g0[1]) == tag && (g1[0] ^ g1[1]) == tag); ++it) {
        __builtin_amdgcn_s_sleep(2);
        g0 = __builtin_amdgcn_raw_buffer_load_b64(rs, m * 8, gs, AUX_SC1);
        g1 = __builtin_amdgcn_raw_buffer_load_b64(rs, m * 8, gs + 128, AUX_SC1);
        CFENCE();
    }
    CFENCE();
    STAMP(12);
    auto mix = [&](float g, f32x4 a, u32x2 b) {
        const float b0 = lo2f(b[0]), b1 = hi2f(b[0]), b2 = lo2f(b[1]), b3 = hi2f(b[1]);
        return u32x2{pk2s(fmaf(g, a[0] - b0, b0), fmaf(g, a[1] - b1, b1)), pk2s(fmaf(g, a[2] - b2, b2), fmaf(g, a[3] - b3, b3))};
    };
    GAS char* orow = (GAS char*)P.out + (rb * 32 + m) * 1152;
#pragma unroll
    for (int r = 0; r < 2; ++r) {
        const float g = __uint_as_float(r ? g1[0] : g0[0]);
        const u32x2 t0 = trread(BI + r * 1024 + kg * 256 + m * 8), t1 = trread(BI + r * 1024 + kg * 256 + 128 + m * 8);
        *(GAS u32x4*)(orow + r * 18432 + grp * 64 + kg * 16) = cat(mix(g, o[r][0], t0), mix(g, o[r][1], t1));
    }
    if (w < 2) {
        flag_wait1(L, w + 2, 24);
        a8 = add4(a8, *(LAS f32x4*)(EX));
        *(GAS u32x2*)(orow + w * 18432 + c8 * 2 + kg * 8) = mix(__uint_as_float(w ? g1[0] : g0[0]), a8, trread(BI + 2048 + lane * 8));
    }
    STAMP(13);
}

struct cf { float r, i; };
DEV cf cmul(cf a, cf b) { return cf{fmaf(a.r, b.r, -(a.i * b.i)), fmaf(a.r, b.i, a.i * b.r)}; }
DEV cf csq(cf a) { return cf{fmaf(a.r, a.r, -(a.i * a.i)), (a.r + a.r) * a.i}; }
DEV cf cexp(float lr, float t) {
    const float e = __builtin_amdgcn_exp2f(lr), f = __builtin_amdgcn_fractf(t);
    return cf{e * __builtin_amdgcn_cosf(f), e * __builtin_amdgcn_sinf(f)};
}

DEV u32 kgen_role(Params& P, lptr L, int cl, int sg, int lane) {
    const int ch = 3 * sg + cl;
    const int m = lane & 15, kg = lane >> 4;
    lptr C = L + L_CH + cl * CH_BYTES;
    const int n = ch * 64 + lane;
    P.arrive<2>();
    u32 are = ((const GAS u32*)P.Are)[n], aim = ((const GAS u32*)P.Aim)[n], bre = ((const GAS u32*)P.Bre)[n];
    u32 bim = ((const GAS u32*)P.Bim)[n], cre = ((const GAS u32*)P.Cre)[n], cim = ((const GAS u32*)P.Cim)[n];
    const u16 ldt = ((const GAS u16*)P.logdt)[ch], dsk = ((const GAS u16*)P.Dskip)[ch];
    SCHED();
    u32 ga[2][20];
#pragma unroll
    for (int it = 0; it < 2; ++it) {
        const int l0 = (it * 64 + lane) * 16 - 4;
#pragma unroll
        for (int j = 0; j < 10; ++j) {
            const int la = l0 + 2 * j, lb = la + 1;
            const u32 bits = pk2((float)la, (float)lb);
            int ia = (int)(bits & 0xffff) - 0x4280, ib = (int)(bits >> 16) - 0x4280;
            if (it == 0) {
                ia = la < 256 ? (la < 0 ? 641 : la) : ia;
                ib = lb < 256 ? (lb < 0 ? 641 : lb) : ib;
            }
            ga[it][2 * j] = L_KT + cl * 1312 + 2 * ia;
            ga[it][2 * j + 1] = L_KT + cl * 1312 + 2 * ib;
        }
    }
#pragma unroll
    for (int it = 0; it < 2; ++it)
#pragma unroll
        for (int j = 0; j < 20; ++j) asm volatile("" : "+v"(ga[it][j]));
    SCHED();
    const float dt = lo2f(pk2(expf(bf2f(ldt)), 0.f));
    STAMP(1); STAMPC(60);
    f32x4 a0 = {lane == 0 ? bf2f(dsk) : 0.f, 0, 0, 0}, a1 = {0, 0, 0, 0}, a2 = a1;
    LAS u32* T = (LAS u32*)(C + CH_KC + (lane >> 2) * TQ + (lane & 3) * 4);
    lptr R = C + CH_KC + kg * TQ + m * 16;
#pragma nounroll
    for (int u = 0; u < 2; ++u) {
        auto hf = [&](u32& v) { const float r = hi2f(v); v <<= 16; return r; };
        const float lr = dt * hf(are) * LOG2E, t = dt * hf(aim) * 0.15915494309189533577f;
        const cf c = cmul(cf{hf(cre), hf(cim)}, cf{dt * hf(bre), dt * hf(bim)});
        cf p[16], A[9];
        p[1] = cexp(lr, t);
        const cf w16 = cexp(16.f * lr, 16.f * t);
        A[1] = cexp(256.f * lr, 256.f * t);
        auto put = [&](int row, cf v) { if (EXP & 4) { u32 z = pk2s(v.r, v.i); asm volatile("" :: "v"(z)); } else T[row * 4] = pk2s(v.r, v.i); };
        T[0] = 0x3f80u;
        put(1, p[1]);
#pragma unroll
        for (int k = 2; k < 16; ++k) {
            p[k] = (k & 1) ? cmul(p[k - 1], p[1]) : csq(p[k / 2]);
            put(k, p[k]);
        }
        {
            const cf w32 = csq(w16);
            cf h0 = cf{c.r, -c.i}, h1 = cmul(c, w16);
            h1.i = -h1.i;
            const cf wc = cf{w32.r, -w32.i};
#pragma unroll
            for (int k = 0; k < 8; ++k) {
                put(41 + 2 * k, h0);
                put(42 + 2 * k, h1);
                if (k < 7) { h0 = cmul(h0, wc); h1 = cmul(h1, wc); }
            }
        }
        A[2] = csq(A[1]); A[3] = cmul(A[2], A[1]); A[4] = csq(A[2]);
        A[5] = cmul(A[4], A[1]); A[6] = csq(A[3]); A[7] = cmul(A[4], A[3]); A[8] = csq(A[4]);
#pragma unroll
        for (int k = 0; k < 8; ++k) put(16 + k, k ? cmul(A[1], p[2 * k]) : A[1]);
#pragma unroll
        for (int k = 0; k < 8; ++k) put(24 + k, (k & 3) ? cmul(A[2 + (k >> 2)], p[4 * (k & 3)]) : A[2 + (k >> 2)]);
#pragma unroll
        for (int k = 0; k < 8; ++k) put(32 + k, (k & 1) ? cmul(A[4 + (k >> 1)], p[8]) : A[4 + (k >> 1)]);
        put(40, A[8]);
        CFENCE();
        STAMPD(8 + u);
#pragma unroll
        for (int s = 0; s < 4; ++s) {
            const u32x4 H = *(LAS u32x4*)(R + s * 4 * TQ + 41 * 16);
            a0 = mfma32(*(LAS u32x4*)(R + s * 4 * TQ), H, a0);
            a1 = mfma32(*(LAS u32x4*)(R + s * 4 * TQ + 256), H, a1);
            a2 = mfma32(*(LAS u32x4*)(R + s * 4 * TQ + 512), H, a2);
        }
        CFENCE();
        STAMPD(6 + u);
    }
    STAMP(2); STAMPC(61);
    const u32 tag = mk_tag(get_epoch(L));
    u32x4 g[6];
    const u32 xs = WS_XT + sg * 16384 + cl * 5120;
#pragma unroll
    for (int i = 0; i < 6; ++i) g[i] = ld16(P.rs, lane * 16, xs + i * 1024);
    SCHED();
    {
        lptr KT = L + L_KT + cl * 1312;
        *(LAS u32x2*)(KT + 2 * (16 * m + 4 * kg)) = u32x2{pk2(a0[0], a0[1]), pk2(a0[2], a0[3])};
        *(LAS u32x2*)(KT + 2 * (kg < 2 ? 256 + 8 * m + 4 * kg : 256 + 64 * kg + 4 * m)) = u32x2{pk2(a1[0], a1[1]), pk2(a1[2], a1[3])};
        if (kg < 2) {
            *(LAS u32*)(KT + 2 * (512 + 64 * kg + 2 * m)) = pk2(a2[0], a2[1]);
            *(LAS u32*)(KT + 2 * (544 + 64 * kg + 2 * m)) = pk2(a2[2], a2[3]);
        }
        if (lane == 32) *(LAS u32*)(KT + 2 * 640) = pk2(a2[0], 0.f);
        CFENCE();
    }
#pragma unroll
    for (int it = 0; it < 2; ++it) {
        u32 v[20], d[10], e[9];
#pragma unroll
        for (int j = 0; j < 20; ++j) v[j] = *(LAS u16*)(L + ga[it][j]);
#pragma unroll
        for (int j = 1; j < 10; ++j) d[j] = v[2 * j] | (v[2 * j + 1] << 16);
#pragma unroll
        for (int j = 0; j < 9; ++j) e[j] = v[2 * j + 1] | (v[2 * j + 2] << 16);
        lptr W = C + CH_KC + 32 + (it * 64 + lane) * 32;
        *(LAS u32x4*)(W) = u32x4{d[2], d[3], d[4], d[5]};
        *(LAS u32x4*)(W + 16) = u32x4{d[6], d[7], d[8], d[9]};
        *(LAS u32x4*)(W + CH_KSTRIDE) = u32x4{e[1], e[2], e[3], e[4]};
        *(LAS u32x4*)(W + CH_KSTRIDE + 16) = u32x4{e[5], e[6], e[7], e[8]};
        *(LAS u32x4*)(W + 2 * CH_KSTRIDE) = u32x4{d[1], d[2], d[3], d[4]};
        *(LAS u32x4*)(W + 2 * CH_KSTRIDE + 16) = u32x4{d[5], d[6], d[7], d[8]};
        *(LAS u32x4*)(W + 3 * CH_KSTRIDE) = u32x4{e[0], e[1], e[2], e[3]};
        *(LAS u32x4*)(W + 3 * CH_KSTRIDE + 16) = u32x4{e[4], e[5], e[6], e[7]};
    }
    if (lane < 8) *(LAS u32x4*)(C + CH_KC + (lane >> 1) * CH_KSTRIDE + (lane & 1) * 16) = u32x4{0, 0, 0, 0};
    *(LAS u32x2*)(C + CH_XR + 4096 + lane * 8) = u32x2{0, 0};
    SCHED();
    STAMP(10);
    poll_rows<6>(g, P.rs, lane * 16, xs, 1024, tag);
    lptr XR = L + L_CH + CH_XR + cl * 1280 + lane * 4;
#pragma unroll
    for (int i = 0; i < 6; ++i)
#pragma unroll
        for (int c = 0; c < 3; ++c) *(LAS u32*)(XR + c * CH_BYTES + i * 256) = g[i][c];
    return tag;
}

template <int MASK, int NA>
DEV void conv_blocks(rsrc_t rs, lptr C, int ch, int lane, u32 tag) {
    const int m = lane & 15, kg = lane >> 4;
    f32x4 acc[8];
#pragma unroll
    for (int i = 0; i < 8; ++i) acc[i] = f32x4{0, 0, 0, 0};
    lptr KA = C + CH_KC + ((~m) & 3) * CH_KSTRIDE + 2 * (8 * kg + ((m + 4) & ~3));
    lptr XB = C + CH_XR + 2 * (240 - 16 * m + 8 * kg);
    u32x4 A[2][NA], B[2][NA];
    auto rd = [&](int j, int a) {
        A[j & 1][a] = cat(*(volatile LAS u32x2*)(KA + 64 * j + 512 * a), *(volatile LAS u32x2*)(KA + 64 * j + 512 * a + 8));
        B[j & 1][a] = *(LAS u32x4*)(XB + 64 * j + 512 * (7 - a));
    };
#pragma unroll
    for (int a = 0; a < NA; ++a) rd(0, a);
    SCHED();
#pragma unroll
    for (int j = 0; j < 8; ++j) {
        int n = 0;
#pragma unroll
        for (int I = 0; I < 8; ++I)
            if (MASK >> I & 1)
#pragma unroll
                for (int a = 0; a <= I; ++a) {
                    acc[I] = mfma32(A[j & 1][a], B[j & 1][I - a], acc[I]);
                    if (j < 7 && !(n & 1) && (n >> 1) < NA) rd(j + 1, n >> 1);
                    ++n;
                    SCHED();
                }
    }
    const u32 vo = (m >> 1) * 73728 + kg * 32 + (m & 1) * 16;
#pragma unroll
    for (int I = 0; I < 8; ++I)
        if (MASK >> I & 1) st16(rs, seal(pk2(acc[I][0], acc[I][1]), pk2(acc[I][2], acc[I][3]), 0u, tag), vo, WS_BT + I * 8 * 73728 + ch * 128);
}

DEV void gate_role(const Params& P, rsrc_t rs, int lane, int rb, int ps, u32 tag) {
    const GAS char* gw = (const GAS char*)P.gw + (lane & 3) * 16;
    {
        const int row = rb * 32 + ps * 16 + (lane >> 2);
        const GAS char* xr = (const GAS char*)P.x + row * 1152 + (lane & 3) * 16;
        u32x4 xv[18], gv[18];
#pragma unroll
        for (int i = 0; i < 18; ++i) { xv[i] = *(const GAS u32x4*)(xr + i * 64); gv[i] = *(const GAS u32x4*)(gw + i * 64); }
        float s0 = 0.f, s1 = 0.f;
#pragma unroll
        for (int i = 0; i < 18; ++i)
#pragma unroll
            for (int j = 0; j < 4; ++j) {
                s0 = fmaf(lo2f(xv[i][j]), lo2f(gv[i][j]), s0);
                s1 = fmaf(hi2f(xv[i][j]), hi2f(gv[i][j]), s1);
            }
        float s = s0 + s1;
        s += __shfl_xor(s, 1);
        s += __shfl_xor(s, 2);
        const u32 g = __float_as_uint(1.0f / (1.0f + __expf(-s)));
        if ((lane & 3) == 0) __builtin_amdgcn_raw_buffer_store_b64(u32x2{g, g ^ tag}, rs, WS_G + row * 8, 0, AUX_SC1);
    }
}

DEV u32 wf_role(Params& P, lptr L, u32 lbase, int cl, int lane, int unit) {
    const int ks = unit / 9, gp = unit - ks * 9;
    P.arrive<3>();
    const rsrc_t rs = P.rs;
    if (unit < 162) {
        const char* src = (const char*)P.wo + ks * 32 * 1152 + gp * 128;
#pragma unroll
        for (int i = 0; i < 4; ++i) glds16((u32)((lane >> 3) * 1152 + (lane & 7) * 16), src + i * 8 * 1152, lbase + L_CH + cl * CH_BYTES + i * 1040);
    }
    if (unit >= 162) return mk_tag(get_epoch(L));
    WAIT_V(0);
    lptr S = L + L_CH + cl * CH_BYTES + (lane >> 4) * 1040 + ((lane >> 2) & 3) * 128;
#pragma unroll
    for (int h = 0; h < 2; ++h) {
        const int grp = 2 * gp + h;
        const bool sp = grp == 4 || grp == 13;
        lptr p = S + h * 64 + (lane & 3) * (sp ? 8 : 16);
#pragma unroll
        for (int t = 0; t < 2; ++t) {
            lptr q = p + t * (sp ? 32 : 8);
            st16(rs, cat(trread(q), trread(q + 512)), lane * 16, WS_WF + ((grp * 18 + ks) * 2 + t) * 1024);
        }
    }
    SCHED();
    const u32 tag = mk_tag(get_epoch(L));
    WAIT_V(0);
    if (lane == 0) __builtin_amdgcn_raw_buffer_store_b32(tag, rs, WS_WFF + unit * 4, 0, AUX_SC1);
    return tag;
}

__global__ __launch_bounds__(512, 2) void fused(Args A) {
    Params P;
    extern __shared__ __attribute__((aligned(64))) char smem[];
    lptr L = (lptr)smem;
    const u32 lbase = (u32)(uintptr_t)L;
    const int tid = threadIdx.x, lane = tid & 63, w = __builtin_amdgcn_readfirstlane(tid >> 6);
    if (w == 5) {
        *(volatile LAS u32*)(L + L_FLAG + (lane & 15) * 4) = (lane & 15) == 7;
        WAIT_L0();
    }
    P.fetch();
    asm volatile("s_barrier" ::: "memory");
    const int bid = blockIdx.x, xcd = bid & 7, slot = bid >> 3;
    const int rt = (xcd >> 1) * 4 + (slot & 3), hp = (xcd & 1) * 8 + ((((xcd & 1) ? 0x60754321u : 0x71654320u) >> (slot & 28)) & 7);
    const int rb = 8 * xcd + (slot & 7), cq = slot >> 3, cg = w & 3;
    if (w < 4) {
        gemm_role(P, L, lbase, w, lane, rt, hp, rb, cq);
        if (bid == 0 && tid == 0) {
            const u32 ep = *(volatile LAS u32*)(L + L_FLAG + 32);
            __builtin_amdgcn_raw_buffer_store_b32(ep + 1, P.rs, WS_EPOCH, 0, AUX_SC1);
        }
    } else if (w == 7) {
        const u32 tag = loader_role(P, L, lbase, lane, rt, hp);
        if (slot < 24) {
            const int sg = xcd * 24 + slot;
            quad_wait(L + L_FLAG + 16, 1);
            STAMP(3);
#pragma nounroll
            for (int c = 0; c < 3; ++c) {
                STAMP(4);
                conv_blocks<0x0E, 4>(P.rs, L + L_CH + c * CH_BYTES, 3 * sg + c, lane, tag);
            }
            STAMP(5);
        }
    } else if (slot < 24) {
        const int sg = xcd * 24 + slot;
        const u32 tag = kgen_role(P, L, cg, sg, lane);
        flag_set(L, w, 1);
        STAMP(3);
        quad_wait(L + L_FLAG + 16, 1);
        STAMP(4);
        conv_blocks<0xF1, 8>(P.rs, L + L_CH + cg * CH_BYTES, 3 * sg + cg, lane, tag);
        STAMP(5);
    } else {
        const u32 tag = wf_role(P, L, lbase, cg, lane, ((slot - 24) * 8 + xcd) * 3 + cg);
        STAMP(3);
        if (cg < 2) {
            flag_wait1(L, 10, 12);
            gate_role(P, P.rs, lane, rb, cg, tag);
        }
        STAMP(4);
    }
}

extern "C" void kernel_launch(void* const* d_in, const int* in_sizes, int n_in, void* d_out, int out_size, void* d_ws,
                              size_t ws_size, hipStream_t stream) {
    Args P;
    P.x = (const u16*)d_in[0];
    P.wqkv = (const u16*)d_in[1];
    P.wo = (const u16*)d_in[2];
    P.Are = (const u16*)d_in[4];
    P.Aim = (const u16*)d_in[5];
    P.Bre = (const u16*)d_in[6];
    P.Bim = (const u16*)d_in[7];
    P.Cre = (const u16*)d_in[8];
    P.Cim = (const u16*)d_in[9];
    P.logdt = (const u16*)d_in[10];
    P.Dskip = (const u16*)d_in[11];
    P.gw = (const u16*)d_in[12];
    P.ws = (char*)d_ws;
    P.out = (u16*)d_out;
    (void)hipFuncSetAttribute((const void*)fused, hipFuncAttributeMaxDynamicSharedMemorySize, LDS_BYTES);
    fused<<<256, 512, LDS_BYTES, stream>>>(P);
}
```

```cpp
#include <hip/hip_runtime.h>
#include <hip/hip_bf16.h>
#include <stdint.h>
#include <utility>

typedef unsigned short u16;
typedef unsigned int u32;
typedef unsigned long long u64;
typedef u32 u32x4 __attribute__((ext_vector_type(4)));
typedef u32 u32x2 __attribute__((ext_vector_type(2)));
typedef float f32x4 __attribute__((ext_vector_type(4)));
typedef float f32x2 __attribute__((ext_vector_type(2)));
typedef __bf16 bf16x8 __attribute__((ext_vector_type(8)));
typedef __bf16 bf16x2 __attribute__((ext_vector_type(2)));
typedef short s16x4 __attribute__((ext_vector_type(4)));
#define LAS __attribute__((address_space(3)))
#define GAS __attribute__((address_space(1)))
#define DEV __device__ __forceinline__
typedef LAS char* lptr;
typedef __amdgpu_buffer_rsrc_t rsrc_t;

constexpr u32 WS_EPOCH = 0;
constexpr u32 WS_WFF = 256;
constexpr u32 WS_G = 4096;
constexpr u32 WS_KVG = 65536;
constexpr u32 WS_WF = WS_KVG + 524288;
constexpr u32 WS_XT = WS_WF + 663552;
constexpr u32 WS_OG = WS_XT + 3145728;
constexpr u32 WS_BT = WS_OG + 3145728;
constexpr u32 WS_DBG = WS_BT + 4718592;
constexpr u32 WS_END = WS_DBG + 1048576;
constexpr int AUX_SC1 = 16;
#define EXP 0
#define SPIN_LOG 17
constexpr u32 SPIN_MAX = 1u << SPIN_LOG;

constexpr u32 L_FLAG = 0;
constexpr u32 CH_KC = 0, CH_KSTRIDE = 4160, CH_XR = 16640, CH_BYTES = 21248;
constexpr u32 L_CH = 64;
constexpr u32 L_KT = L_CH + 3 * CH_BYTES;
constexpr u32 L_XRING = L_KT + 3 * 1312;
constexpr u32 L_WRING = L_XRING + 49152;
constexpr u32 L_AIMG = L_WRING + 40960;
constexpr u32 LDS_BYTES = L_AIMG + 5632;
constexpr u32 L_KVX = L_WRING + 10240;
constexpr u32 TQ = 944;
static_assert(LDS_BYTES <= 163840, "lds");
static_assert(L_XRING % 32 == 0, "align");

DEV float bf2f(u16 v) { return __uint_as_float(((u32)v) << 16); }
DEV u32 pk2(float a, float b) {
    f32x2 v = {a, b};
    return __builtin_bit_cast(u32, __builtin_convertvector(v, bf16x2));
}
DEV u32 pk2s(float a, float b) {
    u32 r;
    asm("v_cvt_pk_bf16_f32 %0, %1, %2" : "=v"(r) : "v"(a), "v"(b));
    return r;
}
DEV float lo2f(u32 v) { return __uint_as_float(v << 16); }
DEV float hi2f(u32 v) { return __uint_as_float(v & 0xffff0000u); }
DEV f32x4 mfma32(u32x4 a, u32x4 b, f32x4 c) {
    return __builtin_amdgcn_mfma_f32_16x16x32_bf16(__builtin_bit_cast(bf16x8, a), __builtin_bit_cast(bf16x8, b), c, 0, 0, 0);
}
DEV void glds16(u32 voff, const void* sbase, u32 ldsaddr) {
    asm volatile("s_mov_b32 m0, %2\n\ts_nop 0\n\tglobal_load_lds_dwordx4 %0, %1" ::"v"(voff), "s"(sbase), "s"(ldsaddr) : "memory");
}
DEV void glds16x4(u32 v0, u32 v1, u32 v2, u32 v3, const void* sbase, u32 ldsaddr) {
    asm volatile("s_mov_b32 m0, %5\n\ts_nop 0\n\tglobal_load_lds_dwordx4 %0, %4\n\tglobal_load_lds_dwordx4 %1, %4 offset:1024\n\t"
                 "global_load_lds_dwordx4 %2, %4 offset:2048\n\tglobal_load_lds_dwordx4 %3, %4 offset:3072" ::"v"(v0), "v"(v1), "v"(v2), "v"(v3),
                 "s"(sbase), "s"(ldsaddr) : "memory");
}
DEV void glds_word(u32 voff, const void* sbase, u32 ldsaddr) {
    asm volatile("s_mov_b32 m0, %2\n\ts_mov_b64 exec, 1\n\tglobal_load_lds_dword %0, %1\n\ts_mov_b64 exec, -1" ::"v"(voff), "s"(sbase), "s"(ldsaddr) : "memory");
}
DEV u32x2 trread(lptr p) { return __builtin_bit_cast(u32x2, __builtin_amdgcn_ds_read_tr16_b64_v4i16((LAS s16x4*)p)); }
DEV u32x4 cat(u32x2 a, u32x2 b) { return u32x4{a[0], a[1], b[0], b[1]}; }
DEV u32x4 ld16(rsrc_t rs, u32 voff, u32 soff) { return __builtin_amdgcn_raw_buffer_load_b128(rs, voff, soff, AUX_SC1); }
DEV void st16(rsrc_t rs, u32x4 v, u32 voff, u32 soff) { __builtin_amdgcn_raw_buffer_store_b128(v, rs, voff + soff, 0, AUX_SC1); }
DEV u32 xor3(u32 a, u32 b, u32 c) { return __builtin_amdgcn_bitop3_b32(a, b, c, 0x96); }
DEV u32x4 seal(u32 a, u32 b, u32 c, u32 tag) { return u32x4{a, b, c, xor3(a, b, tag) ^ c}; }
DEV u32 unseal(u32x4 g, u32 tag) { return xor3(xor3(g[0], g[1], tag), g[2], g[3]); }
#define WAIT_V(n) asm volatile("s_waitcnt vmcnt(%0)" ::"n"(n) : "memory")
#define WAIT_L(n) asm volatile("s_waitcnt lgkmcnt(%0)" ::"n"(n) : "memory")
#define WAIT_L0() asm volatile("s_waitcnt lgkmcnt(0)" ::: "memory")
#define RAW_BARRIER() asm volatile("s_waitcnt lgkmcnt(0)\n\ts_barrier" ::: "memory")
#define CFENCE() asm volatile("" ::: "memory")
#define SCHED() __builtin_amdgcn_sched_barrier(0)

#define STAMP(i) do {} while (0)
#define STAMPD(i) do {} while (0)
#define STAMPC(i) do {} while (0)

#define STAMPXD(i) do {} while (0)
#define STAMPX(i) do {} while (0)
DEV void flag_set(lptr L, int wave, u32 v) {
    WAIT_L0();
    *(volatile LAS u32*)(L + L_FLAG + wave * 4) = v;
}
DEV void flag_wait1(lptr L, int wave, u32 v) {
#pragma nounroll
    for (u32 it = 0; it < SPIN_MAX * 8; ++it) {
        u32 f = *(volatile LAS u32*)(L + L_FLAG + wave * 4);
        if (__builtin_amdgcn_readfirstlane(f) >= v) break;
        __builtin_amdgcn_s_sleep(1);
    }
    CFENCE();
}
DEV bool quad_ok(u32x4 f, u32 v) {
    u32 m = min(min(f[0], f[1]), min(f[2], f[3]));
    return __builtin_amdgcn_readfirstlane(m) >= v;
}
DEV void quad_wait(lptr Q, u32 v) {
#pragma nounroll
    for (u32 it = 0; it < SPIN_MAX * 8; ++it) {
        u32x4 f = *(volatile LAS u32x4*)(Q);
        if (quad_ok(f, v)) break;
        __builtin_amdgcn_s_sleep(1);
    }
    CFENCE();
}
DEV void quad_spin(lptr Q, u32 v) {
#pragma nounroll
    for (u32 it = 0; it < SPIN_MAX * 16; ++it) {
        u32x4 f = *(volatile LAS u32x4*)(Q);
        if (quad_ok(f, v)) break;
        if (EXP & 8) __builtin_amdgcn_s_sleep(1);
    }
    CFENCE();
}
DEV u32 mk_tag(u32 epoch) { return epoch * 0x9E3779B1u + 0x7F4A7C15u; }
DEV u32 get_epoch(lptr L) {
    u32x2 v;
#pragma nounroll
    for (u32 it = 0; it < SPIN_MAX * 8; ++it) {
        v = *(volatile LAS u32x2*)(L + L_FLAG + 32);
        if (__builtin_amdgcn_readfirstlane(v[1])) break;
        __builtin_amdgcn_s_sleep(1);
    }
    CFENCE();
    return __builtin_amdgcn_readfirstlane(v[0]);
}
#define RL(i, so, off) "s_bitcmp1_b32 %[m], 1+" #i "\n\ts_cbranch_scc0 1f\n\tbuffer_load_dwordx4 %" #i ", %[v], %[r], %[" #so "] offen offset:" #off " sc1\n1:\n\t"
#define RL4(i0, i1, i2, i3, so) RL(i0, so, 0) RL(i1, so, 1024) RL(i2, so, 2048) RL(i3, so, 3072)
#define RL_IN [m] "s"(mask), [v] "v"(voff), [r] "s"(rs), [a] "s"(soff), [b] "s"(soff + 4096), [c] "s"(soff + 8192)
#define RL_END "s_waitcnt vmcnt(0)"
#define G4(i) "+v"(g[i]), "+v"(g[i + 1]), "+v"(g[i + 2]), "+v"(g[i + 3])
template <int N>
DEV void reload_rows(u32x4 (&g)[N], u32 mask, rsrc_t rs, u32 voff, u32 soff) {
    if constexpr (N == 1) asm volatile(RL(0, a, 0) RL_END : "+v"(g[0]) : RL_IN : "scc", "memory");
    if constexpr (N == 4) asm volatile(RL4(0, 1, 2, 3, a) RL_END : G4(0) : RL_IN : "scc", "memory");
    if constexpr (N == 6) asm volatile(RL4(0, 1, 2, 3, a) RL(4, b, 0) RL(5, b, 1024) RL_END : G4(0), "+v"(g[4]), "+v"(g[5]) : RL_IN : "scc", "memory");
    if constexpr (N == 8) asm volatile(RL4(0, 1, 2, 3, a) RL4(4, 5, 6, 7, b) RL_END : G4(0), G4(4) : RL_IN : "scc", "memory");
    if constexpr (N == 12) asm volatile(RL4(0, 1, 2, 3, a) RL4(4, 5, 6, 7, b) RL4(8, 9, 10, 11, c) RL_END : G4(0), G4(4), G4(8) : RL_IN : "scc", "memory");
}
template <int N, int... I>
DEV void poll_seq(u32x4 (&g)[N], rsrc_t rs, u32 voff, u32 soff, u32 tag, std::integer_sequence<int, I...>) {
    u32x4 r[N] = {g[I]...};
    if (__builtin_expect(__any((unseal(r[I], tag) | ...) != 0), 0)) {
        u32 left = SPIN_MAX;
#pragma nounroll
        do {
            const u32 mask = ((__builtin_amdgcn_ballot_w64(unseal(r[I], tag) != 0) ? 1u << I : 0u) | ...);
            reload_rows(r, mask, rs, voff, soff);
            left = mask ? left - 1 : 0;
        } while (left);
    }
    ((g[I] = r[I]), ...);
    CFENCE();
}
template <int N>
DEV void poll_rows(u32x4 (&g)[N], rsrc_t rs, u32 voff, u32 soff, u32 tag) { poll_seq(g, rs, voff, soff, tag, std::make_integer_sequence<int, N>{}); }
#define PIN(x) asm volatile("" : "+v"(x))
#define PINS(x) asm volatile("" : "+s"(x))

struct Args {
    const u16 *x, *wqkv, *wo, *Are, *Aim, *Bre, *Bim, *Cre, *Cim, *logdt, *Dskip, *gw;
    char* ws;
    u16* out;
};
typedef u64 u64x8 __attribute__((ext_vector_type(8)));
typedef u64 u64x4 __attribute__((ext_vector_type(4)));
typedef u64 u64x2 __attribute__((ext_vector_type(2)));
struct Params : Args {
    u64x8 k0; u64x4 k1; u64x2 k2;
    rsrc_t rs;
    DEV void fetch() {
        asm volatile("s_load_dwordx16 %0, %3, 0x0\n\ts_load_dwordx8 %1, %3, 0x40\n\ts_load_dwordx4 %2, %3, 0x60"
                     : "=&s"(k0), "=&s"(k1), "=&s"(k2) : "s"(__builtin_amdgcn_kernarg_segment_ptr()));
    }
    DEV static u64 take(u64 v) {
        u64 r;
        asm volatile("s_mov_b64 %0, %1" : "=s"(r) : "s"(v));
        return r;
    }
    template <int ROLE>
    DEV void arrive() {
        asm volatile("s_waitcnt lgkmcnt(0)" ::"s"(k0), "s"(k1), "s"(k2));
        if (ROLE != 2) x = (const u16*)take(k0[0]);
        if (ROLE < 2) wqkv = (const u16*)take(k0[1]);
        if (ROLE == 3) { wo = (const u16*)take(k0[2]); gw = (const u16*)take(k1[3]); }
        if (ROLE == 2) {
            Are = (const u16*)take(k0[3]); Aim = (const u16*)take(k0[4]); Bre = (const u16*)take(k0[5]); Bim = (const u16*)take(k0[6]);
            Cre = (const u16*)take(k0[7]); Cim = (const u16*)take(k1[0]); logdt = (const u16*)take(k1[1]); Dskip = (const u16*)take(k1[2]);
        }
        ws = (char*)take(k2[0]);
        if (ROLE == 0) out = (u16*)take(k2[1]);
        rs = __builtin_amdgcn_make_buffer_rsrc((void*)ws, 0, (int)WS_END, 0x00020000);
    }
};
DEV void pos5(u32 idx, u32& pos, u32& piece) {
    pos = (idx * 52429u) >> 18;
    piece = idx - 5 * pos;
}
DEV u32 pos2row(u32 pos) { return (pos & 56) | ((pos >> 1) & 3) | ((pos & 1) << 2); }

DEV int first_chunk(int hp) { return (9 * hp) >> 4; }
DEV int kv_lead(int hp) { return hp ? 8 : 0; }
DEV int q_lead(int hp) { return hp == 15 ? 8 : 0; }
DEV float add1(float a, float b) { asm("" : "+v"(a)); float r = a + b; asm("" : "+v"(r)); return r; }
DEV f32x4 add4(f32x4 a, f32x4 b) { return f32x4{add1(a[0], b[0]), add1(a[1], b[1]), add1(a[2], b[2]), add1(a[3], b[3])}; }
DEV bool wf_ok(u32x4 f, int lane, u32 tag) { return __all(f[0] == tag && f[1] == tag && (lane >= 40 || (f[2] == tag && f[3] == tag))); }
DEV u32 loader_role(Params& P, lptr L, u32 lbase, int lane, int rt, int hp) {
    u32 wv[5], xv[4];
#pragma unroll
    for (int p = 0; p < 5; ++p) {
        u32 pos, piece;
        pos5(p * 64 + lane, pos, piece);
        wv[p] = pos2row(pos) * 3456 + piece * 16 - (p & 3) * 1024;
    }
#pragma unroll
    for (int j = 0; j < 4; ++j) xv[j] = (u32)((lane >> 3) * 1152 + (((lane & 7) ^ (lane >> 3)) << 4)) + j * 8192;
    const char* seq;
    asm volatile("s_getpc_b64 %0\n\ts_branch 0f\n\t.long 1, 2, 3, 4, 5, 6, 7, 8, 9, 10, 11, 12\n0:" : "=s"(seq));
    u32 nv = 4;
    u64 ord = 0;
    {
        const u64 ks = (((0x87654328765432ull >> (8 * (rt & 3))) & 0xfffffffull) << 8) | 0x10;
        const u32 c0 = first_chunk(hp);
#pragma unroll
        for (int i = 0; i < 9; ++i) {
            const u32 c = c0 + ((u32)(ks >> (4 * i)) & 15);
            ord |= (u64)min(c, c - 9) << (4 * i);
        }
    }
    PINS(ord);
    u32 fo = WS_WFF + min(lane, 40) * 16, vk = (lane >> 4) * 4096 + (lane & 15) * 16 + WS_KVG + hp * 32768 + rt * 256;
    PIN(fo); PIN(vk);
    P.arrive<1>();
    glds_word(WS_EPOCH, P.ws, lbase + L_FLAG + 32);
    glds_word(nv, seq, lbase + L_FLAG + 36);
    const char* xg = (const char*)P.x + rt * 128 * 1152;
    const char* wg = (const char*)P.wqkv + 72 * hp + 1152 - kv_lead(hp);
    const char* qg = (const char*)P.wqkv + 72 * (hp ^ 1) - q_lead(hp ^ 1);
    auto five = [&](const char* src, u32 dst) {
        glds16x4(wv[0], wv[1], wv[2], wv[3], src, dst);
        glds16(wv[4], src, dst + 4096);
    };
    auto full = [&]() { nv += 4; glds_word(nv, seq, lbase + L_FLAG + 40); };
    u32x4 fl = {0, 0, 0, 0};
    auto room = [&](int v) {
        if (v > 0 && !quad_ok(fl, v)) quad_spin(L + L_FLAG, v);
        fl = *(volatile LAS u32x4*)(L + L_FLAG);
    };
    u32 xs = lbase + L_XRING + 16384;
    u64 o = ord;
#pragma nounroll
    for (int j = 1; j < 9; ++j) {
        o >>= 4;
        const u32 c = (u32)o & 15, ws = lbase + L_WRING + (j & 3) * 10240;
        room(j - 3);
        STAMPXD(16 + 2 * j);
        five(wg + c * 221184, ws);
        five(wg + c * 221184 + 1152, ws + 5120);
        if (j == 3) { STAMP(1); room(1); }
#pragma unroll
        for (int w = 0; w < 4; ++w) glds16x4(xv[0], xv[1], xv[2], xv[3], xg + c * 128 + w * 36864, xs + w * 4096);
        full();
        STAMPXD(17 + 2 * j);
        __builtin_amdgcn_s_setprio(3);
        xs = xs == lbase + L_XRING + 32768 ? lbase + L_XRING : xs + 16384;
    }
    u32x4 wff;
    asm volatile("buffer_load_dwordx4 %0, %1, %2, 0 offen sc1" : "=v"(wff) : "v"(fo), "s"(P.rs) : "memory");
    const u32 ep = *(volatile LAS u32*)(L + L_FLAG + 32);
    o = ord;
#pragma nounroll
    for (int s = 0; s < 3; ++s) {
        room(6 + s);
        STAMPXD(34 + 2 * s);
        if (s) full();
#pragma unroll
        for (int i = 0; i < 3; ++i) {
            five(qg + ((u32)o & 15) * 221184, lbase + L_XRING + s * 16384 + i * 5120);
            o >>= 4;
        }
        STAMPXD(35 + 2 * s);
    }
    const u32 tag = mk_tag(__builtin_amdgcn_readfirstlane(ep));
    asm volatile("s_waitcnt vmcnt(47)" : "+v"(wff)::"memory");
#pragma nounroll
    for (u32 it = 0; __builtin_expect(!wf_ok(wff, lane, tag), 0) && it < SPIN_MAX; ++it) {
        __builtin_amdgcn_s_sleep(8);
        asm volatile("buffer_load_dwordx4 %0, %1, %2, 0 offen sc1\n\ts_waitcnt vmcnt(0)" : "=v"(wff) : "v"(fo), "s"(P.rs) : "memory");
    }
    full();
    STAMP(6);
    quad_spin(L + L_FLAG, 20);
    STAMPX(8);
    {
        lptr K4 = L + L_KVX + lane * 16, K2 = L + L_KVX + 1024 + lane * 8;
        f32x4 r4[2][4];
        f32x2 r2[2][4];
#pragma unroll
        for (int h = 0; h < 2; ++h)
#pragma unroll
            for (int w = 0; w < 4; ++w) { r4[h][w] = *(LAS f32x4*)(K4 + w * 3072 + h * 1536); r2[h][w] = *(LAS f32x2*)(K2 + w * 3072 + h * 1536); }
        SCHED();
#pragma unroll
        for (int h = 0; h < 2; ++h) {
            const f32x4 s4 = add4(add4(r4[h][0], r4[h][1]), add4(r4[h][2], r4[h][3]));
            float s2[2];
#pragma unroll
            for (int i = 0; i < 2; ++i) s2[i] = add1(add1(r2[h][0][i], r2[h][1][i]), add1(r2[h][2][i], r2[h][3][i]));
            st16(P.rs, seal(pk2(s4[0], s4[1]), pk2(s4[2], s4[3]), pk2(s2[0], s2[1]), tag), vk, h * 16384);
        }
    }
    __builtin_amdgcn_s_setprio(0);
    STAMP(7);
    WAIT_V(2);
    STAMP(2);
    return tag;
}
#define WA_ 16
#define WB_ 26
#define WC_ 45

constexpr float LOG2E = 1.44269504088896340736f;
constexpr float EPS = 1e-6f;
constexpr float ONE_EPS = 1.0f + 1e-6f;
DEV float phi(float a) {
    float e = __builtin_amdgcn_exp2f(fmaf(a, LOG2E, -ONE_EPS * LOG2E));
    return __builtin_amdgcn_fmed3f(e + EPS, a, ONE_EPS);
}

DEV void gemm_role(Params& P, lptr L, u32 lbase, int w, int lane, int rt, int hp, int rb, int cq) {
    const int m = lane & 15, kg = lane >> 4;
    {
        const u32 xo = (u32)((lane >> 3) * 1152 + (((lane & 7) ^ (lane >> 3)) << 4));
        u32 wv[3];
#pragma unroll
        for (int j = 0; j < 3; ++j) {
            u32 pos, piece;
            pos5((w + 4 * j) * 64 + lane, pos, piece);
            wv[j] = pos2row(pos) * 3456 + (pos >> 6) * 1152 + piece * 16;
        }
        const int c0 = first_chunk(hp);
        P.arrive<0>();
        glds16x4(xo, xo + 8192, xo + 16384, xo + 24576, (const char*)P.x + (rt * 128 + w * 32) * 1152 + c0 * 128, lbase + L_XRING + w * 4096);
        const char* wsrc = (const char*)P.wqkv + 72 * hp + c0 * 221184 + 1152 - kv_lead(hp);
        glds16(wv[0], wsrc, lbase + L_WRING + w * 1024);
        glds16(wv[1], wsrc, lbase + L_WRING + w * 1024 + 4096);
        if (w < 2) glds16(wv[2], wsrc, lbase + L_WRING + w * 1024 + 8192);
    }
    const rsrc_t rs = P.rs;
    __builtin_amdgcn_s_setprio(2);
    *(LAS u32x4*)(L + L_AIMG + w * 1024 + lane * 16) = u32x4{0, 0, 0, 0};
    if (lane < 48) *(LAS u32x2*)(L + L_AIMG + 4096 + w * 384 + lane * 8) = u32x2{0, 0};
    f32x4 acc[2][5];
    const bool kl = !(m & 4);
#pragma unroll
    for (int ct = 0; ct < 5; ++ct) {
        float iv = ct < 2 ? ONE_EPS : (ct < 4 ? 0.f : (kl ? ONE_EPS : 0.f));
        acc[0][ct] = f32x4{iv, iv, iv, iv};
        acc[1][ct] = acc[0][ct];
    }
    lptr XA0 = L + L_XRING + w * 4096 + m * 128 + ((kg ^ (m & 7)) << 4);
    lptr XA1 = L + L_XRING + w * 4096 + m * 128 + (((4 + kg) ^ (m & 7)) << 4);
    const int ra = (8 * kg + 2 * ((lane >> 2) & 3)) * 80;
    lptr WA = L + L_WRING + ra + (lane & 3) * 8 + kv_lead(hp);
    lptr WL = L + L_WRING + ra + (lane & 1) * 5120 + 32 + kv_lead(hp);
    volatile LAS u32* FULL = (volatile LAS u32*)(L + L_FLAG + 40);
    volatile LAS u32* FREE = (volatile LAS u32*)(L + L_FLAG + w * 4);
    u32x4 X[18][2];
    u32x2 B[2][5][2];
    auto rdx = [&](int i, int e) {
        X[2 * i + (e >> 1)][e & 1] = *(LAS u32x4*)(((e >> 1) ? XA1 : XA0) + (i % 3) * 16384 + (e & 1) * 2048);
    };
    auto rdb = [&](int i, int ks, int q) {
        const int o = (i & 3) * 10240 + ks * 2560;
        lptr p = q == 2 ? WL + o : WA + o + (q >= 3 ? 5120 : 0) + ((q == 1 || q == 4) ? 40 : 0);
        B[ks][q][0] = trread(p);
        B[ks][q][1] = trread(p + 80);
    };
    auto mm = [&](int k, int s) {
        constexpr int ord[5] = {0, 1, 4, 2, 3};
        const int q = s >> 1, r = s & 1;
        acc[r][ord[q]] = mfma32(X[k][r], cat(B[k & 1][q][0], B[k & 1][q][1]), acc[r][ord[q]]);
    };
    lptr XTA[3];
    u32 xts = WS_XT + (hp * 12 + min(kg, 2) * 4) * 16384 + (1008 - 64 * rt - 16 * w + m) * 16;
#pragma unroll
    for (int e = 0; e < 3; ++e) {
        const int B = ((72 * hp) & 127) + 24 * min(kg, 2) + 8 * e, r = 30 - 2 * m;
        XTA[e] = L + L_XRING + (B >> 7) * 16384 + w * 4096 + r * 128 + ((((B >> 4) ^ r) & 7) << 4) + (B & 8);
        PIN(XTA[e]);
    }
    PIN(xts);
    u32x2 ev[3], od[3];
    u32 tag = 0, f = 0;
    u32x4 fq;
    WAIT_V(0);
    flag_set(L, 12 + w, 1);
    quad_wait(L + L_FLAG + 48, 1);
    STAMP(1);
#pragma unroll
    for (int e = 0; e < 4; ++e) rdx(0, e);
#pragma unroll
    for (int q = 0; q < 5; ++q) rdb(0, 0, q);
    SCHED();
#pragma unroll
    for (int i = 0; i < 9; ++i) {
#pragma unroll
        for (int s = 0; s < 10; ++s) {
            mm(2 * i, s);
            if (i == 1 && s >= 5 && s < 9) {
                u32 d[3];
#pragma unroll
                for (int c = 0; c < 3; ++c) {
                    const int q = 3 * (s - 5) + c;
                    d[c] = __builtin_amdgcn_perm(ev[q >> 2][(q >> 1) & 1], od[q >> 2][(q >> 1) & 1], (q & 1) ? 0x07060302u : 0x05040100u);
                }
                st16(rs, seal(d[0], d[1], d[2], tag), xts, (s - 5) * 16384);
            }
            if (s < 5) rdb(i, 1, s);
            if (s == 5 && i < 8) f = *FULL;
            SCHED();
        }
#pragma unroll
        for (int s = 0; s < 10; ++s) {
            mm(2 * i + 1, s);
            if (s == 0 && i < 8) {
                if (__builtin_amdgcn_readfirstlane(f) < (u32)(i + 2)) flag_wait1(L, 10, i + 2);
                CFENCE();
                if (i == 0) {
#pragma unroll
                    for (int e = 0; e < 3; ++e) { ev[e] = *(LAS u32x2*)(XTA[e]); od[e] = *(LAS u32x2*)((lptr)((u32)(uintptr_t)XTA[e] ^ 16) + 128); }
                    tag = mk_tag(__builtin_amdgcn_readfirstlane(*(volatile LAS u32*)(L + L_FLAG + 32)));
                }
            }
            if (i == 8 && s == 8) { fq = *(volatile LAS u32x4*)(L + L_FLAG); f = *FULL; }
            if (i < 8) {
                if (s < 2) { rdx(i + 1, 2 * s); rdx(i + 1, 2 * s + 1); }
                if (s >= 2 && s < 7) rdb(i + 1, 0, s - 2);
                if (s == 4) { WAIT_L(6); *FREE = i + 1; }
            }
            SCHED();
        }
        STAMP(16 + i);
    }
    STAMP(2);
    {
        u32x4 op[5];
        const float ca = kl ? EPS : __builtin_inff(), cb = kl ? ONE_EPS : -__builtin_inff();
#pragma unroll
        for (int ct = 0; ct < 5; ++ct) {
            f32x4 v[2];
#pragma unroll
            for (int r = 0; r < 2; ++r)
#pragma unroll
                for (int i = 0; i < 4; ++i) {
                    float a = acc[r][ct][i];
                    if (ct < 2) a = phi(a);
                    if (ct == 4) a = __builtin_amdgcn_fmed3f(__builtin_amdgcn_exp2f(fmaf(a, LOG2E, -ONE_EPS * LOG2E)) + ca, a, cb);
                    v[r][i] = a;
                }
            op[ct] = u32x4{pk2(v[0][0], v[0][1]), pk2(v[0][2], v[0][3]), pk2(v[1][0], v[1][1]), pk2(v[1][2], v[1][3])};
        }
#pragma unroll
        for (int i = 0; i < 4; ++i) op[4][i] = m >= 12 ? 0x3f803f80u : op[4][i];
        const f32x4 Z = {0, 0, 0, 0};
        f32x4 j1[2], j3[2], j5[2], j7;
        j3[0] = mfma32(op[4], op[0], Z);
        j3[1] = mfma32(op[4], op[1], Z);
        j7 = mfma32(op[4], op[4], Z);
        j5[0] = mfma32(op[4], op[2], Z);
        j5[1] = mfma32(op[4], op[3], Z);
        j1[0] = mfma32(op[0], op[2], Z);
        j1[1] = mfma32(op[1], op[3], Z);
        f32x2 ex[2];
#pragma unroll
        for (int h = 0; h < 2; ++h)
#pragma unroll
            for (int i = 0; i < 2; ++i) ex[h][i] = (kg & 1) ? j3[h][2 * h + i] : (kg ? j7[2 * h + i] : j5[h][2 * h + i]);
        if (!quad_ok(fq, 7)) quad_wait(L + L_FLAG, 7);
        lptr K4 = L + L_KVX + w * 3072 + lane * 16, K2 = L + L_KVX + w * 3072 + 1024 + lane * 8;
#pragma unroll
        for (int h = 0; h < 2; ++h) {
            *(LAS f32x4*)(K4 + h * 1536) = j1[h];
            *(LAS f32x2*)(K2 + h * 1536) = ex[h];
        }
        flag_set(L, w, 20);
    }
    STAMP(3);
    const u32 lo = lane * 16;
    const int hq = hp ^ 1;
    if (__builtin_amdgcn_readfirstlane(f) < 10) flag_wait1(L, 10, 10);
    STAMP(4);
    f32x4 q[2][3];
#pragma unroll
    for (int r = 0; r < 2; ++r)
#pragma unroll
        for (int t = 0; t < 3; ++t) q[r][t] = f32x4{ONE_EPS, ONE_EPS, ONE_EPS, ONE_EPS};
    u32x4 g[8], WF[45];
    const int grp = 9 * (cq >> 1) + 5 * (cq & 1) + w;
    const u32 wf_main = WS_WF + grp * 36864;
    const u32 wf_ext = WS_WF + (((9 * (cq >> 1) + 4) * 18 + 9 * (w >> 1)) * 2 + (cq & 1)) * 1024;
    auto ldwf = [&](int i) { WF[i] = __builtin_amdgcn_raw_buffer_load_b128(rs, lo, i < 9 ? wf_ext + i * 2048 : wf_main + (i - 9) * 1024, (EXP & 32) ? 0 : AUX_SC1); };
    {
        lptr QA = L + L_XRING + ra + (lane & 3) * 8 + q_lead(hq), QL = L + L_XRING + ra + 32 + q_lead(hq);
        u32x2 a[3][3][2];
        auto rdq = [&](int k, int t) {
            const int i = k >> 1, o = (i / 3) * 16384 + (i % 3) * 5120 + (k & 1) * 2560;
            lptr p = t == 2 ? QL + o : QA + o + t * 40;
            a[k % 3][t][0] = trread(p);
            a[k % 3][t][1] = trread(p + 80);
        };
#pragma unroll
        for (int t = 0; t < 6; ++t) rdq(t / 3, t % 3);
        SCHED();
#pragma unroll
        for (int k = 0; k < 18; ++k) {
            if (k == 4 || k == 10) {
                if (__builtin_amdgcn_readfirstlane(f) < (k == 4 ? 11u : 12u)) flag_wait1(L, 10, k == 4 ? 11 : 12);
                CFENCE();
            }
#pragma unroll
            for (int s = 0; s < 6; ++s) {
                const int t = s >> 1, r = s & 1;
                q[r][t] = mfma32(cat(a[k % 3][t][0], a[k % 3][t][1]), X[k][r], q[r][t]);
                if (s == 0 && (k == 3 || k == 9)) f = *FULL;
                if ((s & 1) && k < 16) rdq(k + 2, s >> 1);
                if (!(s & 1) && k >= 10 && 3 * (k - 10) + (s >> 1) < WA_) ldwf(3 * (k - 10) + (s >> 1));
                SCHED();
            }
        }
    }
    STAMPX(53);
#pragma unroll
    for (int i = 0; i < 8; ++i) g[i] = ld16(rs, lo, WS_KVG + (hq * 8 + 2 * w) * 4096 + i * 1024);
    SCHED();
    STAMP(5);
    u32x4 pq1[2], pq2[2];
#pragma unroll
    for (int r = 0; r < 2; ++r) {
        u32 p[6];
#pragma unroll
        for (int t = 0; t < 3; ++t) {
            p[2 * t] = pk2(phi(q[r][t][0]), phi(q[r][t][1]));
            p[2 * t + 1] = pk2(phi(q[r][t][2]), phi(q[r][t][3]));
        }
        pq1[r] = u32x4{p[0], p[1], p[2], p[3]};
        pq2[r] = u32x4{p[4], p[5], 0, 0};
    }
    u32x4 sel[2];
    {
        const u32 one = (m & 1) ? 0x3f800000u : 0x3f80u;
#pragma unroll
        for (int s = 0; s < 2; ++s)
#pragma unroll
            for (int i = 0; i < 4; ++i) sel[s][i] = (i < 3 && (m >> 1) == 4 * s + i) ? one : 0u;
    }
    const int hd = w >> 1, kgp = 2 * (w & 1) + (kg >> 1);
    int a0, a1;
    {
        const int DUMP = 38 * 144;
        const int rowE = (20 * hd + m) * 144, ex0 = (16 + 2 * hd) * 144, den = (36 + hd) * 144;
        const int cd = 16 * (m >> 2) + 8 * hd + 2 * (m & 3), cx = 64 + 4 * hd;
        if (!(kg & 1)) {
            a0 = rowE + 16 * kgp + 8 * hd; a1 = 0;
        } else if (kgp == 0) {
            a0 = rowE + cx; a1 = a0 + 2;
        } else if (kgp == 1) {
            a0 = ex0 + cd; a1 = a0 + 144;
        } else if (kgp == 2) {
            a0 = (m >> 1) == 2 + hd ? ex0 + (m & 1) * 144 + cx : (m == 12 ? den + cx : DUMP); a1 = a0 + 2;
        } else {
            a0 = den + cd; a1 = DUMP;
        }
    }
    lptr A0, A2;
    {
        const int kr = m >> 2, i = m & 3;
        A0 = L + L_AIMG + (2 * kr + (i & 1) + 8 * (i >> 1)) * 144 + kg * 16;
        A2 = L + L_AIMG + (i == 0 ? 32 + kr : 36 + ((i - 1) & 1)) * 144 + kg * 16;
    }
    a0 += L_AIMG; a1 += L_AIMG;
    u32 vog = lo + WS_OG + ((rt * 4 + w) * 16 + hq) * 3072;
    PIN(pq1[0]); PIN(pq1[1]); PIN(pq2[0]); PIN(pq2[1]); PIN(sel[0]); PIN(sel[1]); PIN(a0); PIN(a1); PIN(A0); PIN(A2); PIN(vog);
    SCHED();
#pragma unroll
    for (int i = WA_; i < WB_; ++i) ldwf(i);
    SCHED();
    poll_rows<8>(g, rs, lo, WS_KVG + (hq * 8 + 2 * w) * 4096, tag);
    STAMP(6);
    {
        f32x4 d0 = {0, 0, 0, 0}, d1 = d0;
#pragma unroll
        for (int i = 0; i < 4; ++i) {
            g[i][3] = 0; g[4 + i][3] = 0;
            d0 = mfma32(sel[0], g[i], d0);
            d1 = mfma32(sel[1], g[4 + i], d1);
        }
        d0 = add4(d0, d1);
        const u32 p01 = pk2(d0[0], d0[1]), p23 = pk2(d0[2], d0[3]);
        lptr AI = L;
        if (!(kg & 1)) {
            *(LAS u32x2*)(AI + a0) = u32x2{p01, p23};
        } else {
            *(LAS u16*)(AI + a0) = (u16)p01;
            *(LAS u16*)(AI + a1) = (u16)(p01 >> 16);
        }
    }
    flag_set(L, w, 22);
    quad_wait(L + L_FLAG, 22);
    {
        u32x4 A[3][2];
        A[2][0] = *(LAS u32x4*)(A2); A[2][1] = *(LAS u32x4*)(A2 + 64);
        A[0][0] = *(LAS u32x4*)(A0); A[0][1] = *(LAS u32x4*)(A0 + 64);
        A[1][0] = *(LAS u32x4*)(A0 + 16 * 144); A[1][1] = *(LAS u32x4*)(A0 + 16 * 144 + 64);
        f32x4 o[2][3];
#pragma unroll
        for (int tt = 0; tt < 3; ++tt)
#pragma unroll
            for (int r = 0; r < 2; ++r) {
                const int t = (tt + 2) % 3;
                o[r][t] = mfma32(A[t][0], pq1[r], t == 2 ? f32x4{0, EPS, EPS, 0} : f32x4{0, 0, 0, 0});
                o[r][t] = mfma32(A[t][1], pq2[r], o[r][t]);
            }
        u32 d[2][4];
        float e[2];
#pragma unroll
        for (int r = 0; r < 2; ++r) {
            const float ra = __builtin_amdgcn_rcpf(o[r][2][1]), rb = __builtin_amdgcn_rcpf(o[r][2][2]), rc = kg ? rb : ra;
            d[r][0] = pk2s(o[r][0][0] * ra, o[r][0][1] * ra);
            d[r][1] = pk2s(o[r][0][2] * ra, o[r][0][3] * ra);
            d[r][2] = pk2s(o[r][1][0] * rc, o[r][1][1] * rc);
            d[r][3] = pk2s(o[r][1][2] * rb, o[r][1][3] * rb);
            e[r] = o[r][2][0] * rb;
        }
        st16(rs, seal(d[0][0], d[0][1], d[0][2], tag), vog, 0);
        st16(rs, seal(d[1][0], d[1][1], d[1][2], tag), vog, 1024);
        st16(rs, seal(d[0][3], pk2s(e[0], e[1]), d[1][3], tag), vog, 2048);
    }
    STAMP(7);
#pragma unroll
    for (int i = WB_; i < WC_; ++i) ldwf(i);
    SCHED();
    const int c8 = 32 * (9 * (cq >> 1) + 4) + 16 * (cq & 1);
    lptr IM = L + L_XRING;
    {
        u32x4 og[12];
        const u32 so = WS_OG + (rb * 16 + 4 * w) * 3072;
#pragma unroll
        for (int i = 0; i < 12; ++i) og[i] = ld16(rs, lo, so + i * 1024);
        SCHED();
#pragma unroll
        for (int i = WC_; i < 45; ++i) ldwf(i);
        SCHED();
        STAMP(8);
        poll_rows<12>(og, rs, lo, so, tag);
        STAMP(9);
        lptr B0 = IM + w * 4608 + m * 16;
#pragma unroll
        for (int h = 0; h < 4; ++h) {
            lptr E = B0 + (h >> 1) * 2304 + ((h & 1) ? 1024 + (kg >> 1) * 256 + ((8 + 4 * kg) & 15) : 4 * kg);
            lptr E2 = B0 + (h >> 1) * 2304 + ((h & 1) ? 2048 + 8 : 1024) + 2 * kg;
#pragma unroll
            for (int i = 0; i < 3; ++i) {
                *(LAS u32*)(E + i * 256) = og[3 * h][i];
                *(LAS u32*)(E + 18432 + i * 256) = og[3 * h + 1][i];
            }
            *(LAS u32*)(E + 768) = og[3 * h + 2][0];
            *(LAS u32*)(E + 18432 + 768) = og[3 * h + 2][2];
            *(LAS u16*)(E2) = (u16)og[3 * h + 2][1];
            *(LAS u16*)(E2 + 18432) = (u16)(og[3 * h + 2][1] >> 16);
        }
    }
    SCHED();
    u32x4 bt[4], b8[1];
    const u32 bs = WS_BT + (rb * 576 + grp * 32) * 128, bs8 = WS_BT + (rb * 576 + c8) * 128 + (w & 1) * 16;
    const u32 bv = (lane >> 2) * 128 + (lane & 3) * 32, gs = WS_G + rb * 256;
    u32x2 g0, g1;
    flag_set(L, w, 23);
    quad_wait(L + L_FLAG, 23);
    STAMP(10); STAMPC(60);
    const f32x4 Z = {0, 0, 0, 0};
    f32x4 o[2][2] = {{Z, Z}, {Z, Z}}, a8 = Z;
    lptr EX = L + L_WRING + (w & 1) * 1024 + lo;
    {
        lptr F = IM + lo, F8 = IM + (w & 1) * 18432 + (w >> 1) * 9216 + lo;
        u32x4 f8[9], f[3][2];
#pragma unroll
        for (int k = 0; k < 9; ++k) f8[k] = *(LAS u32x4*)(F8 + k * 1024);
        auto rdf = [&](int k) { f[k % 3][0] = *(LAS u32x4*)(F + k * 1024); f[k % 3][1] = *(LAS u32x4*)(F + 18432 + k * 1024); };
        SCHED();
#pragma unroll
        for (int k = 0; k < 9; ++k) {
            a8 = mfma32(WF[k], f8[k], a8);
            if (k == 2 || k == 4) rdf(k / 2 - 1);
            SCHED();
        }
#pragma unroll
        for (int k = 0; k < 18; ++k) {
            if (k + 2 < 18) rdf(k + 2);
            if (k >= 8 && k < 12) bt[k - 8] = ld16(rs, lo, bs + (k - 8) * 1024);
            if (k == 12) b8[0] = ld16(rs, bv, bs8);
            if (k == 6) g0 = __builtin_amdgcn_raw_buffer_load_b64(rs, m * 8, gs, AUX_SC1);
            if (k == 7) g1 = __builtin_amdgcn_raw_buffer_load_b64(rs, m * 8, gs + 128, AUX_SC1);
#pragma unroll
            for (int r = 0; r < 2; ++r)
#pragma unroll
                for (int t = 0; t < 2; ++t) o[r][t] = mfma32(WF[9 + 2 * k + t], f[k % 3][r], o[r][t]);
            if (k == 1 && w >= 2) { *(LAS f32x4*)(EX) = a8; }
            if (k == 3 && w >= 2) flag_set(L, w, 24);
            SCHED();
        }
    }
    STAMP(11); STAMPC(61);
    if (!(EXP & 16)) poll_rows<4>(bt, rs, lo, bs, tag);
    lptr BI = L + L_WRING + 2048 + w * 2560;
#pragma unroll
    for (int i = 0; i < 4; ++i) *(LAS u32x2*)(BI + (lane & 1) * 1024 + i * 256 + (lane >> 1) * 8) = u32x2{bt[i][0], bt[i][1]};
    if (w < 2) {
        if (!(EXP & 16)) poll_rows<1>(b8, rs, bv, bs8, tag);
        *(LAS u32x2*)(BI + 2048 + lane * 8) = u32x2{b8[0][0], b8[0][1]};
    }
#pragma nounroll
    for (u32 it = 0; it < SPIN_MAX && !__all((g0[0] ^ g0[1]) == tag && (g1[0] ^ g1[1]) == tag); ++it) {
        __builtin_amdgcn_s_sleep(2);
        g0 = __builtin_amdgcn_raw_buffer_load_b64(rs, m * 8, gs, AUX_SC1);
        g1 = __builtin_amdgcn_raw_buffer_load_b64(rs, m * 8, gs + 128, AUX_SC1);
        CFENCE();
    }
    CFENCE();
    STAMP(12);
    auto mix = [&](float g, f32x4 a, u32x2 b) {
        const float b0 = lo2f(b[0]), b1 = hi2f(b[0]), b2 = lo2f(b[1]), b3 = hi2f(b[1]);
        return u32x2{pk2s(fmaf(g, a[0] - b0, b0), fmaf(g, a[1] - b1, b1)), pk2s(fmaf(g, a[2] - b2, b2), fmaf(g, a[3] - b3, b3))};
    };
    GAS char* orow = (GAS char*)P.out + (rb * 32 + m) * 1152;
#pragma unroll
    for (int r = 0; r < 2; ++r) {
        const float g = __uint_as_float(r ? g1[0] : g0[0]);
        const u32x2 t0 = trread(BI + r * 1024 + kg * 256 + m * 8), t1 = trread(BI + r * 1024 + kg * 256 + 128 + m * 8);
        *(GAS u32x4*)(orow + r * 18432 + grp * 64 + kg * 16) = cat(mix(g, o[r][0], t0), mix(g, o[r][1], t1));
    }
    if (w < 2) {
        flag_wait1(L, w + 2, 24);
        a8 = add4(a8, *(LAS f32x4*)(EX));
        *(GAS u32x2*)(orow + w * 18432 + c8 * 2 + kg * 8) = mix(__uint_as_float(w ? g1[0] : g0[0]), a8, trread(BI + 2048 + lane * 8));
    }
    STAMP(13);
}
#define KP_ 0
#define CP_ 0

struct cf { float r, i; };
DEV cf cmul(cf a, cf b) { return cf{fmaf(a.r, b.r, -(a.i * b.i)), fmaf(a.r, b.i, a.i * b.r)}; }
DEV cf csq(cf a) { return cf{fmaf(a.r, a.r, -(a.i * a.i)), (a.r + a.r) * a.i}; }
DEV cf cexp(float lr, float t) {
    const float e = __builtin_amdgcn_exp2f(lr), f = __builtin_amdgcn_fractf(t);
    return cf{e * __builtin_amdgcn_cosf(f), e * __builtin_amdgcn_sinf(f)};
}

DEV u32 kgen_role(Params& P, lptr L, int cl, int sg, int lane) {
    const int ch = 3 * sg + cl;
    const int m = lane & 15, kg = lane >> 4;
    lptr C = L + L_CH + cl * CH_BYTES;
    const int n = ch * 64 + lane;
    P.arrive<2>();
    u32 are = ((const GAS u32*)P.Are)[n], aim = ((const GAS u32*)P.Aim)[n], bre = ((const GAS u32*)P.Bre)[n];
    u32 bim = ((const GAS u32*)P.Bim)[n], cre = ((const GAS u32*)P.Cre)[n], cim = ((const GAS u32*)P.Cim)[n];
    const u16 ldt = ((const GAS u16*)P.logdt)[ch], dsk = ((const GAS u16*)P.Dskip)[ch];
    SCHED();
    u32 ga[2][20];
#pragma unroll
    for (int it = 0; it < 2; ++it) {
        const int l0 = (it * 64 + lane) * 16 - 4;
#pragma unroll
        for (int j = 0; j < 10; ++j) {
            const int la = l0 + 2 * j, lb = la + 1;
            const u32 bits = pk2((float)la, (float)lb);
            int ia = (int)(bits & 0xffff) - 0x4280, ib = (int)(bits >> 16) - 0x4280;
            if (it == 0) {
                ia = la < 256 ? (la < 0 ? 641 : la) : ia;
                ib = lb < 256 ? (lb < 0 ? 641 : lb) : ib;
            }
            ga[it][2 * j] = L_KT + cl * 1312 + 2 * ia;
            ga[it][2 * j + 1] = L_KT + cl * 1312 + 2 * ib;
        }
    }
#pragma unroll
    for (int it = 0; it < 2; ++it)
#pragma unroll
        for (int j = 0; j < 20; ++j) asm volatile("" : "+v"(ga[it][j]));
    SCHED();
    const float dt = lo2f(pk2(expf(bf2f(ldt)), 0.f));
    STAMP(1); STAMPC(60);
    __builtin_amdgcn_s_setprio(KP_);
    f32x4 a0 = {lane == 0 ? bf2f(dsk) : 0.f, 0, 0, 0}, a1 = {0, 0, 0, 0}, a2 = a1;
    LAS u32* T = (LAS u32*)(C + CH_KC + (lane >> 2) * TQ + (lane & 3) * 4);
    lptr R = C + CH_KC + kg * TQ + m * 16;
#pragma nounroll
    for (int u = 0; u < 2; ++u) {
        auto hf = [&](u32& v) { const float r = hi2f(v); v <<= 16; return r; };
        const float lr = dt * hf(are) * LOG2E, t = dt * hf(aim) * 0.15915494309189533577f;
        const cf c = cmul(cf{hf(cre), hf(cim)}, cf{dt * hf(bre), dt * hf(bim)});
        cf p[16], A[9];
        p[1] = cexp(lr, t);
        const cf w16 = cexp(16.f * lr, 16.f * t);
        A[1] = cexp(256.f * lr, 256.f * t);
        auto put = [&](int row, cf v) { if (EXP & 4) { u32 z = pk2s(v.r, v.i); asm volatile("" :: "v"(z)); } else T[row * 4] = pk2s(v.r, v.i); };
        T[0] = 0x3f80u;
        put(1, p[1]);
#pragma unroll
        for (int k = 2; k < 16; ++k) {
            p[k] = (k & 1) ? cmul(p[k - 1], p[1]) : csq(p[k / 2]);
            put(k, p[k]);
        }
        {
            const cf w32 = csq(w16);
            cf h0 = cf{c.r, -c.i}, h1 = cmul(c, w16);
            h1.i = -h1.i;
            const cf wc = cf{w32.r, -w32.i};
#pragma unroll
            for (int k = 0; k < 8; ++k) {
                put(41 + 2 * k, h0);
                put(42 + 2 * k, h1);
                if (k < 7) { h0 = cmul(h0, wc); h1 = cmul(h1, wc); }
            }
        }
        A[2] = csq(A[1]); A[3] = cmul(A[2], A[1]); A[4] = csq(A[2]);
        A[5] = cmul(A[4], A[1]); A[6] = csq(A[3]); A[7] = cmul(A[4], A[3]); A[8] = csq(A[4]);
#pragma unroll
        for (int k = 0; k < 8; ++k) put(16 + k, k ? cmul(A[1], p[2 * k]) : A[1]);
#pragma unroll
        for (int k = 0; k < 8; ++k) put(24 + k, (k & 3) ? cmul(A[2 + (k >> 2)], p[4 * (k & 3)]) : A[2 + (k >> 2)]);
#pragma unroll
        for (int k = 0; k < 8; ++k) put(32 + k, (k & 1) ? cmul(A[4 + (k >> 1)], p[8]) : A[4 + (k >> 1)]);
        put(40, A[8]);
        CFENCE();
        STAMPD(8 + u);
#pragma unroll
        for (int s = 0; s < 4; ++s) {
            const u32x4 H = *(LAS u32x4*)(R + s * 4 * TQ + 41 * 16);
            a0 = mfma32(*(LAS u32x4*)(R + s * 4 * TQ), H, a0);
            a1 = mfma32(*(LAS u32x4*)(R + s * 4 * TQ + 256), H, a1);
            a2 = mfma32(*(LAS u32x4*)(R + s * 4 * TQ + 512), H, a2);
        }
        CFENCE();
        STAMPD(6 + u);
    }
    STAMP(2); STAMPC(61);
    const u32 tag = mk_tag(get_epoch(L));
    u32x4 g[6];
    const u32 xs = WS_XT + sg * 16384 + cl * 5120;
#pragma unroll
    for (int i = 0; i < 6; ++i) g[i] = ld16(P.rs, lane * 16, xs + i * 1024);
    SCHED();
    {
        lptr KT = L + L_KT + cl * 1312;
        *(LAS u32x2*)(KT + 2 * (16 * m + 4 * kg)) = u32x2{pk2(a0[0], a0[1]), pk2(a0[2], a0[3])};
        *(LAS u32x2*)(KT + 2 * (kg < 2 ? 256 + 8 * m + 4 * kg : 256 + 64 * kg + 4 * m)) = u32x2{pk2(a1[0], a1[1]), pk2(a1[2], a1[3])};
        if (kg < 2) {
            *(LAS u32*)(KT + 2 * (512 + 64 * kg + 2 * m)) = pk2(a2[0], a2[1]);
            *(LAS u32*)(KT + 2 * (544 + 64 * kg + 2 * m)) = pk2(a2[2], a2[3]);
        }
        if (lane == 32) *(LAS u32*)(KT + 2 * 640) = pk2(a2[0], 0.f);
        CFENCE();
    }
#pragma unroll
    for (int it = 0; it < 2; ++it) {
        u32 v[20], d[10], e[9];
#pragma unroll
        for (int j = 0; j < 20; ++j) v[j] = *(LAS u16*)(L + ga[it][j]);
#pragma unroll
        for (int j = 1; j < 10; ++j) d[j] = v[2 * j] | (v[2 * j + 1] << 16);
#pragma unroll
        for (int j = 0; j < 9; ++j) e[j] = v[2 * j + 1] | (v[2 * j + 2] << 16);
        lptr W = C + CH_KC + 32 + (it * 64 + lane) * 32;
        *(LAS u32x4*)(W) = u32x4{d[2], d[3], d[4], d[5]};
        *(LAS u32x4*)(W + 16) = u32x4{d[6], d[7], d[8], d[9]};
        *(LAS u32x4*)(W + CH_KSTRIDE) = u32x4{e[1], e[2], e[3], e[4]};
        *(LAS u32x4*)(W + CH_KSTRIDE + 16) = u32x4{e[5], e[6], e[7], e[8]};
        *(LAS u32x4*)(W + 2 * CH_KSTRIDE) = u32x4{d[1], d[2], d[3], d[4]};
        *(LAS u32x4*)(W + 2 * CH_KSTRIDE + 16) = u32x4{d[5], d[6], d[7], d[8]};
        *(LAS u32x4*)(W + 3 * CH_KSTRIDE) = u32x4{e[0], e[1], e[2], e[3]};
        *(LAS u32x4*)(W + 3 * CH_KSTRIDE + 16) = u32x4{e[4], e[5], e[6], e[7]};
    }
    if (lane < 8) *(LAS u32x4*)(C + CH_KC + (lane >> 1) * CH_KSTRIDE + (lane & 1) * 16) = u32x4{0, 0, 0, 0};
    *(LAS u32x2*)(C + CH_XR + 4096 + lane * 8) = u32x2{0, 0};
    SCHED();
    __builtin_amdgcn_s_setprio(CP_);
    STAMP(10);
    poll_rows<6>(g, P.rs, lane * 16, xs, tag);
    lptr XR = L + L_CH + CH_XR + cl * 1280 + lane * 4;
#pragma unroll
    for (int i = 0; i < 6; ++i)
#pragma unroll
        for (int c = 0; c < 3; ++c) *(LAS u32*)(XR + c * CH_BYTES + i * 256) = g[i][c];
    return tag;
}

template <int MASK, int NA>
DEV void conv_blocks(rsrc_t rs, lptr C, int ch, int lane, u32 tag) {
    const int m = lane & 15, kg = lane >> 4;
    f32x4 acc[8];
#pragma unroll
    for (int i = 0; i < 8; ++i) acc[i] = f32x4{0, 0, 0, 0};
    lptr KA = C + CH_KC + ((~m) & 3) * CH_KSTRIDE + 2 * (8 * kg + ((m + 4) & ~3));
    lptr XB = C + CH_XR + 2 * (240 - 16 * m + 8 * kg);
    u32x4 A[2][NA], B[2][NA];
    auto rd = [&](int j, int a) {
        A[j & 1][a] = cat(*(volatile LAS u32x2*)(KA + 64 * j + 512 * a), *(volatile LAS u32x2*)(KA + 64 * j + 512 * a + 8));
        B[j & 1][a] = *(LAS u32x4*)(XB + 64 * j + 512 * (7 - a));
    };
#pragma unroll
    for (int a = 0; a < NA; ++a) rd(0, a);
    SCHED();
#pragma unroll
    for (int j = 0; j < 8; ++j) {
        int n = 0;
#pragma unroll
        for (int I = 0; I < 8; ++I)
            if (MASK >> I & 1)
#pragma unroll
                for (int a = 0; a <= I; ++a) {
                    acc[I] = mfma32(A[j & 1][a], B[j & 1][I - a], acc[I]);
                    if (j < 7 && !(n & 1) && (n >> 1) < NA) rd(j + 1, n >> 1);
                    ++n;
                    SCHED();
                }
    }
    const u32 vo = (m >> 1) * 73728 + kg * 32 + (m & 1) * 16;
#pragma unroll
    for (int I = 0; I < 8; ++I)
        if (MASK >> I & 1) st16(rs, seal(pk2(acc[I][0], acc[I][1]), pk2(acc[I][2], acc[I][3]), 0u, tag), vo, WS_BT + I * 8 * 73728 + ch * 128);
}

DEV void gate_role(const Params& P, rsrc_t rs, int lane, int rb, int ps, u32 tag) {
    const GAS char* gw = (const GAS char*)P.gw + (lane & 3) * 16;
    {
        const int row = rb * 32 + ps * 16 + (lane >> 2);
        const GAS char* xr = (const GAS char*)P.x + row * 1152 + (lane & 3) * 16;
        u32x4 xv[18], gv[18];
#pragma unroll
        for (int i = 0; i < 18; ++i) { xv[i] = *(const GAS u32x4*)(xr + i * 64); gv[i] = *(const GAS u32x4*)(gw + i * 64); }
        float s0 = 0.f, s1 = 0.f;
#pragma unroll
        for (int i = 0; i < 18; ++i)
#pragma unroll
            for (int j = 0; j < 4; ++j) {
                s0 = fmaf(lo2f(xv[i][j]), lo2f(gv[i][j]), s0);
                s1 = fmaf(hi2f(xv[i][j]), hi2f(gv[i][j]), s1);
            }
        float s = s0 + s1;
        s += __shfl_xor(s, 1);
        s += __shfl_xor(s, 2);
        const u32 g = __float_as_uint(1.0f / (1.0f + __expf(-s)));
        if ((lane & 3) == 0) __builtin_amdgcn_raw_buffer_store_b64(u32x2{g, g ^ tag}, rs, WS_G + row * 8, 0, AUX_SC1);
    }
}

DEV u32 wf_role(Params& P, lptr L, u32 lbase, int cl, int lane, int unit) {
    const int ks = unit / 9, gp = unit - ks * 9;
    P.arrive<3>();
    const rsrc_t rs = P.rs;
    if (unit < 162) {
        const char* src = (const char*)P.wo + ks * 32 * 1152 + gp * 128;
#pragma unroll
        for (int i = 0; i < 4; ++i) glds16((u32)((lane >> 3) * 1152 + (lane & 7) * 16), src + i * 8 * 1152, lbase + L_CH + cl * CH_BYTES + i * 1040);
    }
    if (unit >= 162) return mk_tag(get_epoch(L));
    WAIT_V(0);
    lptr S = L + L_CH + cl * CH_BYTES + (lane >> 4) * 1040 + ((lane >> 2) & 3) * 128;
#pragma unroll
    for (int h = 0; h < 2; ++h) {
        const int grp = 2 * gp + h;
        const bool sp = grp == 4 || grp == 13;
        lptr p = S + h * 64 + (lane & 3) * (sp ? 8 : 16);
#pragma unroll
        for (int t = 0; t < 2; ++t) {
            lptr q = p + t * (sp ? 32 : 8);
            st16(rs, cat(trread(q), trread(q + 512)), lane * 16, WS_WF + ((grp * 18 + ks) * 2 + t) * 1024);
        }
    }
    SCHED();
    const u32 tag = mk_tag(get_epoch(L));
    WAIT_V(0);
    if (lane == 0) __builtin_amdgcn_raw_buffer_store_b32(tag, rs, WS_WFF + unit * 4, 0, AUX_SC1);
    return tag;
}

__global__ __launch_bounds__(512, 2) void fused(Args A) {
    Params P;
    extern __shared__ __attribute__((aligned(64))) char smem[];
    lptr L = (lptr)smem;
    const u32 lbase = (u32)(uintptr_t)L;
    const int tid = threadIdx.x, lane = tid & 63, w = __builtin_amdgcn_readfirstlane(tid >> 6);
    if (w == 5) {
        *(volatile LAS u32*)(L + L_FLAG + (lane & 15) * 4) = (lane & 15) == 7;
        WAIT_L0();
    }
    P.fetch();
    asm volatile("s_barrier" ::: "memory");
    const int bid = blockIdx.x, xcd = bid & 7, slot = bid >> 3;
    const int rt = (xcd >> 1) * 4 + (slot & 3), hp = (xcd & 1) * 8 + ((((xcd & 1) ? 0x60754321u : 0x71654320u) >> (slot & 28)) & 7);
    const int rb = 8 * xcd + (slot & 7), cq = slot >> 3, cg = w & 3;
    if (w < 4) {
        gemm_role(P, L, lbase, w, lane, rt, hp, rb, cq);
        if (bid == 0 && tid == 0) {
            const u32 ep = *(volatile LAS u32*)(L + L_FLAG + 32);
            __builtin_amdgcn_raw_buffer_store_b32(ep + 1, P.rs, WS_EPOCH, 0, AUX_SC1);
        }
    } else if (w == 7) {
        const u32 tag = loader_role(P, L, lbase, lane, rt, hp);
        if (slot < 24) {
            const int sg = xcd * 24 + slot;
            quad_wait(L + L_FLAG + 16, 1);
            STAMP(3);
#pragma nounroll
            for (int c = 0; c < 3; ++c) {
                STAMP(4);
                conv_blocks<0x0E, 4>(P.rs, L + L_CH + c * CH_BYTES, 3 * sg + c, lane, tag);
            }
            STAMP(5);
        }
    } else if (slot < 24) {
        const int sg = xcd * 24 + slot;
        const u32 tag = kgen_role(P, L, cg, sg, lane);
        flag_set(L, w, 1);
        STAMP(3);
        quad_wait(L + L_FLAG + 16, 1);
        STAMP(4);
        conv_blocks<0xF1, 8>(P.rs, L + L_CH + cg * CH_BYTES, 3 * sg + cg, lane, tag);
        STAMP(5);
    } else {
        const u32 tag = wf_role(P, L, lbase, cg, lane, ((slot - 24) * 8 + xcd) * 3 + cg);
        STAMP(3);
        if (cg < 2) {
            flag_wait1(L, 10, 12);
            gate_role(P, P.rs, lane, rb, cg, tag);
        }
        STAMP(4);
    }
}

extern "C" void kernel_launch(void* const* d_in, const int* in_sizes, int n_in, void* d_out, int out_size, void* d_ws,
                              size_t ws_size, hipStream_t stream) {
    Args P;
    P.x = (const u16*)d_in[0];
    P.wqkv = (const u16*)d_in[1];
    P.wo = (const u16*)d_in[2];
    P.Are = (const u16*)d_in[4];
    P.Aim = (const u16*)d_in[5];
    P.Bre = (const u16*)d_in[6];
    P.Bim = (const u16*)d_in[7];
    P.Cre = (const u16*)d_in[8];
    P.Cim = (const u16*)d_in[9];
    P.logdt = (const u16*)d_in[10];
    P.Dskip = (const u16*)d_in[11];
    P.gw = (const u16*)d_in[12];
    P.ws = (char*)d_ws;
    P.out = (u16*)d_out;
    (void)hipFuncSetAttribute((const void*)fused, hipFuncAttributeMaxDynamicSharedMemorySize, LDS_BYTES);
    fused<<<256, 512, LDS_BYTES, stream>>>(P);
}
```

```cpp
#include <hip/hip_runtime.h>
#include <hip/hip_bf16.h>
#include <stdint.h>
#include <utility>

typedef unsigned short u16;
typedef unsigned int u32;
typedef unsigned long long u64;
typedef u32 u32x4 __attribute__((ext_vector_type(4)));
typedef u32 u32x2 __attribute__((ext_vector_type(2)));
typedef u32 u32x32 __attribute__((ext_vector_type(32)));
typedef float f32x4 __attribute__((ext_vector_type(4)));
typedef float f32x2 __attribute__((ext_vector_type(2)));
typedef __bf16 bf16x8 __attribute__((ext_vector_type(8)));
typedef __bf16 bf16x2 __attribute__((ext_vector_type(2)));
typedef short s16x4 __attribute__((ext_vector_type(4)));
#define LAS __attribute__((address_space(3)))
#define GAS __attribute__((address_space(1)))
#define DEV __device__ __forceinline__
typedef LAS char* lptr;
typedef __amdgpu_buffer_rsrc_t rsrc_t;

constexpr u32 WS_EPOCH = 0;
constexpr u32 WS_WFF = 256;
constexpr u32 WS_G = 4096;
constexpr u32 WS_KVG = 65536;
constexpr u32 WS_WF = WS_KVG + 524288;
constexpr u32 WS_XT = WS_WF + 663552;
constexpr u32 WS_OG = WS_XT + 3145728;
constexpr u32 WS_BT = WS_OG + 3145728;
constexpr u32 WS_END = WS_BT + 4718592;
constexpr int AUX_SC1 = 16;
constexpr int T_OG = 126;
constexpr int T_KV = 92;
constexpr int T_KVMAX = 1000;
constexpr int BT_STEP = -5;
constexpr int WF_A = 20;
constexpr int WF_B = 27;
constexpr int CONV_EARLY = 4;
constexpr u32 SPIN_MAX = 1u << 17;

constexpr u32 L_FLAG = 0;
constexpr u32 CH_KC = 0, CH_KSTRIDE = 4160, CH_XR = 16640, CH_BYTES = 21248;
constexpr u32 L_CH = 64;
constexpr u32 KT_FAR = 1028, KT_BYTES = 1344;
constexpr u32 L_KT = L_CH + 3 * CH_BYTES;
constexpr u32 L_XRING = L_KT + 3 * KT_BYTES;
constexpr u32 L_WRING = L_XRING + 49152;
constexpr u32 L_AIMG = L_WRING + 40960;
constexpr u32 LDS_BYTES = L_AIMG + 5632;
constexpr u32 L_KVX = L_WRING + 10240;
constexpr u32 TQ = 944;
static_assert(LDS_BYTES <= 163840, "lds");
static_assert(L_XRING % 32 == 0, "align");

DEV float bf2f(u16 v) { return __uint_as_float(((u32)v) << 16); }
DEV u32 pk2(float a, float b) {
    f32x2 v = {a, b};
    return __builtin_bit_cast(u32, __builtin_convertvector(v, bf16x2));
}
DEV u32 pk2s(float a, float b) {
    u32 r;
    asm("v_cvt_pk_bf16_f32 %0, %1, %2" : "=v"(r) : "v"(a), "v"(b));
    return r;
}
DEV float lo2f(u32 v) { return __uint_as_float(v << 16); }
DEV float hi2f(u32 v) { return __uint_as_float(v & 0xffff0000u); }
DEV f32x4 mfma32(u32x4 a, u32x4 b, f32x4 c) {
    return __builtin_amdgcn_mfma_f32_16x16x32_bf16(__builtin_bit_cast(bf16x8, a), __builtin_bit_cast(bf16x8, b), c, 0, 0, 0);
}
DEV void glds16(u32 voff, const void* sbase, u32 ldsaddr) {
    asm volatile("s_mov_b32 m0, %2\n\ts_nop 0\n\tglobal_load_lds_dwordx4 %0, %1" ::"v"(voff), "s"(sbase), "s"(ldsaddr) : "memory");
}
DEV void glds16x4(u32 v0, u32 v1, u32 v2, u32 v3, const void* sbase, u32 ldsaddr) {
    asm volatile("s_mov_b32 m0, %5\n\ts_nop 0\n\tglobal_load_lds_dwordx4 %0, %4\n\tglobal_load_lds_dwordx4 %1, %4 offset:1024\n\t"
                 "global_load_lds_dwordx4 %2, %4 offset:2048\n\tglobal_load_lds_dwordx4 %3, %4 offset:3072" ::"v"(v0), "v"(v1), "v"(v2), "v"(v3),
                 "s"(sbase), "s"(ldsaddr) : "memory");
}
DEV void glds_word(u32 voff, const void* sbase, u32 ldsaddr) {
    asm volatile("s_mov_b32 m0, %2\n\ts_mov_b64 exec, 1\n\tglobal_load_lds_dword %0, %1\n\ts_mov_b64 exec, -1" ::"v"(voff), "s"(sbase), "s"(ldsaddr) : "memory");
}
DEV u32x2 trread(lptr p) { return __builtin_bit_cast(u32x2, __builtin_amdgcn_ds_read_tr16_b64_v4i16((LAS s16x4*)p)); }
DEV u32x4 cat(u32x2 a, u32x2 b) { return u32x4{a[0], a[1], b[0], b[1]}; }
DEV u32x4 ld16(rsrc_t rs, u32 voff, u32 soff) { return __builtin_amdgcn_raw_buffer_load_b128(rs, voff, soff, AUX_SC1); }
DEV void st16(rsrc_t rs, u32x4 v, u32 voff, u32 soff) { __builtin_amdgcn_raw_buffer_store_b128(v, rs, voff + soff, 0, AUX_SC1); }
DEV u32 xor3(u32 a, u32 b, u32 c) { return __builtin_amdgcn_bitop3_b32(a, b, c, 0x96); }
DEV u32x4 seal(u32 a, u32 b, u32 c, u32 tag) { return u32x4{a, b, c, xor3(a, b, tag) ^ c}; }
DEV u32 unseal(u32x4 g, u32 tag) { return xor3(xor3(g[0], g[1], tag), g[2], g[3]); }
#define WAIT_V(n) asm volatile("s_waitcnt vmcnt(%0)" ::"n"(n) : "memory")
#define WAIT_L(n) asm volatile("s_waitcnt lgkmcnt(%0)" ::"n"(n) : "memory")
#define WAIT_L0() asm volatile("s_waitcnt lgkmcnt(0)" ::: "memory")
#define RAW_BARRIER() asm volatile("s_waitcnt lgkmcnt(0)\n\ts_barrier" ::: "memory")
#define CFENCE() asm volatile("" ::: "memory")
#define SCHED() __builtin_amdgcn_sched_barrier(0)

DEV void flag_set(lptr L, int wave, u32 v) {
    WAIT_L0();
    *(volatile LAS u32*)(L + L_FLAG + wave * 4) = v;
}
DEV void flag_wait1(lptr L, int wave, u32 v) {
#pragma nounroll
    for (u32 it = 0; it < SPIN_MAX * 8; ++it) {
        u32 f = *(volatile LAS u32*)(L + L_FLAG + wave * 4);
        if (__builtin_amdgcn_readfirstlane(f) >= v) break;
        __builtin_amdgcn_s_sleep(1);
    }
    CFENCE();
}
DEV bool quad_ok(u32x4 f, u32 v) {
    u32 m = min(min(f[0], f[1]), min(f[2], f[3]));
    return __builtin_amdgcn_readfirstlane(m) >= v;
}
DEV void quad_wait(lptr Q, u32 v) {
#pragma nounroll
    for (u32 it = 0; it < SPIN_MAX * 8; ++it) {
        u32x4 f = *(volatile LAS u32x4*)(Q);
        if (quad_ok(f, v)) break;
        __builtin_amdgcn_s_sleep(1);
    }
    CFENCE();
}
DEV void quad_spin(lptr Q, u32 v) {
#pragma nounroll
    for (u32 it = 0; it < SPIN_MAX * 16; ++it) {
        u32x4 f = *(volatile LAS u32x4*)(Q);
        if (quad_ok(f, v)) break;
    }
    CFENCE();
}
DEV u32 now() {
    u64 t;
    asm volatile("s_memrealtime %0\n\ts_waitcnt lgkmcnt(0)" : "=s"(t)::"memory");
    return (u32)t;
}
DEV u32 mk_tag(u32 epoch) { return epoch * 0x9E3779B1u + 0x7F4A7C15u; }
DEV u32 get_epoch(lptr L) {
    u32x2 v;
#pragma nounroll
    for (u32 it = 0; it < SPIN_MAX * 8; ++it) {
        v = *(volatile LAS u32x2*)(L + L_FLAG + 32);
        if (__builtin_amdgcn_readfirstlane(v[1])) break;
        __builtin_amdgcn_s_sleep(1);
    }
    CFENCE();
    return __builtin_amdgcn_readfirstlane(v[0]);
}
#define RL(i, so, off) "s_bitcmp1_b32 %[m], 1+" #i "\n\ts_cbranch_scc0 1f\n\tbuffer_load_dwordx4 %" #i ", %[v], %[r], %[" #so "] offen offset:" #off " sc1\n1:\n\t"
#define RL4(i0, i1, i2, i3, so) RL(i0, so, 0) RL(i1, so, 1024) RL(i2, so, 2048) RL(i3, so, 3072)
#define RL_IN [m] "s"(mask), [v] "v"(voff), [r] "s"(rs), [a] "s"(soff), [b] "s"(soff + 4096), [c] "s"(soff + 8192)
#define RL_END "s_waitcnt vmcnt(0)"
#define G4(i) "+v"(g[i]), "+v"(g[i + 1]), "+v"(g[i + 2]), "+v"(g[i + 3])
template <int N>
DEV void reload_rows(u32x4 (&g)[N], u32 mask, rsrc_t rs, u32 voff, u32 soff) {
    if constexpr (N == 1) asm volatile(RL(0, a, 0) RL_END : "+v"(g[0]) : RL_IN : "scc", "memory");
    if constexpr (N == 4) asm volatile(RL4(0, 1, 2, 3, a) RL_END : G4(0) : RL_IN : "scc", "memory");
    if constexpr (N == 6) asm volatile(RL4(0, 1, 2, 3, a) RL(4, b, 0) RL(5, b, 1024) RL_END : G4(0), "+v"(g[4]), "+v"(g[5]) : RL_IN : "scc", "memory");
    if constexpr (N == 8) asm volatile(RL4(0, 1, 2, 3, a) RL4(4, 5, 6, 7, b) RL_END : G4(0), G4(4) : RL_IN : "scc", "memory");
    if constexpr (N == 12) asm volatile(RL4(0, 1, 2, 3, a) RL4(4, 5, 6, 7, b) RL4(8, 9, 10, 11, c) RL_END : G4(0), G4(4), G4(8) : RL_IN : "scc", "memory");
}
template <int N, int... I>
DEV void poll_seq(u32x4 (&g)[N], rsrc_t rs, u32 voff, u32 soff, u32 tag, std::integer_sequence<int, I...>) {
    u32x4 r[N] = {g[I]...};
    if (__builtin_expect(__any((unseal(r[I], tag) | ...) != 0), 0)) {
        u32 left = SPIN_MAX;
#pragma nounroll
        do {
            const u32 mask = ((__builtin_amdgcn_ballot_w64(unseal(r[I], tag) != 0) ? 2u << I : 0u) | ...);
            reload_rows(r, mask, rs, voff, soff);
            left = mask ? left - 1 : 0;
        } while (left);
    }
    ((g[I] = r[I]), ...);
    CFENCE();
}
template <int N>
DEV void poll_rows(u32x4 (&g)[N], rsrc_t rs, u32 voff, u32 soff, u32 tag) { poll_seq(g, rs, voff, soff, tag, std::make_integer_sequence<int, N>{}); }
#define PIN(x) asm volatile("" : "+v"(x))
#define PINS(x) asm volatile("" : "+s"(x))

struct Args {
    const u16 *x, *wqkv, *wo, *Are, *Aim, *Bre, *Bim, *Cre, *Cim, *logdt, *Dskip, *gw;
    char* ws;
    u16* out;
};
typedef u64 u64x8 __attribute__((ext_vector_type(8)));
typedef u64 u64x4 __attribute__((ext_vector_type(4)));
typedef u64 u64x2 __attribute__((ext_vector_type(2)));
struct Params : Args {
    u64x8 k0; u64x4 k1; u64x2 k2; u64 k3;
    rsrc_t rs;
    u32 t0;
    DEV void fetch() {
        asm volatile("s_load_dwordx16 %0, %4, 0x0\n\ts_load_dwordx8 %1, %4, 0x40\n\ts_load_dwordx4 %2, %4, 0x60\n\ts_memrealtime %3"
                     : "=&s"(k0), "=&s"(k1), "=&s"(k2), "=&s"(k3) : "s"(__builtin_amdgcn_kernarg_segment_ptr()));
    }
    DEV static u64 take(u64 v) {
        u64 r;
        asm volatile("s_mov_b64 %0, %1" : "=s"(r) : "s"(v));
        return r;
    }
    template <int ROLE>
    DEV void arrive() {
        asm volatile("s_waitcnt lgkmcnt(0)" ::"s"(k0), "s"(k1), "s"(k2), "s"(k3));
        if (ROLE == 1) t0 = (u32)take(k3);
        if (ROLE != 2) x = (const u16*)take(k0[0]);
        if (ROLE < 2) wqkv = (const u16*)take(k0[1]);
        if (ROLE == 3) { wo = (const u16*)take(k0[2]); gw = (const u16*)take(k1[3]); }
        if (ROLE == 2) {
            Are = (const u16*)take(k0[3]); Aim = (const u16*)take(k0[4]); Bre = (const u16*)take(k0[5]); Bim = (const u16*)take(k0[6]);
            Cre = (const u16*)take(k0[7]); Cim = (const u16*)take(k1[0]); logdt = (const u16*)take(k1[1]); Dskip = (const u16*)take(k1[2]);
        }
        ws = (char*)take(k2[0]);
        if (ROLE == 0) out = (u16*)take(k2[1]);
        rs = __builtin_amdgcn_make_buffer_rsrc((void*)ws, 0, (int)WS_END, 0x00020000);
    }
};
DEV void pos5(u32 idx, u32& pos, u32& piece) {
    pos = (idx * 52429u) >> 18;
    piece = idx - 5 * pos;
}
DEV u32 pos2row(u32 pos) { return (pos & 56) | ((pos >> 1) & 3) | ((pos & 1) << 2); }

DEV int first_chunk(int hp) { return (9 * hp) >> 4; }
DEV int kv_lead(int hp) { return hp ? 8 : 0; }
DEV int q_lead(int hp) { return hp == 15 ? 8 : 0; }
DEV float add1(float a, float b) { asm("" : "+v"(a)); float r = a + b; asm("" : "+v"(r)); return r; }
DEV f32x4 add4(f32x4 a, f32x4 b) { return f32x4{add1(a[0], b[0]), add1(a[1], b[1]), add1(a[2], b[2]), add1(a[3], b[3])}; }
DEV bool wf_ok(u32x4 f, int lane, u32 tag) { return __all(f[0] == tag && f[1] == tag && (lane >= 40 || (f[2] == tag && f[3] == tag))); }
DEV u32 loader_role(Params& P, lptr L, u32 lbase, int lane, int rt, int hp) {
    u32 wv[5], xv[4];
#pragma unroll
    for (int p = 0; p < 5; ++p) {
        u32 pos, piece;
        pos5(p * 64 + lane, pos, piece);
        wv[p] = pos2row(pos) * 3456 + piece * 16 - (p & 3) * 1024;
    }
#pragma unroll
    for (int j = 0; j < 4; ++j) xv[j] = (u32)((lane >> 3) * 1152 + (((lane & 7) ^ (lane >> 3)) << 4)) + j * 8192;
    const char* seq;
    asm volatile("s_getpc_b64 %0\n\ts_branch 0f\n\t.long 1, 2, 3, 4, 5, 6, 7, 8, 9, 10, 11, 12\n0:" : "=s"(seq));
    u32 nv = 4;
    u64 ord = 0;
    {
        const u64 ks = (((0x87654328765432ull >> (8 * (rt & 3))) & 0xfffffffull) << 8) | 0x10;
        const u32 c0 = first_chunk(hp);
#pragma unroll
        for (int i = 0; i < 9; ++i) {
            const u32 c = c0 + ((u32)(ks >> (4 * i)) & 15);
            ord |= (u64)min(c, c - 9) << (4 * i);
        }
    }
    PINS(ord);
    u32 fo = WS_WFF + min(lane, 40) * 16, vk = (lane >> 4) * 4096 + (lane & 15) * 16 + WS_KVG + hp * 32768 + rt * 256;
    PIN(fo); PIN(vk);
    P.arrive<1>();
    glds_word(WS_EPOCH, P.ws, lbase + L_FLAG + 32);
    glds_word(nv, seq, lbase + L_FLAG + 36);
    const char* xg = (const char*)P.x + rt * 128 * 1152;
    const char* wg = (const char*)P.wqkv + 72 * hp + 1152 - kv_lead(hp);
    const char* qg = (const char*)P.wqkv + 72 * (hp ^ 1) - q_lead(hp ^ 1);
    auto five = [&](const char* src, u32 dst) {
        glds16x4(wv[0], wv[1], wv[2], wv[3], src, dst);
        glds16(wv[4], src, dst + 4096);
    };
    auto full = [&]() { nv += 4; glds_word(nv, seq, lbase + L_FLAG + 40); };
    u32x4 fl = {0, 0, 0, 0};
    auto room = [&](int v) {
        if (v > 0 && !quad_ok(fl, v)) quad_spin(L + L_FLAG, v);
        fl = *(volatile LAS u32x4*)(L + L_FLAG);
    };
    u32 xs = lbase + L_XRING + 16384;
    u64 o = ord;
#pragma nounroll
    for (int j = 1; j < 9; ++j) {
        o >>= 4;
        const u32 c = (u32)o & 15, ws = lbase + L_WRING + (j & 3) * 10240;
        room(j - 3);
        five(wg + c * 221184, ws);
        five(wg + c * 221184 + 1152, ws + 5120);
        if (j == 3) { room(1); }
#pragma unroll
        for (int w = 0; w < 4; ++w) glds16x4(xv[0], xv[1], xv[2], xv[3], xg + c * 128 + w * 36864, xs + w * 4096);
        full();
        __builtin_amdgcn_s_setprio(3);
        xs = xs == lbase + L_XRING + 32768 ? lbase + L_XRING : xs + 16384;
    }
    u32x4 wff;
    asm volatile("buffer_load_dwordx4 %0, %1, %2, 0 offen sc1" : "=v"(wff) : "v"(fo), "s"(P.rs) : "memory");
    const u32 ep = *(volatile LAS u32*)(L + L_FLAG + 32);
    o = ord;
#pragma nounroll
    for (int s = 0; s < 3; ++s) {
        room(6 + s);
        if (s) full();
#pragma unroll
        for (int i = 0; i < 3; ++i) {
            five(qg + ((u32)o & 15) * 221184, lbase + L_XRING + s * 16384 + i * 5120);
            o >>= 4;
        }
    }
    const u32 tag = mk_tag(__builtin_amdgcn_readfirstlane(ep));
    asm volatile("s_waitcnt vmcnt(47)" : "+v"(wff)::"memory");
#pragma nounroll
    for (u32 it = 0; __builtin_expect(!wf_ok(wff, lane, tag), 0) && it < SPIN_MAX; ++it) {
        __builtin_amdgcn_s_sleep(8);
        asm volatile("buffer_load_dwordx4 %0, %1, %2, 0 offen sc1\n\ts_waitcnt vmcnt(0)" : "=v"(wff) : "v"(fo), "s"(P.rs) : "memory");
    }
    full();
    const int m = lane & 15, kg = lane >> 4;
    u32x4 sel[2];
    u32 a0[4], a1[4];
    {
        const u32 one = (m & 1) ? 0x3f800000u : 0x3f80u;
#pragma unroll
        for (int s = 0; s < 2; ++s)
#pragma unroll
            for (int i = 0; i < 4; ++i) sel[s][i] = (i < 3 && (m >> 1) == 4 * s + i) ? one : 0u;
#pragma unroll
        for (int w = 0; w < 4; ++w) {
            const int hd = w >> 1, kgp = 2 * (w & 1) + (kg >> 1), DUMP = 38 * 144;
            const int rowE = (20 * hd + m) * 144, ex0 = (16 + 2 * hd) * 144, den = (36 + hd) * 144;
            const int cd = 16 * (m >> 2) + 8 * hd + 2 * (m & 3), cx = 64 + 4 * hd;
            if (!(w & 1)) {
                a0[w] = kg < 2 ? rowE + cx : ex0 + cd;
                a1[w] = a0[w] + (kg < 2 ? 2 : 144);
            } else {
                a0[w] = kg < 2 ? ((m >> 1) == 2 + hd ? ex0 + (m & 1) * 144 + cx : (m == 12 ? den + cx : DUMP)) : den + cd;
                a1[w] = kg < 2 ? a0[w] + 2 : DUMP;
            }
            if (!(kg & 1)) a0[w] = rowE + 16 * kgp + 8 * hd;
            a0[w] += lbase + L_AIMG; a1[w] += lbase + L_AIMG;
            PIN(a0[w]); PIN(a1[w]);
        }
        PIN(sel[0]); PIN(sel[1]);
    }
    quad_spin(L + L_FLAG, 20);
    {
        lptr K4 = L + L_KVX + lane * 16, K2 = L + L_KVX + 1024 + lane * 8;
        f32x4 r4[2][4];
        f32x2 r2[2][4];
#pragma unroll
        for (int h = 0; h < 2; ++h)
#pragma unroll
            for (int w = 0; w < 4; ++w) { r4[h][w] = *(LAS f32x4*)(K4 + w * 3072 + h * 1536); r2[h][w] = *(LAS f32x2*)(K2 + w * 3072 + h * 1536); }
        SCHED();
#pragma unroll
        for (int h = 0; h < 2; ++h) {
            const f32x4 s4 = add4(add4(r4[h][0], r4[h][1]), add4(r4[h][2], r4[h][3]));
            float s2[2];
#pragma unroll
            for (int i = 0; i < 2; ++i) s2[i] = add1(add1(r2[h][0][i], r2[h][1][i]), add1(r2[h][2][i], r2[h][3][i]));
            st16(P.rs, seal(pk2(s4[0], s4[1]), pk2(s4[2], s4[3]), pk2(s2[0], s2[1]), tag), vk, h * 16384);
        }
    }
    {
        u32x32 r0, r1, r2, r3;
        u64 m0 = ~0ull, m1 = ~0ull, m2 = ~0ull, m3 = ~0ull;
        u32 left = SPIN_MAX, so, t, dn;
        f32x4 d[4] = {};
        const u32 lim = min(now() - P.t0 + T_KV, (u32)T_KVMAX);
#pragma nounroll
        while (now() - P.t0 < lim) __builtin_amdgcn_s_sleep(1);
        asm volatile(
            ".macro kv_ld q, mk\n\ts_mov_b64 exec, \\mk\n\ts_cbranch_execz 1f\n\t.set kv_s, 0\n\t.rept 8\n\t"
            "s_add_u32 %[so], %[base], kv_s * 4096\n\t"
            "buffer_load_dwordx4 v[64 + \\q * 32 + kv_s * 4 : 67 + \\q * 32 + kv_s * 4], %[v], %[r], %[so] offen offset:\\q * 1024 sc1\n\t"
            ".set kv_s, kv_s + 1\n\t.endr\n1:\n\t.endm\n\t"
            ".macro kv_ck q, mk\n\ts_mov_b64 exec, \\mk\n\ts_cbranch_execz 1f\n\t.set kv_s, 64 + \\q * 32\n\t.rept 8\n\t"
            "v_bitop3_b32 %[t], v[kv_s], v[kv_s + 1], %[tag] bitop3:0x96\n\t"
            "v_bitop3_b32 v[kv_s + 3], %[t], v[kv_s + 2], v[kv_s + 3] bitop3:0x96\n\t"
            ".set kv_s, kv_s + 4\n\t.endr\n\t.set kv_s, 67 + \\q * 32\n\t"
            "v_or3_b32 %[t], v[kv_s], v[kv_s + 4], v[kv_s + 8]\n\tv_or3_b32 %[t], %[t], v[kv_s + 12], v[kv_s + 16]\n\t"
            "v_or3_b32 %[t], %[t], v[kv_s + 20], v[kv_s + 24]\n\tv_or_b32 %[t], %[t], v[kv_s + 28]\n\t"
            "v_cmp_ne_u32_e64 \\mk, 0, %[t]\n1:\n\t.endm\n\t"
            ".macro kv_mm q\n\t"
            "v_mfma_f32_16x16x32_bf16 %[d0], %[s0], v[64 + \\q * 32 : 67 + \\q * 32], %[d0]\n\t"
            "v_mfma_f32_16x16x32_bf16 %[d1], %[s0], v[72 + \\q * 32 : 75 + \\q * 32], %[d1]\n\t"
            "v_mfma_f32_16x16x32_bf16 %[d2], %[s0], v[80 + \\q * 32 : 83 + \\q * 32], %[d2]\n\t"
            "v_mfma_f32_16x16x32_bf16 %[d3], %[s0], v[88 + \\q * 32 : 91 + \\q * 32], %[d3]\n\t"
            "v_mfma_f32_16x16x32_bf16 %[d0], %[s1], v[68 + \\q * 32 : 71 + \\q * 32], %[d0]\n\t"
            "v_mfma_f32_16x16x32_bf16 %[d1], %[s1], v[76 + \\q * 32 : 79 + \\q * 32], %[d1]\n\t"
            "v_mfma_f32_16x16x32_bf16 %[d2], %[s1], v[84 + \\q * 32 : 87 + \\q * 32], %[d2]\n\t"
            "v_mfma_f32_16x16x32_bf16 %[d3], %[s1], v[92 + \\q * 32 : 95 + \\q * 32], %[d3]\n\t.endm\n\t"
            ".macro kv_go q, mk\n\ts_waitcnt vmcnt(24 - 8 * \\q)\n\tkv_ck \\q, \\mk\n\ts_or_b64 vcc, vcc, \\mk\n\ts_cbranch_scc1 1f\n\t"
            "kv_mm \\q\n\ts_mov_b32 %[dn], \\q + 1\n1:\n\t.endm\n\t"
            ".macro kv_fin q\n\ts_cmp_gt_u32 %[dn], \\q\n\ts_cbranch_scc1 1f\n\tkv_mm \\q\n1:\n\t.endm\n\t"
            "kv_ld 0, %[m0]\n\tkv_ld 1, %[m1]\n\tkv_ld 2, %[m2]\n\tkv_ld 3, %[m3]\n\t"
            "s_mov_b64 vcc, 0\n\ts_mov_b32 %[dn], 0\n\t"
            "kv_go 0, %[m0]\n\tkv_go 1, %[m1]\n\tkv_go 2, %[m2]\n\tkv_go 3, %[m3]\n\t"
            "s_cmp_eq_u32 %[dn], 4\n\ts_cbranch_scc1 4f\n"
            "2:\n\tkv_ld 0, %[m0]\n\tkv_ld 1, %[m1]\n\tkv_ld 2, %[m2]\n\tkv_ld 3, %[m3]\n\t"
            "s_waitcnt vmcnt(0)\n\t"
            "kv_ck 0, %[m0]\n\tkv_ck 1, %[m1]\n\tkv_ck 2, %[m2]\n\tkv_ck 3, %[m3]\n\t"
            "s_sub_u32 %[n], %[n], 1\n\ts_cbranch_scc1 3f\n\t"
            "s_or_b64 vcc, %[m0], %[m1]\n\ts_or_b64 vcc, vcc, %[m2]\n\ts_or_b64 vcc, vcc, %[m3]\n\ts_cbranch_scc1 2b\n"
            "3:\n\ts_mov_b64 exec, -1\n\tkv_fin 0\n\tkv_fin 1\n\tkv_fin 2\n\tkv_fin 3\n"
            "4:\n\ts_nop 7\n\ts_nop 7\n\t.purgem kv_ld\n\t.purgem kv_ck\n\t.purgem kv_mm\n\t.purgem kv_go\n\t.purgem kv_fin"
            : "=&{v[64:95]}"(r0), "=&{v[96:127]}"(r1), "=&{v[128:159]}"(r2), "=&{v[160:191]}"(r3), [m0] "+s"(m0), [m1] "+s"(m1), [m2] "+s"(m2),
              [m3] "+s"(m3), [n] "+s"(left), [so] "=&s"(so), [t] "=&v"(t), [dn] "=&s"(dn), [d0] "+v"(d[0]), [d1] "+v"(d[1]), [d2] "+v"(d[2]),
              [d3] "+v"(d[3])
            : [v] "v"(lane * 16), [r] "s"(P.rs), [base] "s"(WS_KVG + (hp ^ 1) * 32768), [tag] "s"(tag), [s0] "v"(sel[0]), [s1] "v"(sel[1])
            : "vcc", "scc", "memory");
        if (!(kg & 1)) {
#pragma unroll
            for (int w = 0; w < 4; ++w) *(LAS u32x2*)(uintptr_t)a0[w] = u32x2{pk2(d[w][0], d[w][1]), pk2(d[w][2], d[w][3])};
        } else {
#pragma unroll
            for (int w = 0; w < 4; ++w) {
                const u32 p01 = pk2(d[w][0], d[w][1]);
                *(LAS u16*)(uintptr_t)a0[w] = (u16)p01;
                *(LAS u16*)(uintptr_t)a1[w] = (u16)(p01 >> 16);
            }
        }
        flag_set(L, 11, (now() + T_OG) | 1);
    }
    __builtin_amdgcn_s_setprio(0);
    return tag;
}

constexpr float LOG2E = 1.44269504088896340736f;
constexpr float EPS = 1e-6f;
constexpr float ONE_EPS = 1.0f + 1e-6f;
DEV float phi(float a) {
    float e = __builtin_amdgcn_exp2f(fmaf(a, LOG2E, -ONE_EPS * LOG2E));
    return __builtin_amdgcn_fmed3f(e + EPS, a, ONE_EPS);
}

DEV void gemm_role(Params& P, lptr L, u32 lbase, int w, int lane, int rt, int hp, int rb, int cq) {
    const int m = lane & 15, kg = lane >> 4;
    {
        const u32 xo = (u32)((lane >> 3) * 1152 + (((lane & 7) ^ (lane >> 3)) << 4));
        u32 wv[3];
#pragma unroll
        for (int j = 0; j < 3; ++j) {
            u32 pos, piece;
            pos5((w + 4 * j) * 64 + lane, pos, piece);
            wv[j] = pos2row(pos) * 3456 + (pos >> 6) * 1152 + piece * 16;
        }
        const int c0 = first_chunk(hp);
        P.arrive<0>();
        glds16x4(xo, xo + 8192, xo + 16384, xo + 24576, (const char*)P.x + (rt * 128 + w * 32) * 1152 + c0 * 128, lbase + L_XRING + w * 4096);
        const char* wsrc = (const char*)P.wqkv + 72 * hp + c0 * 221184 + 1152 - kv_lead(hp);
        glds16(wv[0], wsrc, lbase + L_WRING + w * 1024);
        glds16(wv[1], wsrc, lbase + L_WRING + w * 1024 + 4096);
        if (w < 2) glds16(wv[2], wsrc, lbase + L_WRING + w * 1024 + 8192);
    }
    const rsrc_t rs = P.rs;
    __builtin_amdgcn_s_setprio(2);
    *(LAS u32x4*)(L + L_AIMG + w * 1024 + lane * 16) = u32x4{0, 0, 0, 0};
    if (lane < 48) *(LAS u32x2*)(L + L_AIMG + 4096 + w * 384 + lane * 8) = u32x2{0, 0};
    f32x4 acc[2][5];
    const bool kl = !(m & 4);
#pragma unroll
    for (int ct = 0; ct < 5; ++ct) {
        float iv = ct < 2 ? ONE_EPS : (ct < 4 ? 0.f : (kl ? ONE_EPS : 0.f));
        acc[0][ct] = f32x4{iv, iv, iv, iv};
        acc[1][ct] = acc[0][ct];
    }
    lptr XA0 = L + L_XRING + w * 4096 + m * 128 + ((kg ^ (m & 7)) << 4);
    lptr XA1 = L + L_XRING + w * 4096 + m * 128 + (((4 + kg) ^ (m & 7)) << 4);
    const int ra = (8 * kg + 2 * ((lane >> 2) & 3)) * 80;
    lptr WA = L + L_WRING + ra + (lane & 3) * 8 + kv_lead(hp);
    lptr WL = L + L_WRING + ra + (lane & 1) * 5120 + 32 + kv_lead(hp);
    volatile LAS u32* FULL = (volatile LAS u32*)(L + L_FLAG + 40);
    volatile LAS u32* FREE = (volatile LAS u32*)(L + L_FLAG + w * 4);
    u32x4 X[18][2];
    u32x2 B[2][5][2];
    auto rdx = [&](int i, int e) {
        X[2 * i + (e >> 1)][e & 1] = *(LAS u32x4*)(((e >> 1) ? XA1 : XA0) + (i % 3) * 16384 + (e & 1) * 2048);
    };
    auto rdb = [&](int i, int ks, int q) {
        const int o = (i & 3) * 10240 + ks * 2560;
        lptr p = q == 2 ? WL + o : WA + o + (q >= 3 ? 5120 : 0) + ((q == 1 || q == 4) ? 40 : 0);
        B[ks][q][0] = trread(p);
        B[ks][q][1] = trread(p + 80);
    };
    auto mm = [&](int k, int s) {
        constexpr int ord[5] = {0, 1, 4, 2, 3};
        const int q = s >> 1, r = s & 1;
        acc[r][ord[q]] = mfma32(X[k][r], cat(B[k & 1][q][0], B[k & 1][q][1]), acc[r][ord[q]]);
    };
    lptr XTA[3];
    u32 xts = WS_XT + (hp * 12 + min(kg, 2) * 4) * 16384 + (1008 - 64 * rt - 16 * w + m) * 16;
#pragma unroll
    for (int e = 0; e < 3; ++e) {
        const int B = ((72 * hp) & 127) + 24 * min(kg, 2) + 8 * e, r = 30 - 2 * m;
        XTA[e] = L + L_XRING + (B >> 7) * 16384 + w * 4096 + r * 128 + ((((B >> 4) ^ r) & 7) << 4) + (B & 8);
        PIN(XTA[e]);
    }
    PIN(xts);
    u32x2 ev[3], od[3];
    u32 tag = 0, f = 0;
    u32x4 fq;
    WAIT_V(0);
    flag_set(L, 12 + w, 1);
    quad_wait(L + L_FLAG + 48, 1);
#pragma unroll
    for (int e = 0; e < 4; ++e) rdx(0, e);
#pragma unroll
    for (int q = 0; q < 5; ++q) rdb(0, 0, q);
    SCHED();
#pragma unroll
    for (int i = 0; i < 9; ++i) {
#pragma unroll
        for (int s = 0; s < 10; ++s) {
            mm(2 * i, s);
            if (i == 1 && s >= 5 && s < 9) {
                u32 d[3];
#pragma unroll
                for (int c = 0; c < 3; ++c) {
                    const int q = 3 * (s - 5) + c;
                    d[c] = __builtin_amdgcn_perm(ev[q >> 2][(q >> 1) & 1], od[q >> 2][(q >> 1) & 1], (q & 1) ? 0x07060302u : 0x05040100u);
                }
                st16(rs, seal(d[0], d[1], d[2], tag), xts, (s - 5) * 16384);
            }
            if (s < 5) rdb(i, 1, s);
            if (s == 5 && i < 8) f = *FULL;
            SCHED();
        }
#pragma unroll
        for (int s = 0; s < 10; ++s) {
            mm(2 * i + 1, s);
            if (s == 0 && i < 8) {
                if (__builtin_amdgcn_readfirstlane(f) < (u32)(i + 2)) flag_wait1(L, 10, i + 2);
                CFENCE();
                if (i == 0) {
#pragma unroll
                    for (int e = 0; e < 3; ++e) { ev[e] = *(LAS u32x2*)(XTA[e]); od[e] = *(LAS u32x2*)((lptr)((u32)(uintptr_t)XTA[e] ^ 16) + 128); }
                    tag = mk_tag(__builtin_amdgcn_readfirstlane(*(volatile LAS u32*)(L + L_FLAG + 32)));
                }
            }
            if (i == 8 && s == 8) { fq = *(volatile LAS u32x4*)(L + L_FLAG); f = *FULL; }
            if (i < 8) {
                if (s < 2) { rdx(i + 1, 2 * s); rdx(i + 1, 2 * s + 1); }
                if (s >= 2 && s < 7) rdb(i + 1, 0, s - 2);
                if (s == 4) { WAIT_L(6); *FREE = i + 1; }
            }
            SCHED();
        }
    }
    {
        u32x4 op[5];
        const float ca = kl ? EPS : __builtin_inff(), cb = kl ? ONE_EPS : -__builtin_inff();
#pragma unroll
        for (int ct = 0; ct < 5; ++ct) {
            f32x4 v[2];
#pragma unroll
            for (int r = 0; r < 2; ++r)
#pragma unroll
                for (int i = 0; i < 4; ++i) {
                    float a = acc[r][ct][i];
                    if (ct < 2) a = phi(a);
                    if (ct == 4) a = __builtin_amdgcn_fmed3f(__builtin_amdgcn_exp2f(fmaf(a, LOG2E, -ONE_EPS * LOG2E)) + ca, a, cb);
                    v[r][i] = a;
                }
            op[ct] = u32x4{pk2(v[0][0], v[0][1]), pk2(v[0][2], v[0][3]), pk2(v[1][0], v[1][1]), pk2(v[1][2], v[1][3])};
        }
#pragma unroll
        for (int i = 0; i < 4; ++i) op[4][i] = m >= 12 ? 0x3f803f80u : op[4][i];
        const f32x4 Z = {0, 0, 0, 0};
        f32x4 j1[2], j3[2], j5[2], j7;
        j3[0] = mfma32(op[4], op[0], Z);
        j3[1] = mfma32(op[4], op[1], Z);
        j7 = mfma32(op[4], op[4], Z);
        j5[0] = mfma32(op[4], op[2], Z);
        j5[1] = mfma32(op[4], op[3], Z);
        j1[0] = mfma32(op[0], op[2], Z);
        j1[1] = mfma32(op[1], op[3], Z);
        f32x2 ex[2];
#pragma unroll
        for (int h = 0; h < 2; ++h)
#pragma unroll
            for (int i = 0; i < 2; ++i) ex[h][i] = (kg & 1) ? j3[h][2 * h + i] : (kg ? j7[2 * h + i] : j5[h][2 * h + i]);
        if (!quad_ok(fq, 7)) quad_wait(L + L_FLAG, 7);
        lptr K4 = L + L_KVX + w * 3072 + lane * 16, K2 = L + L_KVX + w * 3072 + 1024 + lane * 8;
#pragma unroll
        for (int h = 0; h < 2; ++h) {
            *(LAS f32x4*)(K4 + h * 1536) = j1[h];
            *(LAS f32x2*)(K2 + h * 1536) = ex[h];
        }
        flag_set(L, w, 20);
        __builtin_amdgcn_s_setprio(2);
    }
    const u32 lo = lane * 16;
    const int hq = hp ^ 1;
    if (__builtin_amdgcn_readfirstlane(f) < 10) flag_wait1(L, 10, 10);
    f32x4 q[2][3];
#pragma unroll
    for (int r = 0; r < 2; ++r)
#pragma unroll
        for (int t = 0; t < 3; ++t) q[r][t] = f32x4{ONE_EPS, ONE_EPS, ONE_EPS, ONE_EPS};
    u32x4 WF[45];
    const int grp = 9 * (cq >> 1) + 5 * (cq & 1) + w;
    const u32 wf_main = WS_WF + grp * 36864;
    const u32 wf_ext = WS_WF + (((9 * (cq >> 1) + 4) * 18 + 9 * (w >> 1)) * 2 + (cq & 1)) * 1024;
    auto ldwf = [&](int i) { WF[i] = __builtin_amdgcn_raw_buffer_load_b128(rs, lo, i < 9 ? wf_ext + i * 2048 : wf_main + (i - 9) * 1024, AUX_SC1); };
    {
        lptr QA = L + L_XRING + ra + (lane & 3) * 8 + q_lead(hq), QL = L + L_XRING + ra + 32 + q_lead(hq);
        u32x2 a[3][3][2];
        auto rdq = [&](int k, int t) {
            const int i = k >> 1, o = (i / 3) * 16384 + (i % 3) * 5120 + (k & 1) * 2560;
            lptr p = t == 2 ? QL + o : QA + o + t * 40;
            a[k % 3][t][0] = trread(p);
            a[k % 3][t][1] = trread(p + 80);
        };
#pragma unroll
        for (int t = 0; t < 6; ++t) rdq(t / 3, t % 3);
        SCHED();
#pragma unroll
        for (int k = 0; k < 18; ++k) {
            if (k == 4 || k == 10) {
                if (__builtin_amdgcn_readfirstlane(f) < (k == 4 ? 11u : 12u)) flag_wait1(L, 10, k == 4 ? 11 : 12);
                CFENCE();
            }
#pragma unroll
            for (int s = 0; s < 6; ++s) {
                const int t = s >> 1, r = s & 1;
                q[r][t] = mfma32(cat(a[k % 3][t][0], a[k % 3][t][1]), X[k][r], q[r][t]);
                if (s == 0 && (k == 3 || k == 9)) f = *FULL;
                if ((s & 1) && k < 16) rdq(k + 2, s >> 1);
                if (!(s & 1) && k >= 10 && 3 * (k - 10) + (s >> 1) < WF_A) ldwf(3 * (k - 10) + (s >> 1));
                SCHED();
            }
        }
    }
    flag_set(L, w, 21);
    __builtin_amdgcn_s_setprio(2);
    u32x4 pq1[2], pq2[2];
#pragma unroll
    for (int r = 0; r < 2; ++r) {
        u32 p[6];
#pragma unroll
        for (int t = 0; t < 3; ++t) {
            p[2 * t] = pk2(phi(q[r][t][0]), phi(q[r][t][1]));
            p[2 * t + 1] = pk2(phi(q[r][t][2]), phi(q[r][t][3]));
        }
        pq1[r] = u32x4{p[0], p[1], p[2], p[3]};
        pq2[r] = u32x4{p[4], p[5], 0, 0};
    }
    lptr A0, A2;
    {
        const int kr = m >> 2, i = m & 3;
        A0 = L + L_AIMG + (2 * kr + (i & 1) + 8 * (i >> 1)) * 144 + kg * 16;
        A2 = L + L_AIMG + (i == 0 ? 32 + kr : 36 + ((i - 1) & 1)) * 144 + kg * 16;
    }
    u32 vog = lo + WS_OG + ((rt * 4 + w) * 16 + hq) * 3072;
    PIN(pq1[0]); PIN(pq1[1]); PIN(pq2[0]); PIN(pq2[1]); PIN(A0); PIN(A2); PIN(vog);
    SCHED();
#pragma unroll
    for (int i = WF_A; i < WF_B; ++i) ldwf(i);
    SCHED();
    flag_wait1(L, 11, 1);
    f = __builtin_amdgcn_readfirstlane(*(volatile LAS u32*)(L + L_FLAG + 44));
    CFENCE();
    {
        u32x4 A[3][2];
        A[2][0] = *(LAS u32x4*)(A2); A[2][1] = *(LAS u32x4*)(A2 + 64);
        A[0][0] = *(LAS u32x4*)(A0); A[0][1] = *(LAS u32x4*)(A0 + 64);
        A[1][0] = *(LAS u32x4*)(A0 + 16 * 144); A[1][1] = *(LAS u32x4*)(A0 + 16 * 144 + 64);
        f32x4 o[2][3];
#pragma unroll
        for (int tt = 0; tt < 3; ++tt)
#pragma unroll
            for (int r = 0; r < 2; ++r) {
                const int t = (tt + 2) % 3;
                o[r][t] = mfma32(A[t][0], pq1[r], t == 2 ? f32x4{0, EPS, EPS, 0} : f32x4{0, 0, 0, 0});
                o[r][t] = mfma32(A[t][1], pq2[r], o[r][t]);
            }
        u32 d[2][4];
        float e[2];
#pragma unroll
        for (int r = 0; r < 2; ++r) {
            const float ra = __builtin_amdgcn_rcpf(o[r][2][1]), rb = __builtin_amdgcn_rcpf(o[r][2][2]), rc = kg ? rb : ra;
            d[r][0] = pk2s(o[r][0][0] * ra, o[r][0][1] * ra);
            d[r][1] = pk2s(o[r][0][2] * ra, o[r][0][3] * ra);
            d[r][2] = pk2s(o[r][1][0] * rc, o[r][1][1] * rc);
            d[r][3] = pk2s(o[r][1][2] * rb, o[r][1][3] * rb);
            e[r] = o[r][2][0] * rb;
        }
        st16(rs, seal(d[0][0], d[0][1], d[0][2], tag), vog, 0);
        st16(rs, seal(d[1][0], d[1][1], d[1][2], tag), vog, 1024);
        st16(rs, seal(d[0][3], pk2s(e[0], e[1]), d[1][3], tag), vog, 2048);
    }
    const int c8 = 32 * (9 * (cq >> 1) + 4) + 16 * (cq & 1);
    lptr IM = L + L_XRING;
#pragma unroll
    for (int i = WF_B; i < 45; ++i) ldwf(i);
    SCHED();
    {
        u32x4 og[12];
        const u32 so = WS_OG + (rb * 16 + 4 * w) * 3072;
#pragma nounroll
        while ((int)(now() - f) < 0) __builtin_amdgcn_s_sleep(1);
#pragma unroll
        for (int i = 0; i < 12; ++i) og[i] = ld16(rs, lo, so + i * 1024);
        SCHED();
        quad_wait(L + L_FLAG, 21);
        poll_rows<12>(og, rs, lo, so, tag);
        lptr B0 = IM + w * 4608 + m * 16;
#pragma unroll
        for (int h = 0; h < 4; ++h) {
            lptr E = B0 + (h >> 1) * 2304 + ((h & 1) ? 1024 + (kg >> 1) * 256 + ((8 + 4 * kg) & 15) : 4 * kg);
            lptr E2 = B0 + (h >> 1) * 2304 + ((h & 1) ? 2048 + 8 : 1024) + 2 * kg;
#pragma unroll
            for (int i = 0; i < 3; ++i) {
                *(LAS u32*)(E + i * 256) = og[3 * h][i];
                *(LAS u32*)(E + 18432 + i * 256) = og[3 * h + 1][i];
            }
            *(LAS u32*)(E + 768) = og[3 * h + 2][0];
            *(LAS u32*)(E + 18432 + 768) = og[3 * h + 2][2];
            *(LAS u16*)(E2) = (u16)og[3 * h + 2][1];
            *(LAS u16*)(E2 + 18432) = (u16)(og[3 * h + 2][1] >> 16);
        }
    }
    SCHED();
    u32x4 bt[4], b8[1];
    const u32 bs = WS_BT + (rb * 576 + grp * 32) * 128, bs8 = WS_BT + (rb * 576 + c8) * 128 + (w & 1) * 16;
    const u32 bv = (lane >> 2) * 128 + (lane & 3) * 32, gs = WS_G + rb * 256;
    u32x2 g0 = __builtin_amdgcn_raw_buffer_load_b64(rs, m * 8, gs, AUX_SC1), g1 = __builtin_amdgcn_raw_buffer_load_b64(rs, m * 8, gs + 128, AUX_SC1);
    SCHED();
    flag_set(L, w, 23);
    quad_wait(L + L_FLAG, 23);
    const f32x4 Z = {0, 0, 0, 0};
    f32x4 o[2][2] = {{Z, Z}, {Z, Z}}, a8 = Z, c[2][2];
    lptr EX = L + L_WRING + (w & 1) * 1024 + lo;
    lptr BI = L + L_WRING + 2048 + w * 2560;
    GAS char* orow = (GAS char*)P.out + (rb * 32 + m) * 1152;
    GAS char *o8 = orow + (w & 1) * 18432 + c8 * 2 + kg * 8, *om = orow + grp * 64 + kg * 16, *on = om + 18432;
    lptr BW = BI + (lane & 1) * 1024 + (lane >> 1) * 8, BR = BI + kg * 256 + m * 8, B8 = BI + 2048 + lane * 8;
    u32x2 tb[2][2], t8;
    u32 bad = 0, fx;
    f32x4 ex, c8v;
    float g8;
    auto stage = [&]() {
#pragma unroll
        for (int i = 0; i < 4; ++i) *(LAS u32x2*)(BW + i * 256) = u32x2{bt[i][0], bt[i][1]};
        *(LAS u32x2*)(B8) = u32x2{b8[0][0], b8[0][1]};
        CFENCE();
    };
    auto part = [&](float g, u32x2 b, int i) { const float v = (i & 1) ? hi2f(b[i >> 1]) : lo2f(b[i >> 1]); return fmaf(-g, v, v); };
    auto mix = [&](float g, f32x4 a, f32x4 b) { return u32x2{pk2s(fmaf(g, a[0], b[0]), fmaf(g, a[1], b[1])), pk2s(fmaf(g, a[2], b[2]), fmaf(g, a[3], b[3]))}; };
    {
        lptr F = IM + lo, F8 = IM + (w & 1) * 18432 + (w >> 1) * 9216 + lo;
        u32x4 f8[9], f[3][2];
#pragma unroll
        for (int k = 0; k < 9; ++k) f8[k] = *(LAS u32x4*)(F8 + k * 1024);
        auto rdf = [&](int k) { f[k % 3][0] = *(LAS u32x4*)(F + k * 1024); f[k % 3][1] = *(LAS u32x4*)(F + 18432 + k * 1024); };
        SCHED();
#pragma unroll
        for (int k = 0; k < 9; ++k) {
            a8 = mfma32(WF[k], f8[k], a8);
            if (k == 2 || k == 4) rdf(k / 2 - 1);
            if (k - 9 >= BT_STEP && k - 9 < BT_STEP + 4) bt[k - 9 - BT_STEP] = ld16(rs, lo, bs + (k - 9 - BT_STEP) * 1024);
            if (k - 9 == BT_STEP + 4) b8[0] = ld16(rs, bv, bs8);
            SCHED();
        }
#pragma unroll
        for (int k = 0; k < 18; ++k) {
            if (k + 2 < 18) rdf(k + 2);
#pragma unroll
            for (int j = 0; j < 4; ++j) {
                o[j >> 1][j & 1] = mfma32(WF[9 + 2 * k + (j & 1)], f[k % 3][j >> 1], o[j >> 1][j & 1]);
                if (k < 11) continue;
                SCHED();
                if (k == 11) { bad |= unseal(bt[j], tag); PIN(bad); }
                if (k == 12 && j == 0) { bad |= unseal(b8[0], tag) | xor3(g0[0], g0[1], tag) | xor3(g1[0], g1[1], tag); PIN(bad); }
                if (k == 12 && j == 1) stage();
                if (k == 12 && j == 2 && __builtin_expect(__any(bad != 0), 0)) {
                    poll_rows<4>(bt, rs, lo, bs, tag);
                    poll_rows<1>(b8, rs, bv, bs8, tag);
                    stage();
#pragma nounroll
                    for (u32 it = 0; it < SPIN_MAX && !__all((g0[0] ^ g0[1]) == tag && (g1[0] ^ g1[1]) == tag); ++it) {
                        __builtin_amdgcn_s_sleep(2);
                        g0 = __builtin_amdgcn_raw_buffer_load_b64(rs, m * 8, gs, AUX_SC1);
                        g1 = __builtin_amdgcn_raw_buffer_load_b64(rs, m * 8, gs + 128, AUX_SC1);
                        CFENCE();
                    }
                }
                if (k == 12 && j == 3) {
#pragma unroll
                    for (int i = 0; i < 4; ++i) tb[i >> 1][i & 1] = trread(BR + (i >> 1) * 1024 + (i & 1) * 128);
                    t8 = trread(B8);
                }
                if (k == 13 && j == 0) {
                    fx = *(volatile LAS u32*)(L + L_FLAG + (w | 2) * 4);
                    ex = *(volatile LAS f32x4*)(EX);
                    g8 = __uint_as_float((w & 1) ? g1[0] : g0[0]);
                    PIN(g8);
                }
                if (k == 14 && j == 3 && __builtin_expect(__builtin_amdgcn_readfirstlane(fx) < 24, 0)) {
                    flag_wait1(L, w | 2, 24);
                    ex = *(volatile LAS f32x4*)(EX);
                }
                if (k >= 14) {
                    const int r = (k - 14) >> 1, t = k & 1;
                    c[r][t][j] = part(__uint_as_float(r ? g1[0] : g0[0]), tb[r][t], j);
                    PIN(c[r][t][j]);
                }
                if (k == 14) { c8v[j] = part(g8, t8, j); PIN(c8v[j]); }
                if (k == 15) a8[j] = add1(a8[j], ex[j]);
                if (k == 16) { c8v[j] = fmaf(g8, a8[j], c8v[j]); PIN(c8v[j]); }
                if (k == 17 && j == 0 && w < 2) *(GAS u32x2*)(o8) = u32x2{pk2s(c8v[0], c8v[1]), pk2s(c8v[2], c8v[3])};
                SCHED();
            }
            if (k == 1 && w >= 2) { *(LAS f32x4*)(EX) = a8; }
            if (k == 3 && w >= 2) flag_set(L, w, 24);
            if (k == 5) { PIN(o8); PIN(om); PIN(on); PIN(BW); PIN(BR); PIN(B8); }
            SCHED();
        }
    }
#pragma unroll
    for (int r = 0; r < 2; ++r) {
        const float g = __uint_as_float(r ? g1[0] : g0[0]);
        *(GAS u32x4*)(r ? on : om) = cat(mix(g, o[r][0], c[r][0]), mix(g, o[r][1], c[r][1]));
        SCHED();
    }
}

struct cf { float r, i; };
DEV cf cmul(cf a, cf b) { return cf{fmaf(a.r, b.r, -(a.i * b.i)), fmaf(a.r, b.i, a.i * b.r)}; }
DEV cf csq(cf a) { return cf{fmaf(a.r, a.r, -(a.i * a.i)), (a.r + a.r) * a.i}; }
DEV cf cexp(float lr, float t) {
    const float e = __builtin_amdgcn_exp2f(lr), f = __builtin_amdgcn_fractf(t);
    return cf{e * __builtin_amdgcn_cosf(f), e * __builtin_amdgcn_sinf(f)};
}

DEV void band_ops(u32x4 (&band)[2], int lane) {
    const int c = (lane & 15) + 8 * (lane >> 4);
#pragma unroll
    for (int i = 0; i < 4; ++i) {
        const u32 lo = c + 2 * i, hi = lo + 1;
        band[0][i] = (lo - 16 < 7 ? 0x3f80u : 0u) | (hi - 16 < 7 ? 0x3f800000u : 0u);
        band[1][i] = (lo - 23 < 9 ? 0x3f80u : 0u) | (hi - 23 < 9 ? 0x3f800000u : 0u);
    }
    PIN(band[0]); PIN(band[1]);
}
DEV u32 kgen_role(Params& P, lptr L, int cl, int sg, int lane, u32x4 (&band)[2]) {
    const int ch = 3 * sg + cl;
    const int m = lane & 15, kg = lane >> 4;
    lptr C = L + L_CH + cl * CH_BYTES;
    const int n = ch * 64 + lane;
    P.arrive<2>();
    u32 are = ((const GAS u32*)P.Are)[n], aim = ((const GAS u32*)P.Aim)[n], bre = ((const GAS u32*)P.Bre)[n];
    u32 bim = ((const GAS u32*)P.Bim)[n], cre = ((const GAS u32*)P.Cre)[n], cim = ((const GAS u32*)P.Cim)[n];
    const u16 ldt = ((const GAS u16*)P.logdt)[ch], dsk = ((const GAS u16*)P.Dskip)[ch];
    SCHED();
    u32 ga[20];
#pragma unroll
    for (int j = 0; j < 10; ++j) {
        const int la = lane * 16 - 4 + 2 * j, lb = la + 1;
        const u32 bits = pk2((float)la, (float)lb);
        const int ia = (int)(bits & 0xffff) - 0x4280, ib = (int)(bits >> 16) - 0x4280;
        ga[2 * j] = L_KT + cl * KT_BYTES + 2 * (la < 256 ? (la < 0 ? 513 : la) : ia);
        ga[2 * j + 1] = L_KT + cl * KT_BYTES + 2 * (lb < 256 ? (lb < 0 ? 513 : lb) : ib);
    }
#pragma unroll
    for (int j = 0; j < 20; ++j) PIN(ga[j]);
    u32 ea = lane < 8 ? (lane >> 1) * CH_KSTRIDE + (lane & 1) * 16 : ((lane >> 2) - 2) * CH_KSTRIDE + 2080 + (lane & 3) * 16;
    const bool e0 = lane >= 8 && !(lane & 3);
    u32 em0 = e0 ? ~0u : 0u, em2 = e0 ? (lane == 8 ? 0xffffu : ~0u) : 0u, em3 = lane == 16 ? 0xffffu : (lane == 20 ? ~0u : 0u);
    PIN(ea); PIN(em0); PIN(em2); PIN(em3);
    band_ops(band, lane);
    SCHED();
    const float dt = lo2f(pk2(expf(bf2f(ldt)), 0.f));
    __builtin_amdgcn_s_setprio(0);
    f32x4 a0 = {lane == 0 ? bf2f(dsk) : 0.f, 0, 0, 0}, a1 = {0, 0, 0, 0}, a2 = a1;
    LAS u32* T = (LAS u32*)(C + CH_KC + (lane >> 2) * TQ + (lane & 3) * 4);
    lptr R = C + CH_KC + kg * TQ + m * 16;
#pragma nounroll
    for (int u = 0; u < 2; ++u) {
        auto hf = [&](u32& v) { const float r = hi2f(v); v <<= 16; return r; };
        const float lr = dt * hf(are) * LOG2E, t = dt * hf(aim) * 0.15915494309189533577f;
        const cf c = cmul(cf{hf(cre), hf(cim)}, cf{dt * hf(bre), dt * hf(bim)});
        cf p[16], A[8];
        p[1] = cexp(lr, t);
        const cf w16 = cexp(16.f * lr, 16.f * t);
        A[1] = cexp(256.f * lr, 256.f * t);
        auto put = [&](int row, cf v) { T[row * 4] = pk2s(v.r, v.i); };
        T[0] = 0x3f80u;
        put(1, p[1]);
#pragma unroll
        for (int k = 2; k < 16; ++k) {
            p[k] = (k & 1) ? cmul(p[k - 1], p[1]) : csq(p[k / 2]);
            put(k, p[k]);
        }
        {
            const cf w32 = csq(w16);
            cf h0 = cf{c.r, -c.i}, h1 = cmul(c, w16);
            h1.i = -h1.i;
            const cf wc = cf{w32.r, -w32.i};
#pragma unroll
            for (int k = 0; k < 8; ++k) {
                put(41 + 2 * k, h0);
                put(42 + 2 * k, h1);
                if (k < 7) { h0 = cmul(h0, wc); h1 = cmul(h1, wc); }
            }
        }
        A[2] = csq(A[1]); A[3] = cmul(A[2], A[1]); A[4] = csq(A[2]);
        put(40, A[4]);
        A[4] = cmul(A[4], p[8]); A[5] = cmul(A[4], A[1]); A[6] = cmul(A[4], A[2]); A[7] = cmul(A[4], A[3]);
#pragma unroll
        for (int k = 0; k < 8; ++k) put(16 + k, k ? cmul(A[1], p[2 * k]) : A[1]);
#pragma unroll
        for (int k = 0; k < 8; ++k) put(24 + k, (k & 3) ? cmul(A[2 + (k >> 2)], p[4 * (k & 3)]) : A[2 + (k >> 2)]);
#pragma unroll
        for (int k = 0; k < 8; ++k) put(32 + k, (k & 1) ? cmul(A[4 + (k >> 1)], p[8]) : A[4 + (k >> 1)]);
        CFENCE();
#pragma unroll
        for (int s = 0; s < 4; ++s) {
            const u32x4 H = *(LAS u32x4*)(R + s * 4 * TQ + 41 * 16);
            a0 = mfma32(*(LAS u32x4*)(R + s * 4 * TQ), H, a0);
            a1 = mfma32(*(LAS u32x4*)(R + s * 4 * TQ + 256), H, a1);
            a2 = mfma32(*(LAS u32x4*)(R + s * 4 * TQ + 512), H, a2);
        }
        CFENCE();
    }
    __builtin_amdgcn_s_setprio(0);
    const u32 tag = mk_tag(get_epoch(L));
    u32x4 g[6];
    const u32 xs = WS_XT + sg * 16384 + cl * 5120;
#pragma unroll
    for (int i = 0; i < 6; ++i) g[i] = ld16(P.rs, lane * 16, xs + i * 1024);
    SCHED();
    {
        lptr KT = L + L_KT + cl * KT_BYTES;
        *(LAS u32x2*)(KT + 2 * (16 * m + 4 * kg)) = u32x2{pk2(a0[0], a0[1]), pk2(a0[2], a0[3])};
        *(LAS u32x2*)(KT + 2 * (kg < 2 ? 256 + 8 * m + 4 * kg : 256 + 64 * kg + 4 * m)) = u32x2{pk2(a1[0], a1[1]), pk2(a1[2], a1[3])};
        const u32 f0 = pk2(a2[0], kg < 2 ? a2[1] : 0.f);
        if (kg < 3) *(LAS u32*)(KT + KT_FAR + (kg < 2 ? 60 + 128 * kg : -4) + 4 * m) = (kg < 2 || !m) ? f0 : 0u;
        if (kg < 2) *(LAS u32*)(KT + KT_FAR + 124 + 128 * kg + 4 * m) = pk2(a2[2], a2[3]);
        CFENCE();
    }
    {
        u32 v[20], d[10], e[9];
#pragma unroll
        for (int j = 0; j < 20; ++j) v[j] = *(LAS u16*)(L + ga[j]);
#pragma unroll
        for (int j = 1; j < 10; ++j) d[j] = v[2 * j] | (v[2 * j + 1] << 16);
#pragma unroll
        for (int j = 0; j < 9; ++j) e[j] = v[2 * j + 1] | (v[2 * j + 2] << 16);
        lptr W = C + CH_KC + 32 + lane * 32;
        *(LAS u32x4*)(W) = u32x4{d[2], d[3], d[4], d[5]};
        *(LAS u32x4*)(W + 16) = u32x4{d[6], d[7], d[8], d[9]};
        *(LAS u32x4*)(W + CH_KSTRIDE) = u32x4{e[1], e[2], e[3], e[4]};
        *(LAS u32x4*)(W + CH_KSTRIDE + 16) = u32x4{e[5], e[6], e[7], e[8]};
        *(LAS u32x4*)(W + 2 * CH_KSTRIDE) = u32x4{d[1], d[2], d[3], d[4]};
        *(LAS u32x4*)(W + 2 * CH_KSTRIDE + 16) = u32x4{d[5], d[6], d[7], d[8]};
        *(LAS u32x4*)(W + 3 * CH_KSTRIDE) = u32x4{e[0], e[1], e[2], e[3]};
        *(LAS u32x4*)(W + 3 * CH_KSTRIDE + 16) = u32x4{e[4], e[5], e[6], e[7]};
        const u32 aa = __builtin_amdgcn_readlane(d[9], 63), ba = __builtin_amdgcn_readlane(e[8], 63);
        if (lane < 24) *(LAS u32x4*)(C + CH_KC + ea) = u32x4{lane == 20 ? ba : aa & em0, aa & em0, aa & em2, aa & em3};
    }
    *(LAS u32x2*)(C + CH_XR + 4096 + lane * 8) = u32x2{0, 0};
    SCHED();
    poll_rows<6>(g, P.rs, lane * 16, xs, tag);
    lptr XR = L + L_CH + CH_XR + cl * 1280 + lane * 4;
#pragma unroll
    for (int i = 0; i < 6; ++i)
#pragma unroll
        for (int c = 0; c < 3; ++c) *(LAS u32*)(XR + c * CH_BYTES + i * 256) = g[i][c];
    return tag;
}

DEV void conv_main(Params& P, rsrc_t rs, lptr C, lptr F, const u32x4 (&band)[2], int ch, int lane, u32 tag) {
    const int m = lane & 15, kg = lane >> 4;
    const f32x4 Z = {0, 0, 0, 0};
    f32x4 acc[8];
    __builtin_amdgcn_s_setprio(0);
#pragma unroll
    for (int i = 0; i < 8; ++i) acc[i] = Z;
    lptr KA = C + CH_KC + ((~m) & 3) * CH_KSTRIDE + 2 * (8 * kg + ((m + 4) & ~3));
    lptr XB = C + CH_XR + 2 * (240 - 16 * m + 8 * kg);
    lptr XW = C + CH_XR + 2 * (1796 + 16 * m + 8 * kg);
    const LAS u32* FB = (const LAS u32*)(F + 4 * (m + 4 * kg));
    constexpr int PD = 4;
    u32x4 A[8][4], B[PD], A4, xw[4], fb[4];
    auto rd8 = [&](lptr p) { return cat(*(volatile LAS u32x2*)(p), *(volatile LAS u32x2*)(p + 8)); };
    auto rdb = [&](int n) { B[n % PD] = *(LAS u32x4*)(XB + 64 * (n & 7) + 512 * (7 - (n >> 3))); };
    auto rda = [&](int j) {
#pragma unroll
        for (int a = 0; a < 4; ++a) A[j][a] = rd8(KA + 64 * j + 512 * a);
    };
#pragma unroll
    for (int s = 0; s < 4; ++s) xw[s] = rd8(XW - 512 * s);
#pragma unroll
    for (int dl = 0; dl < 4; ++dl) fb[dl] = u32x4{FB[16 * dl], FB[16 * dl + 1], FB[16 * dl + 2], FB[16 * dl + 3]};
    A4 = rd8(KA + 2048);
    rdb(0); rda(0); rdb(1); rda(1); rdb(2);
    SCHED();
    {
        f32x4 s7[4], s9[4];
        u32x4 sw[4];
#pragma unroll
        for (int s = 0; s < 4; ++s) { s7[s] = mfma32(xw[s], band[0], Z); s9[s] = mfma32(xw[s], band[1], Z); }
        rda(2);
        SCHED();
#pragma unroll
        for (int s = 0; s < 4; ++s) sw[s] = u32x4{pk2(s7[s][0], s9[s][0]), pk2(s7[s][1], s9[s][1]), pk2(s7[s][2], s9[s][2]), pk2(s7[s][3], s9[s][3])};
#pragma unroll
        for (int s = 0; s < 4; ++s)
#pragma unroll
            for (int I = 4 + s; I < 8; ++I) acc[I] = mfma32(sw[s], fb[I - s - 4], acc[I]);
        SCHED();
    }
    const u32 vo = (m >> 1) * 73728 + kg * 32 + (m & 1) * 16 + WS_BT + ch * 128;
    auto put = [&](int I) {
        {
            u32x4 v = seal(pk2(acc[I][0], acc[I][1]), pk2(acc[I][2], acc[I][3]), 0u, tag);
            u32 a = vo + I * 8 * 73728;
            PIN(v); PIN(a);
            SCHED();
            if (I < CONV_EARLY) __builtin_amdgcn_s_setprio(3);
            st16(rs, v, a, 0);
            if (I < CONV_EARLY) __builtin_amdgcn_s_setprio(0);
            SCHED();
        }
    };
#pragma unroll
    for (int n = 0; n < 64; ++n) {
        const int b = n >> 3, j = n & 7;
        if (n + PD - 1 < 64) rdb(n + PD - 1);
        if (n + 3 < 8) rda(n + 3);
#pragma unroll
        for (int a = 0; a < 4; ++a)
            if (a + b < 8) acc[a + b] = mfma32(A[j][a], B[n % PD], acc[a + b]);
        if (j == 0 && b < 4) acc[b + 4] = mfma32(A4, B[n % PD], acc[b + 4]);
        if (j == 1 && b && b <= CONV_EARLY) put(b - 1);
        SCHED();
    }
    __builtin_amdgcn_s_setprio(0);
#pragma unroll
    for (int I = CONV_EARLY < 7 ? CONV_EARLY : 7; I < 8; ++I) put(I);
}

DEV void gate_role(const Params& P, rsrc_t rs, int lane, int rb, int ps, u32 tag) {
    const GAS char* gw = (const GAS char*)P.gw + (lane & 3) * 16;
    {
        const int row = rb * 32 + ps * 16 + (lane >> 2);
        const GAS char* xr = (const GAS char*)P.x + row * 1152 + (lane & 3) * 16;
        u32x4 xv[18], gv[18];
#pragma unroll
        for (int i = 0; i < 18; ++i) { xv[i] = *(const GAS u32x4*)(xr + i * 64); gv[i] = *(const GAS u32x4*)(gw + i * 64); }
        float s0 = 0.f, s1 = 0.f;
#pragma unroll
        for (int i = 0; i < 18; ++i)
#pragma unroll
            for (int j = 0; j < 4; ++j) {
                s0 = fmaf(lo2f(xv[i][j]), lo2f(gv[i][j]), s0);
                s1 = fmaf(hi2f(xv[i][j]), hi2f(gv[i][j]), s1);
            }
        float s = s0 + s1;
        s += __shfl_xor(s, 1);
        s += __shfl_xor(s, 2);
        const u32 g = __float_as_uint(1.0f / (1.0f + __expf(-s)));
        if ((lane & 3) == 0) __builtin_amdgcn_raw_buffer_store_b64(u32x2{g, g ^ tag}, rs, WS_G + row * 8, 0, AUX_SC1);
    }
}

DEV u32 wf_role(Params& P, lptr L, u32 lbase, int cl, int lane, int unit) {
    const int ks = unit / 9, gp = unit - ks * 9;
    P.arrive<3>();
    const rsrc_t rs = P.rs;
    if (unit < 162) {
        const char* src = (const char*)P.wo + ks * 32 * 1152 + gp * 128;
#pragma unroll
        for (int i = 0; i < 4; ++i) glds16((u32)((lane >> 3) * 1152 + (lane & 7) * 16), src + i * 8 * 1152, lbase + L_CH + cl * CH_BYTES + i * 1040);
    }
    if (unit >= 162) return mk_tag(get_epoch(L));
    WAIT_V(0);
    lptr S = L + L_CH + cl * CH_BYTES + (lane >> 4) * 1040 + ((lane >> 2) & 3) * 128;
#pragma unroll
    for (int h = 0; h < 2; ++h) {
        const int grp = 2 * gp + h;
        const bool sp = grp == 4 || grp == 13;
        lptr p = S + h * 64 + (lane & 3) * (sp ? 8 : 16);
#pragma unroll
        for (int t = 0; t < 2; ++t) {
            lptr q = p + t * (sp ? 32 : 8);
            st16(rs, cat(trread(q), trread(q + 512)), lane * 16, WS_WF + ((grp * 18 + ks) * 2 + t) * 1024);
        }
    }
    SCHED();
    const u32 tag = mk_tag(get_epoch(L));
    WAIT_V(0);
    if (lane == 0) __builtin_amdgcn_raw_buffer_store_b32(tag, rs, WS_WFF + unit * 4, 0, AUX_SC1);
    return tag;
}

__global__ __launch_bounds__(512, 2) void fused(Args A) {
    Params P;
    extern __shared__ __attribute__((aligned(64))) char smem[];
    lptr L = (lptr)smem;
    const u32 lbase = (u32)(uintptr_t)L;
    const int tid = threadIdx.x, lane = tid & 63, w = __builtin_amdgcn_readfirstlane(tid >> 6);
    if (w == 5) {
        *(volatile LAS u32*)(L + L_FLAG + (lane & 15) * 4) = (lane & 15) == 7;
        WAIT_L0();
    }
    P.fetch();
    asm volatile("s_barrier" ::: "memory");
    const int bid = blockIdx.x, xcd = bid & 7, slot = bid >> 3;
    const int rt = (xcd >> 1) * 4 + (slot & 3), hp = (xcd & 1) * 8 + ((((xcd & 1) ? 0x60754321u : 0x71654320u) >> (slot & 28)) & 7);
    const int rb = 8 * xcd + (slot & 7), cq = slot >> 3, cg = w & 3;
    u32x4 band[2];
    if (w < 4) {
        gemm_role(P, L, lbase, w, lane, rt, hp, rb, cq);
        if (bid == 0 && tid == 0) {
            const u32 ep = *(volatile LAS u32*)(L + L_FLAG + 32);
            __builtin_amdgcn_raw_buffer_store_b32(ep + 1, P.rs, WS_EPOCH, 0, AUX_SC1);
        }
    } else if (w == 7) {
        loader_role(P, L, lbase, lane, rt, hp);
    } else if (slot < 24) {
        const int sg = xcd * 24 + slot;
        const u32 tag = kgen_role(P, L, cg, sg, lane, band);
        flag_set(L, w, 1);
        quad_wait(L + L_FLAG + 16, 1);
        conv_main(P, P.rs, L + L_CH + cg * CH_BYTES, L + L_KT + cg * KT_BYTES + KT_FAR, band, 3 * sg + cg, lane, tag);
    } else {
        const u32 tag = wf_role(P, L, lbase, cg, lane, ((slot - 24) * 8 + xcd) * 3 + cg);
        if (cg < 2) {
            flag_wait1(L, 10, 12);
            gate_role(P, P.rs, lane, rb, cg, tag);
        }
    }
}

extern "C" void kernel_launch(void* const* d_in, const int* in_sizes, int n_in, void* d_out, int out_size, void* d_ws,
                              size_t ws_size, hipStream_t stream) {
    Args P;
    P.x = (const u16*)d_in[0];
    P.wqkv = (const u16*)d_in[1];
    P.wo = (const u16*)d_in[2];
    P.Are = (const u16*)d_in[4];
    P.Aim = (const u16*)d_in[5];
    P.Bre = (const u16*)d_in[6];
    P.Bim = (const u16*)d_in[7];
    P.Cre = (const u16*)d_in[8];
    P.Cim = (const u16*)d_in[9];
    P.logdt = (const u16*)d_in[10];
    P.Dskip = (const u16*)d_in[11];
    P.gw = (const u16*)d_in[12];
    P.ws = (char*)d_ws;
    P.out = (u16*)d_out;
    (void)hipFuncSetAttribute((const void*)fused, hipFuncAttributeMaxDynamicSharedMemorySize, LDS_BYTES);
    fused<<<256, 512, LDS_BYTES, stream>>>(P);
}
```
